```python
import torch

def _softmax(x, axis=-1):
    return torch.softmax(x, dim=axis)
def _gelu(x):
    return torch.nn.functional.gelu(x, approximate="tanh")
def _silu(x):
    return torch.nn.functional.silu(x)
def _sigmoid(x):
    return torch.sigmoid(x)
def _layernorm(x, axis=-1, eps=1e-5):
    m = x.mean(dim=axis, keepdim=True)
    v = x.var(dim=axis, keepdim=True, unbiased=False)
    return (x - m) / torch.sqrt(v + eps)
def _scatter_add(tgt, idx, src, axis=0):
    return tgt.clone().index_add_(axis, idx, src)
def _segment_sum(src, seg, n):
    out = torch.zeros((n,) + src.shape[1:], dtype=src.dtype)
    return out.index_add_(0, seg, src)
def _pad(x, pw):
    flat = tuple(v for pair in reversed(pw) for v in pair)
    return torch.nn.functional.pad(x, flat)

kernel_name = 'sequential_preLN__spectral_gfnet__mixer_s_mlp__prompt'

def setup_inputs():

    _w = (lambda a: (lambda t: t.to(torch.bfloat16) if t.is_floating_point() else t)(torch.as_tensor(a)))
    def _A_setup():
        B, S, D = 1, 2048, 640
        Df = D // 2 + 1
        return {
            "x": _w((lambda *s: torch.randn(*s, dtype=torch.bfloat16) * 0.05)(B, S, D)),
            "w_r": _w((lambda *s: torch.randn(*s, dtype=torch.bfloat16) * 0.4)(S, Df) * 0.02),
            "w_i": _w((lambda *s: torch.randn(*s, dtype=torch.bfloat16) * 0.4)(S, Df) * 0.02),
        }
    _A_in = _A_setup()

    def _B_setup():
        B, S, D, F = 1, 2048, 640, 1728
        return {
            "x":  _w((lambda *s: torch.randn(*s, dtype=torch.bfloat16) * 0.05)(B, S, D)),
            "w1": _w((lambda *s: torch.randn(*s, dtype=torch.bfloat16) * 0.8)(D, D) * (D ** -0.5)),
            "w2": _w((lambda *s: torch.randn(*s, dtype=torch.bfloat16) * 0.8)(D, D) * (D ** -0.5)),
            "w3": _w((lambda *s: torch.randn(*s, dtype=torch.bfloat16) * 0.8)(D, F) * (D ** -0.5)),
            "w4": _w((lambda *s: torch.randn(*s, dtype=torch.bfloat16) * 0.8)(F, D) * (F ** -0.5)),
        }
    _B_in = _B_setup()

    return {
        "A_x": _A_in["x"],
        "A_w_r": _A_in["w_r"],
        "A_w_i": _A_in["w_i"],
        "B_x": _B_in["x"],
        "B_w1": _B_in["w1"],
        "B_w2": _B_in["w2"],
        "B_w3": _B_in["w3"],
        "B_w4": _B_in["w4"],
    }

def reference(A_x, A_w_r, A_w_i, B_x, B_w1, B_w2, B_w3, B_w4):
    def _A_ref(x, w_r, w_i):
        B, S, D = x.shape
        X = (lambda x, axes=(-2,-1): torch.fft.rfft2(x.float(), dim=axes))(x, axes=(-2, -1))
        W = w_r + 1j * w_i
        X = X * W
        y = (lambda x, s, axes=(-2,-1): torch.fft.irfft2(x, s=s, dim=axes).to(torch.bfloat16))(X, (S, D), axes=(-2, -1))
        return y

    def _B_ref(x, w1, w2, w3, w4):
        B, S, D = x.shape
        h = _gelu(_layernorm(x, axis=-1) @ w1)
        c = D // 4
        g0 = _pad(h[:, :, 0*c:1*c], ((0, 0), (1, 0), (0, 0)))[:, :S, :]
        g1 = _pad(h[:, :, 1*c:2*c], ((0, 0), (0, 1), (0, 0)))[:, 1:, :]
        g2 = _pad(h[:, :, 2*c:3*c], ((0, 0), (2, 0), (0, 0)))[:, :S, :]
        g3 = _pad(h[:, :, 3*c:   ], ((0, 0), (0, 2), (0, 0)))[:, 2:, :]
        hs = torch.concatenate([g0, g1, g2, g3], axis=-1) @ w2
        x1 = x + hs
        h2 = _gelu(_layernorm(x1, axis=-1) @ w3) @ w4
        return x1 + h2

    x = A_x
    h = x + _A_ref(_layernorm(x, axis=-1), A_w_r, A_w_i)
    h2 = h + _B_ref(_layernorm(h, axis=-1), B_w1, B_w2, B_w3, B_w4)
    return h2
```

```cpp
#include <hip/hip_runtime.h>
#include <stdint.h>

#define NLAUNCH 1

typedef unsigned u32;
typedef unsigned long long u64;
typedef unsigned short u16;
typedef u32 v4u __attribute__((ext_vector_type(4)));
typedef u32 v2u __attribute__((ext_vector_type(2)));
typedef float f32x4 __attribute__((ext_vector_type(4)));
typedef float f32x2 __attribute__((ext_vector_type(2)));
typedef __bf16 bf16x8 __attribute__((ext_vector_type(8)));
typedef __bf16 bf16x2 __attribute__((ext_vector_type(2)));
typedef short s16x4 __attribute__((ext_vector_type(4)));
typedef __amdgpu_buffer_rsrc_t rsrc_t;
#define LAS __attribute__((address_space(3)))
#define GAS __attribute__((address_space(1)))
#define DI __device__ __forceinline__

constexpr int ACT = 2048 * 640 * 2;
constexpr int O_EPOCH = 0;
constexpr int O_FA = 4096;
constexpr int O_FW = 8192;
constexpr int O_FS = 12288;
constexpr int O_FB = 16384;
constexpr int O_FC = 20480;
constexpr int O_F5 = 24576;
constexpr int O_F6 = 28672;
constexpr int O_SIG = 65536;
constexpr int O_MU = O_SIG + 5 * 8192;
constexpr int O_CS3 = O_MU + 8192;
constexpr int O_ST1 = O_CS3 + 20 * 1792 * 4;
constexpr int O_ZT = O_ST1 + 2048 * 128;
constexpr int O_WT = O_ZT + ACT;
constexpr int O_HB = O_WT + ACT;
constexpr int O_BC = O_HB + ACT;
constexpr int O_B5 = O_BC + 16 * 102400 + 8192;
constexpr int O_B6 = O_B5 + 8 * 102400 + 8192;
constexpr int O_B7 = O_B6 + 16 * 143360 + 8192;
constexpr int O_G = O_B7 + 8 * 276480 + 8192;
constexpr int O_XB = O_G + ACT;
constexpr int O_HH = O_XB + ACT;
constexpr int O_X1 = O_HH + ACT;
constexpr int O_U = O_X1 + ACT;
constexpr int WS_END = O_U + 2048 * 1728 * 2;

constexpr int LDS_BYTES = 160 * 1024;
constexpr int L_MISC = 156 * 1024;
constexpr float EPS = 1e-5f;
constexpr int SC1 = 16;

DI u32 pk(float a, float b) { f32x2 v = {a, b}; return __builtin_bit_cast(u32, __builtin_convertvector(v, bf16x2)); }
DI float blo(u32 v) { return __builtin_bit_cast(float, v << 16); }
DI float bhi(u32 v) { return __builtin_bit_cast(float, v & 0xffff0000u); }
DI f32x4 mfma(v4u a, v4u b, f32x4 c) {
  return __builtin_amdgcn_mfma_f32_16x16x32_bf16(__builtin_bit_cast(bf16x8, a), __builtin_bit_cast(bf16x8, b), c, 0, 0, 0);
}
DI float dot2(u32 a, u32 b, float c) {
  return __builtin_amdgcn_fdot2_f32_bf16(__builtin_bit_cast(bf16x2, a), __builtin_bit_cast(bf16x2, b), c, false);
}
DI float dot8(v4u a, v4u b, float c) { return dot2(a.w, b.w, dot2(a.z, b.z, dot2(a.y, b.y, dot2(a.x, b.x, c)))); }
DI v2u trread(LAS char* p) { return __builtin_bit_cast(v2u, __builtin_amdgcn_ds_read_tr16_b64_v4i16((LAS s16x4*)p)); }
DI float cosr(float rev) { return __builtin_amdgcn_cosf(rev); }
DI float sinr(float rev) { return __builtin_amdgcn_sinf(rev); }
DI float gelu(float x) {
  const float k0 = -2.302208198f, k1 = -0.1029432397f;
  float e = __builtin_amdgcn_exp2f(x * (k0 + k1 * x * x));
  return x * __builtin_amdgcn_rcpf(1.f + e);
}
DI void dma16(const void* gsrc, u32 lds_dst) {
  u32 keep;
  asm volatile("s_mov_b32 %0, m0\n\ts_mov_b32 m0, %2\n\ts_nop 0\n\tglobal_load_lds_dwordx4 %1, off\n\ts_mov_b32 m0, %0"
               : "=&s"(keep) : "v"(gsrc), "s"(lds_dst) : "memory");
}
#define VMWAIT(n) asm volatile("s_waitcnt vmcnt(" #n ")" ::: "memory")
#define BAR() asm volatile("s_waitcnt lgkmcnt(0)\n\ts_barrier" ::: "memory")
#define SCHED() __builtin_amdgcn_sched_barrier(0)
DI v4u ld16(rsrc_t r, int voff, int soff) { return __builtin_amdgcn_raw_buffer_load_b128(r, voff, soff, SC1); }
DI v2u ld8(rsrc_t r, int voff, int soff) { return __builtin_amdgcn_raw_buffer_load_b64(r, voff, soff, SC1); }
DI u32 ld4(rsrc_t r, int voff, int soff) { return __builtin_amdgcn_raw_buffer_load_b32(r, voff, soff, SC1); }
DI void st16(rsrc_t r, v4u v, int voff, int soff) { __builtin_amdgcn_raw_buffer_store_b128(v, r, voff, soff, SC1); }
DI void st8(rsrc_t r, v2u v, int voff, int soff) { __builtin_amdgcn_raw_buffer_store_b64(v, r, voff, soff, SC1); }
DI void st4(rsrc_t r, u32 v, int voff, int soff) { __builtin_amdgcn_raw_buffer_store_b32(v, r, voff, soff, SC1); }
DI float xsum16_32(float v) {
  v += __shfl_xor(v, 16);
  v += __shfl_xor(v, 32);
  return v;
}

struct Ctx {
  char* ws; rsrc_t rs; LAS char* lds; u64 tag;
  int tid, lane, w, g, c;
};
DI void publish(const Ctx& x, int flag_off) {
  VMWAIT(0);
  BAR();
  if (x.tid == 0) __hip_atomic_store((GAS u64*)(x.ws + flag_off), x.tag, __ATOMIC_RELAXED, __HIP_MEMORY_SCOPE_AGENT);
}
DI void poll(const Ctx& x, int flag_off, bool active) {
  for (int spin = 0; spin < (1 << 16); ++spin) {
    u64 v = x.tag;
    if (active) v = __hip_atomic_load((GAS u64*)(x.ws + flag_off), __ATOMIC_RELAXED, __HIP_MEMORY_SCOPE_AGENT);
    if (__all(v == x.tag)) break;
    __builtin_amdgcn_s_sleep(1);
  }
  __builtin_amdgcn_fence(__ATOMIC_ACQUIRE, "wavefront");
}

template <int GP> DI void group_dst(int gam, int h, int& tile, int& nb, bool& single) {
  int sup = gam / (2 * GP + 1), e = gam - sup * (2 * GP + 1);
  single = (e == GP);
  tile = 2 * sup + (e > GP ? 1 : (single ? h : 0));
  nb = single ? 2 * GP : 2 * (e > GP ? e - GP - 1 : e) + h;
}

DI void gen_q(int slot, int g, v4u (&q)[20]) {
  int k = slot >> 1;
  float amp = 0.05590169944f;
  if (slot == 0) { k = 320; amp = 0.03952847075f; }
  if (slot == 1) amp = 0.f;
  const float kf = (float)k * (1.f / 640.f), ph = (slot & 1) ? 0.25f : 0.f;
  float v0[8], v1[8];
#pragma unroll
  for (int j = 0; j < 8; ++j) {
    v0[j] = amp * cosr((float)((8 * g + j) * k) * (1.f / 640.f) + ph);
    v1[j] = amp * cosr((float)((32 + 8 * g + j) * k) * (1.f / 640.f) + ph);
  }
  const float c2 = 2.f * cosr(32.f * kf);
#pragma unroll
  for (int ks = 0; ks < 20; ks += 2) {
    q[ks] = v4u{pk(v0[0], v0[1]), pk(v0[2], v0[3]), pk(v0[4], v0[5]), pk(v0[6], v0[7])};
    q[ks + 1] = v4u{pk(v1[0], v1[1]), pk(v1[2], v1[3]), pk(v1[4], v1[5]), pk(v1[6], v1[7])};
    if (ks + 2 < 20) {
#pragma unroll
      for (int j = 0; j < 8; ++j) { v0[j] = c2 * v1[j] - v0[j]; v1[j] = c2 * v0[j] - v1[j]; }
    }
  }
}

DI void phase_a(const Ctx& x, const u16* xin, int mm, int q4, int nt) {
  const int w = x.w, lane = x.lane, g = x.g, c = x.c;
  const int lam = 8 * w + (lane >> 3);
  const int srow = 256 * mm + 32 * (4 * ((lam >> 4) & 1) + (lam & 3)) + 8 * q4 + 4 * (lam >> 5) + ((lam >> 2) & 3);
  {
    const char* src = (const char*)xin + srow * 1280 + (((lane & 7) ^ (lam & 7)) << 4);
#pragma unroll
    for (int kc = 0; kc < 10; ++kc) dma16(src + kc * 128, (u32)(uintptr_t)x.lds + (kc * 8 + w) * 1024);
  }
  v4u q[20];
  gen_q(128 * nt + 16 * w + c, g, q);
  f32x4 acc[4];
#pragma unroll
  for (int i = 0; i < 4; ++i) acc[i] = f32x4{0.f, 0.f, 0.f, 0.f};
  float s1 = 0.f, s2 = 0.f;
  const v4u ones = {0x3f803f80u, 0x3f803f80u, 0x3f803f80u, 0x3f803f80u};
  LAS char* a0 = x.lds + (c >> 3) * 1024 + (c & 7) * 128 + ((g ^ (c & 7)) << 4);
  LAS char* a1 = x.lds + (c >> 3) * 1024 + (c & 7) * 128 + (((g + 4) ^ (c & 7)) << 4);
  LAS char* own = x.lds + w * 1024 + lane * 16;
#pragma unroll
  for (int kc = 0; kc < 10; ++kc) {
    if (kc == 0) { VMWAIT(7); BAR(); }
    if (kc == 3) { VMWAIT(4); BAR(); }
    if (kc == 6) { VMWAIT(0); BAR(); }
#pragma unroll
    for (int rb = 0; rb < 4; ++rb) acc[rb] = mfma(*(LAS v4u*)(a0 + kc * 8192 + rb * 2048), q[2 * kc], acc[rb]);
#pragma unroll
    for (int rb = 0; rb < 4; ++rb) acc[rb] = mfma(*(LAS v4u*)(a1 + kc * 8192 + rb * 2048), q[2 * kc + 1], acc[rb]);
    v4u o = *(LAS v4u*)(own + kc * 8192);
    s1 = dot8(o, ones, s1);
    s2 = dot8(o, o, s2);
  }
#pragma unroll
  for (int m = 1; m < 8; m <<= 1) { s1 += __shfl_xor(s1, m); s2 += __shfl_xor(s2, m); }
  const float mu = s1 * (1.f / 640.f);
  const float var = fmaxf(s2 * (1.f / 640.f) - mu * mu, 0.f) + EPS;
  const float rstd = __builtin_amdgcn_rsqf(var);
  if ((lane & 7) == 0) {
    *(LAS float*)(x.lds + L_MISC + lam * 4) = rstd;
    const int n1 = srow >> 5, n2 = srow & 31;
    st4(x.rs, __builtin_bit_cast(u32, var * rstd), O_SIG + nt * 8192 + (((n1 >> 2) * 32 + n2) * 4 + (n1 & 3)) * 4, 0);
    if (nt == 0) st4(x.rs, __builtin_bit_cast(u32, mu), O_MU + srow * 4, 0);
  }
  BAR();
  const int slot = 128 * nt + 16 * w + c;
#pragma unroll
  for (int u = 0; u < 2; ++u) {
    f32x4 r0 = *(LAS f32x4*)(x.lds + L_MISC + (32 * u + 4 * g) * 4), r1 = *(LAS f32x4*)(x.lds + L_MISC + (32 * u + 16 + 4 * g) * 4);
    f32x4 p0 = acc[2 * u] * r0, p1 = acc[2 * u + 1] * r1;
    v4u o = {pk(p0[0], p0[1]), pk(p0[2], p0[3]), pk(p1[0], p1[1]), pk(p1[2], p1[3])};
    const int off = slot * 4096 + ((q4 >> 1) * 2 + (mm >> 2)) * 1024 + ((mm & 3) * 16 + 8 * (q4 & 1) + 4 * u + g) * 16;
    st16(x.rs, o, O_ZT + off, 0);
  }
}

DI void side_wt_load(const Ctx& x, const u16* wr, const u16* wi, int v, u32 (&a)[2][8]) {
  const int sg = v / 5, cb = v - sg * 5;
#pragma unroll
  for (int it = 0; it < 2; ++it) {
    const int item = x.w + 8 * it;
    const int rho = (sg * 4 + (item >> 2)) * 8 + (x.lane & 7);
    const int p = cb * 32 + (item & 3) * 8 + (x.lane >> 3);
    const int f0 = 16 * (rho >> 7) + (rho & 15) + 64 * (16 * ((rho >> 6) & 1) + 4 * ((rho >> 4) & 3));
#pragma unroll
    for (int r = 0; r < 4; ++r) {
      const int e = (f0 + 64 * r) * 321;
      u32 lr, li;
      __builtin_memcpy(&lr, wr + e + 2 * p, 4);
      __builtin_memcpy(&li, wi + e + 2 * p, 4);
      if (p == 0) {
        lr = (lr & 0xffff0000u) | wr[e + 320];
        li = (li & 0xffff0000u) | wi[e + 320];
      }
      a[it][2 * r] = lr; a[it][2 * r + 1] = li;
    }
  }
}
DI void side_wt_store(const Ctx& x, int v, const u32 (&a)[2][8]) {
  const int sg = v / 5, cb = v - sg * 5;
#pragma unroll
  for (int it = 0; it < 2; ++it) {
    const int item = x.w + 8 * it;
    const int rho = (sg * 4 + (item >> 2)) * 8 + (x.lane & 7);
    const int p = cb * 32 + (item & 3) * 8 + (x.lane >> 3);
    v4u lo, hi;
#pragma unroll
    for (int r = 0; r < 4; ++r) {
      lo[r] = __builtin_amdgcn_perm(a[it][2 * r + 1], a[it][2 * r], 0x05040100u);
      hi[r] = __builtin_amdgcn_perm(a[it][2 * r + 1], a[it][2 * r], 0x07060302u);
    }
    st16(x.rs, lo, O_WT + p * 16384 + rho * 16, 0);
    st16(x.rs, hi, O_WT + p * 16384 + 8192 + rho * 16, 0);
  }
}
template <int CPR> DI int tile_swz(int row) { return CPR == 80 ? (row & 3) << 2 : ((row >> 1) & 1) << 2; }
template <int CPR> DI void tile_dma(const Ctx& x, const u16* src, int lds_off) {
  for (int pc = x.w; pc < CPR / 2; pc += 8) {
    const int L = pc * 64 + x.lane, row = L / CPR, ch = L - row * CPR;
    dma16((const char*)src + (row * CPR + (ch ^ tile_swz<CPR>(row))) * 16, (u32)(uintptr_t)x.lds + lds_off + pc * 1024);
  }
}
template <int CPR, int GP, bool COLSUM> DI void tile_out(const Ctx& x, int lds_off, int dst, int tile_bytes, int ks) {
  const int q = x.c >> 2, p = x.c & 3;
  const v4u ones = {0x3f803f80u, 0x3f803f80u, 0x3f803f80u, 0x3f803f80u};
  for (int blk = x.w; blk < CPR / 2; blk += 8) {
    int tile, nb; bool single;
    group_dst<GP>(blk >> 1, blk & 1, tile, nb, single);
    const int ch = (blk >> 1) * 4 + (single ? 2 * (blk & 1) + (p >> 1) : p), hb = single ? (p & 1) : (blk & 1);
    const int r0 = 8 * x.g + q, r1 = r0 + 4;
    v2u lo = trread(x.lds + lds_off + (r0 * CPR + (ch ^ tile_swz<CPR>(r0))) * 16 + hb * 8);
    v2u hi = trread(x.lds + lds_off + (r1 * CPR + (ch ^ tile_swz<CPR>(r1))) * 16 + hb * 8);
    v4u f = {lo.x, lo.y, hi.x, hi.y};
    st16(x.rs, f, dst + tile * tile_bytes + (ks * (2 * GP + 1) + nb) * 1024 + x.lane * 16, 0);
    if (COLSUM) {
      f32x4 s = mfma(f, ones, f32x4{0.f, 0.f, 0.f, 0.f});
      if (x.c == 0) st16(x.rs, __builtin_bit_cast(v4u, s), O_CS3 + (ks * 1792 + tile * 112 + nb * 16 + 4 * x.g) * 4, 0);
    }
  }
}
DI void side_qc(const Ctx& x, int job, int njobs) {
  for (int pc = job * 8 + x.w; pc < 800; pc += njobs * 8) {
    const int ks = pc / 40, blk = pc - ks * 40;
    int tile, nb; bool single;
    group_dst<2>(blk >> 1, blk & 1, tile, nb, single);
    const int n = (blk >> 1) * 32 + (single ? 16 * (blk & 1) + x.c : 8 * (x.c >> 2) + 4 * (blk & 1) + (x.c & 3));
    v4u f;
#pragma unroll
    for (int i = 0; i < 4; ++i) {
      const int k = 16 * ks + 4 * x.g + i;
      const float a = (float)((n * k) % 640) * (1.f / 640.f);
      float cv = 0.05590169944f * cosr(a), sv = -0.05590169944f * sinr(a);
      if (k == 0) { cv = (n & 1) ? -0.03952847075f : 0.03952847075f; sv = 0.f; }
      f[i] = pk(cv, sv);
    }
    st16(x.rs, f, O_BC + (8 + tile) * 102400 + (ks * 5 + nb) * 1024 + x.lane * 16, 0);
  }
}
DI void w1p_dma(const Ctx& x, const u16* w1, int ct) {
  const int row = 8 * x.w + (x.lane >> 3);
  const char* src = (const char*)w1 + row * 1280 + ct * 128 + (((x.lane & 7) ^ (((row >> 1) & 1) << 2)) << 4);
#pragma unroll
  for (int i = 0; i < 10; ++i) dma16(src + i * 64 * 1280, (u32)(uintptr_t)x.lds + (i * 8 + x.w) * 1024);
}
DI void w1p_run(const Ctx& x, int ct, int nt) {
  v4u q[20];
  gen_q(128 * nt + 16 * x.w + x.c, x.g, q);
  f32x4 acc[4];
  int addr[4], dsto[4];
  const int qq = x.c >> 2, p = x.c & 3, r0 = 8 * x.g + qq;
#pragma unroll
  for (int rb = 0; rb < 4; ++rb) {
    acc[rb] = f32x4{0.f, 0.f, 0.f, 0.f};
    int tile, nb; bool single;
    group_dst<2>(2 * ct + (rb >> 1), rb & 1, tile, nb, single);
    const int ch = (rb >> 1) * 4 + (single ? 2 * (rb & 1) + (p >> 1) : p), hb = single ? (p & 1) : (rb & 1);
    addr[rb] = r0 * 128 + ((ch ^ (((r0 >> 1) & 1) << 2)) << 4) + hb * 8;
    dsto[rb] = tile * 102400 + nb * 1024;
  }
  VMWAIT(0);
  BAR();
#pragma unroll
  for (int ks = 0; ks < 20; ++ks) {
#pragma unroll
    for (int rb = 0; rb < 4; ++rb) {
      v2u lo = trread(x.lds + addr[rb] + ks * 4096), hi = trread(x.lds + addr[rb] + ks * 4096 + 512);
      acc[rb] = mfma(q[ks], v4u{lo.x, lo.y, hi.x, hi.y}, acc[rb]);
    }
  }
  const int ksd = 4 * nt + (x.w >> 1);
#pragma unroll
  for (int rb = 0; rb < 4; ++rb)
    st8(x.rs, v2u{pk(acc[rb][0], acc[rb][1]), pk(acc[rb][2], acc[rb][3])},
        O_BC + dsto[rb] + ksd * 5120 + ((2 * (x.w & 1) + (x.g >> 1)) * 16 + x.c) * 16 + (x.g & 1) * 8, 0);
}
DI void side_jobs(const Ctx& x, int v, const u16* wr, const u16* wi, const u16* w1, const u16* w2, const u16* w3, const u16* w4) {
  u32 a[2][8];
  const int vw = 95 - v;
  if (vw < 80) side_wt_load(x, wr, wi, vw, a);
  const int u0 = (v - 70) * 3;
  if (v < 50) w1p_dma(x, w1, v / 5);
  else if (v < 70) tile_dma<216>(x, w3 + (v - 50) * 32 * 1728, 0);
  else {
    for (int i = 0; i < 3; ++i) {
      const int u = u0 + i;
      if (u < 74) tile_dma<80>(x, u < 20 ? w2 + u * 32 * 640 : w4 + (u - 20) * 32 * 640, i * 40960);
    }
  }
  if (vw < 80) {
    side_wt_store(x, vw, a);
    publish(x, O_FW + ((vw % 5) * 16 + vw / 5) * 8);
  }
  if (v < 50) w1p_run(x, v / 5, v % 5);
  else {
    side_qc(x, v - 50, 46);
    VMWAIT(0);
    BAR();
    if (v < 70) tile_out<216, 3, true>(x, 0, O_B6, 143360, v - 50);
    else {
      for (int i = 0; i < 3; ++i) {
        const int u = u0 + i;
        if (u < 20) tile_out<80, 2, false>(x, i * 40960, O_B5, 102400, u);
        else if (u < 74) tile_out<80, 2, false>(x, i * 40960, O_B7, 276480, u - 20);
      }
    }
  }
  publish(x, O_FS + v * 8);
}

DI v4u negv(v4u a) { return a ^ 0x80008000u; }
DI v4u pack8(f32x4 a, f32x4 b) { return v4u{pk(a[0], a[1]), pk(a[2], a[3]), pk(b[0], b[1]), pk(b[2], b[3])}; }
template <bool WAIT> DI void phase_b(const Ctx& x, int p) {
  const int w = x.w, lane = x.lane, g = x.g, c = x.c, wq = w & 3, col = w >> 2;
  v4u C64[2], S64[2], N64[2], C32[2], S32[2], N32[2];
  f32x4 tc[2], ts[2];
  {
    const float f1 = (float)(16 * wq + c);
#pragma unroll
    for (int ks = 0; ks < 2; ++ks) {
      float cv[8], sv[8];
#pragma unroll
      for (int j = 0; j < 8; ++j) { const float a = f1 * (float)(32 * ks + 8 * g + j) * (1.f / 64.f); cv[j] = cosr(a); sv[j] = sinr(a); }
      C64[ks] = v4u{pk(cv[0], cv[1]), pk(cv[2], cv[3]), pk(cv[4], cv[5]), pk(cv[6], cv[7])};
      S64[ks] = v4u{pk(sv[0], sv[1]), pk(sv[2], sv[3]), pk(sv[4], sv[5]), pk(sv[6], sv[7])};
      N64[ks] = negv(S64[ks]);
#pragma unroll
      for (int j = 0; j < 8; ++j) {
        const float a = (float)(16 * ks + c) * (float)(16 * (j >> 2) + 4 * g + (j & 3)) * (1.f / 32.f);
        cv[j] = cosr(a); sv[j] = sinr(a);
      }
      C32[ks] = v4u{pk(cv[0], cv[1]), pk(cv[2], cv[3]), pk(cv[4], cv[5]), pk(cv[6], cv[7])};
      S32[ks] = v4u{pk(sv[0], sv[1]), pk(sv[2], sv[3]), pk(sv[4], sv[5]), pk(sv[6], sv[7])};
      N32[ks] = negv(S32[ks]);
#pragma unroll
      for (int r = 0; r < 4; ++r) {
        const float a = f1 * (float)(16 * ks + 4 * g + r) * (1.f / 2048.f);
        tc[ks][r] = 0.02209708691f * cosr(a); ts[ks][r] = 0.02209708691f * sinr(a);
      }
    }
  }
  const int nt = p >> 5, kap = 2 * p + col;
  if (WAIT) poll(x, O_FW + (nt * 16 + (lane & 15)) * 8, lane < 16);
  v4u wv[2];
#pragma unroll
  for (int t = 0; t < 2; ++t) wv[t] = ld16(x.rs, O_WT + kap * 8192 + (wq * 2 + t) * 1024 + lane * 16, 0);
  if (WAIT) poll(x, O_FA + (nt * 32 + (lane & 31)) * 8, lane < 32);
  v4u xr[2][2], xi[2][2];
#pragma unroll
  for (int i = 0; i < 4; ++i) {
    xr[i >> 1][i & 1] = ld16(x.rs, O_ZT + kap * 8192 + i * 1024 + lane * 16, 0);
    xi[i >> 1][i & 1] = ld16(x.rs, O_ZT + kap * 8192 + 4096 + i * 1024 + lane * 16, 0);
  }
  v2u z[4];
#pragma unroll
  for (int i = 0; i < 4; ++i)
    z[i] = ld8(x.rs, O_ZT + (4 * p + i) * 4096 + (col * 2 + (wq >> 1)) * 1024 + ((2 * (wq & 1) + (g >> 1)) * 16 + c) * 16 + (g & 1) * 8, 0);
  const f32x4 sg = __builtin_bit_cast(f32x4, ld16(x.rs, O_SIG + nt * 8192 + ((4 * wq + g) * 32 + 16 * col + c) * 16, 0));
  const f32x4 zero = {0.f, 0.f, 0.f, 0.f};
  f32x4 ar[2], ai[2], br[2], bi[2];
#pragma unroll
  for (int b = 0; b < 2; ++b) {
    ar[b] = mfma(xr[b][0], C64[0], zero); ai[b] = mfma(xi[b][0], C64[0], zero);
    ar[b] = mfma(xi[b][0], S64[0], ar[b]); ai[b] = mfma(xr[b][0], N64[0], ai[b]);
    ar[b] = mfma(xr[b][1], C64[1], ar[b]); ai[b] = mfma(xi[b][1], C64[1], ai[b]);
    ar[b] = mfma(xi[b][1], S64[1], ar[b]); ai[b] = mfma(xr[b][1], N64[1], ai[b]);
  }
#pragma unroll
  for (int b = 0; b < 2; ++b) { br[b] = ar[b] * tc[b] + ai[b] * ts[b]; bi[b] = ai[b] * tc[b] - ar[b] * ts[b]; }
  v4u pr = pack8(br[0], br[1]), pi = pack8(bi[0], bi[1]);
#pragma unroll
  for (int t = 0; t < 2; ++t) {
    ar[t] = mfma(C32[t], pr, zero); ai[t] = mfma(C32[t], pi, zero);
    ar[t] = mfma(S32[t], pi, ar[t]); ai[t] = mfma(N32[t], pr, ai[t]);
  }
#pragma unroll
  for (int t = 0; t < 2; ++t)
#pragma unroll
    for (int r = 0; r < 4; ++r) {
      const float wre = blo(wv[t][r]), wim = bhi(wv[t][r]);
      br[t][r] = ar[t][r] * wre - ai[t][r] * wim;
      bi[t][r] = ar[t][r] * wim + ai[t][r] * wre;
    }
  pr = pack8(br[0], br[1]); pi = pack8(bi[0], bi[1]);
#pragma unroll
  for (int t = 0; t < 2; ++t) {
    ar[t] = mfma(C32[t], pr, zero); ai[t] = mfma(C32[t], pi, zero);
    ar[t] = mfma(N32[t], pi, ar[t]); ai[t] = mfma(S32[t], pr, ai[t]);
  }
  {
    LAS char* img = x.lds + col * 9216 + (16 * wq + c) * 72 + g * 8;
#pragma unroll
    for (int t = 0; t < 2; ++t) {
      f32x4 ur = ar[t] * tc[t] - ai[t] * ts[t], ui = ai[t] * tc[t] + ar[t] * ts[t];
      *(LAS v2u*)(img + t * 32) = v2u{pk(ur[0], ur[1]), pk(ur[2], ur[3])};
      *(LAS v2u*)(img + 4608 + t * 32) = v2u{pk(ui[0], ui[1]), pk(ui[2], ui[3])};
    }
  }
  BAR();
  LAS char* rd = x.lds + (8 * g + (c >> 2)) * 72 + col * 32 + (c & 3) * 8;
#pragma unroll
  for (int cc = 0; cc < 2; ++cc) {
    ar[cc] = zero; ai[cc] = zero;
#pragma unroll
    for (int ks = 0; ks < 2; ++ks) {
      v2u r0 = trread(rd + cc * 9216 + ks * 2304), r1 = trread(rd + cc * 9216 + ks * 2304 + 288);
      v2u i0 = trread(rd + cc * 9216 + 4608 + ks * 2304), i1 = trread(rd + cc * 9216 + 4608 + ks * 2304 + 288);
      const v4u ur = {r0.x, r0.y, r1.x, r1.y}, ui = {i0.x, i0.y, i1.x, i1.y};
      ar[cc] = mfma(C64[ks], ur, ar[cc]); ai[cc] = mfma(C64[ks], ui, ai[cc]);
      ar[cc] = mfma(N64[ks], ui, ar[cc]); ai[cc] = mfma(S64[ks], ur, ai[cc]);
    }
  }
  if (p == 0) ai[0] = zero;
  u32 o[8];
#pragma unroll
  for (int r = 0; r < 4; ++r) {
    float zz[4];
#pragma unroll
    for (int i = 0; i < 4; ++i) zz[i] = (r & 1) ? bhi(z[i][r >> 1]) : blo(z[i][r >> 1]);
    o[2 * r] = pk(sg[r] * zz[0] + ar[0][r], sg[r] * zz[1] + ai[0][r]);
    o[2 * r + 1] = pk(sg[r] * zz[2] + ar[1][r], sg[r] * zz[3] + ai[1][r]);
  }
  const int ob = O_HB + p * 16384 + (((8 * wq + 2 * g) * 2 + col) * 16 + c) * 16;
  st16(x.rs, v4u{o[0], o[1], o[2], o[3]}, ob, 0);
  st16(x.rs, v4u{o[4], o[5], o[6], o[7]}, ob + 512, 0);
}

DI void dma_lin(const Ctx& x, int ws_off, int lds_off, int npieces) {
  const char* src = x.ws + ws_off + x.lane * 16;
  for (int pc = x.w; pc < npieces; pc += 8) dma16(src + pc * 1024, (u32)(uintptr_t)x.lds + lds_off + pc * 1024);
}
DI int ft_pair(int rowblk, int KS, int gam, int g, int c) { return (rowblk * KS + gam) * 1024 + (g * 16 + c) * 16; }
DI int ft_single(int rowblk, int KS, int gam, int h, int g, int c) {
  return (rowblk * KS + gam) * 1024 + ((2 * h + (g >> 1)) * 16 + c) * 16 + (g & 1) * 8;
}

template <int NB, bool HPLANES>
DI void gemm128(const Ctx& x, int aoff, f32x4 (&acc)[2][NB], float& ssq) {
  constexpr int LA = 4;
  const int kw = x.w >> 2;
  v4u a[10][2];
  auto lda = [&](int it) {
    if (HPLANES) { a[it][0] = ld16(x.rs, aoff, it * 262144); a[it][1] = ld16(x.rs, aoff, it * 262144 + 16384); }
    else { a[it][0] = ld16(x.rs, aoff, it * 2048); a[it][1] = ld16(x.rs, aoff, it * 2048 + 20480); }
  };
#pragma unroll
  for (int it = 0; it < LA; ++it) lda(it);
  SCHED();
  VMWAIT(8);
  BAR();
#pragma unroll
  for (int i = 0; i < 2; ++i)
#pragma unroll
    for (int n = 0; n < NB; ++n) acc[i][n] = f32x4{0.f, 0.f, 0.f, 0.f};
  float s0 = 0.f, s1 = 0.f;
  LAS char* bp = x.lds + kw * NB * 1024 + x.lane * 16;
#pragma unroll
  for (int it = 0; it < 10; ++it) {
    if (it + LA < 10) lda(it + LA);
    v4u a0 = a[it][0], a1 = a[it][1];
    if (HPLANES) {
      a0 = v4u{a[it][0].x, a[it][0].y, a[it][1].x, a[it][1].y};
      a1 = v4u{a[it][0].z, a[it][0].w, a[it][1].z, a[it][1].w};
      s0 = dot8(a0, a0, s0); s1 = dot8(a1, a1, s1);
    }
#pragma unroll
    for (int n = 0; n < NB; ++n) {
      const v4u b = *(LAS v4u*)(bp + (it * 2 * NB + n) * 1024);
      acc[0][n] = mfma(b, a0, acc[0][n]);
      acc[1][n] = mfma(b, a1, acc[1][n]);
    }
    SCHED();
  }
  BAR();
  LAS char* mine = x.lds + x.w * (NB + 1) * 1024 + x.lane * 16;
  LAS char* other = x.lds + (x.w ^ 4) * (NB + 1) * 1024 + x.lane * 16;
  if (kw) {
#pragma unroll
    for (int n = 0; n < NB; ++n) { *(LAS f32x4*)(mine + n * 1024) = acc[0][n]; acc[0][n] = acc[1][n]; }
    if (HPLANES) { *(LAS float*)(mine + NB * 1024) = s0; s0 = s1; }
  } else {
#pragma unroll
    for (int n = 0; n < NB; ++n) *(LAS f32x4*)(mine + n * 1024) = acc[1][n];
    if (HPLANES) *(LAS float*)(mine + NB * 1024) = s1;
  }
  BAR();
#pragma unroll
  for (int n = 0; n < NB; ++n) acc[0][n] += *(LAS f32x4*)(other + n * 1024);
  if (HPLANES) ssq = xsum16_32(s0 + *(LAS float*)(other + NB * 1024));
}

template <int KS>
DI void gemm64(const Ctx& x, int aoff, int bws, f32x4 (&o)[3]) {
  constexpr int NIT = (KS + 3) / 4, LA = KS == 20 ? 5 : 4;
  const int kw = x.w >> 1;
  v4u a[NIT][2];
  const int a0off = aoff + (kw & 1) * KS * 1024, a1off = aoff + ((kw & 1) ^ 1) * KS * 1024;
  auto lda = [&](int it) { a[it][0] = ld16(x.rs, a0off, it * 4096); a[it][1] = ld16(x.rs, a1off, it * 4096); };
#pragma unroll
  for (int it = 0; it < LA; ++it) lda(it);
  SCHED();
  if (LA == 5) VMWAIT(10); else VMWAIT(8);
  BAR();
  f32x4 acc[2][5];
#pragma unroll
  for (int i = 0; i < 2; ++i)
#pragma unroll
    for (int n = 0; n < 5; ++n) acc[i][n] = f32x4{0.f, 0.f, 0.f, 0.f};
  LAS char* bs = x.lds + kw * 5120 + x.lane * 16;
  LAS char* b0 = bs + (kw >> 1) * 2048;
  LAS char* b1 = bs + ((kw >> 1) ^ 1) * 2048;
#pragma unroll
  for (int it = 0; it < NIT; ++it) {
    if (KS == 54) {
      if (it == 4) { BAR(); dma_lin(x, bws + 28 * 5120, 0, 80); }
      if (it == 7) { VMWAIT(6); BAR(); dma_lin(x, bws + 44 * 5120, 16 * 5120, 50); }
      if (it == 11) { VMWAIT(6); BAR(); }
    }
    if (it + LA < NIT) lda(it + LA);
    if (it * 4 + 3 < KS || kw < KS - it * 4) {
      const int so = (it % 7) * 20480;
      const v4u f0 = *(LAS v4u*)(b0 + so), f1 = *(LAS v4u*)(b0 + so + 1024), f2 = *(LAS v4u*)(b1 + so), f3 = *(LAS v4u*)(b1 + so + 1024),
                f4 = *(LAS v4u*)(bs + so + 4096);
#pragma unroll
      for (int i = 0; i < 2; ++i) {
        acc[i][0] = mfma(f0, a[it][i], acc[i][0]); acc[i][1] = mfma(f1, a[it][i], acc[i][1]);
        acc[i][2] = mfma(f2, a[it][i], acc[i][2]); acc[i][3] = mfma(f3, a[it][i], acc[i][3]);
        acc[i][4] = mfma(f4, a[it][i], acc[i][4]);
      }
    }
    SCHED();
  }
  BAR();
  LAS char* mine = x.lds + x.w * 10240 + x.lane * 16;
#pragma unroll
  for (int n = 2; n < 5; ++n) *(LAS f32x4*)(mine + n * 1024) = acc[0][n];
#pragma unroll
  for (int n = 0; n < 5; ++n) *(LAS f32x4*)(mine + (5 + n) * 1024) = acc[1][n];
  BAR();
  o[0] = acc[0][0]; o[1] = acc[0][1]; o[2] = acc[0][4];
#pragma unroll
  for (int m = 1; m < 4; ++m) {
    LAS char* s = x.lds + (x.w ^ (2 * m)) * 10240 + x.lane * 16 + (m & 1) * 5120;
    o[0] += *(LAS f32x4*)(s + (m >> 1) * 2048);
    o[1] += *(LAS f32x4*)(s + (m >> 1) * 2048 + 1024);
    o[2] += *(LAS f32x4*)(s + 4096);
  }
}

DI f32x4 gelu4(f32x4 v) { return f32x4{gelu(v[0]), gelu(v[1]), gelu(v[2]), gelu(v[3])}; }
DI v2u pack4(f32x4 a) { return v2u{pk(a[0], a[1]), pk(a[2], a[3])}; }
DI f32x4 unlo(v4u v) { return f32x4{blo(v.x), bhi(v.x), blo(v.y), bhi(v.y)}; }
DI f32x4 unhi(v4u v) { return f32x4{blo(v.z), bhi(v.z), blo(v.w), bhi(v.w)}; }
DI f32x4 un4(v2u v) { return f32x4{blo(v.x), bhi(v.x), blo(v.y), bhi(v.y)}; }

DI void phase_c(const Ctx& x, int P, int t) {
  const int mw = x.w & 3, kw = x.w >> 2, g = x.g, c = x.c;
  const int np = 2 * P + (mw >> 1), b = mw & 1;
  const int rowblk = 4 * np + b + 2 * kw, row = rowblk * 16 + c;
  const float mu = __builtin_bit_cast(float, ld4(x.rs, O_MU + row * 4, 0));
  f32x4 acc[2][5];
  float ssq;
  gemm128<5, true>(x, O_HB + (8 * kw + 2 * g) * 16384 + ((np * 2 + b) * 16 + c) * 16, acc, ssq);
  const float vh = ssq * (1.f / 640.f);
  const float rstd = __builtin_amdgcn_rsqf(vh + EPS);
  const int t8 = t & 7, g0 = (t8 >> 1) * 5 + (t8 & 1) * 3, gs = (t8 >> 1) * 5 + 2;
  if (t < 8) {
    const float a2 = rstd * __builtin_amdgcn_rsqf(vh * rstd * rstd + EPS);
    const int sh = (t8 >> 1) == 0 ? 1 : (t8 >> 1) == 1 ? -1 : (t8 >> 1) == 2 ? 2 : -2;
    const int dr = row + sh, drow = dr & 2047;
    const float keep = (dr == drow) ? a2 : 0.f;
    const int db = drow >> 4, dc = drow & 15;
#pragma unroll
    for (int i = 0; i < 2; ++i)
      st16(x.rs, pack8(gelu4(acc[0][2 * i] * keep), gelu4(acc[0][2 * i + 1] * keep)), O_G + ft_pair(db, 20, g0 + i, g, dc), 0);
    st8(x.rs, pack4(gelu4(acc[0][4] * keep)), O_G + ft_single(db, 20, gs, t8 & 1, g, dc), 0);
  } else {
#pragma unroll
    for (int i = 0; i < 2; ++i) {
      const int off = ft_pair(rowblk, 20, g0 + i, g, c);
      st16(x.rs, pack8(acc[0][2 * i] * rstd, acc[0][2 * i + 1] * rstd), O_XB + off, 0);
      st16(x.rs, pack8(acc[0][2 * i] + mu, acc[0][2 * i + 1] + mu), O_HH + off, 0);
    }
    const int off = ft_single(rowblk, 20, gs, t8 & 1, g, c);
    st8(x.rs, pack4(acc[0][4] * rstd), O_XB + off, 0);
    st8(x.rs, pack4(acc[0][4] + mu), O_HH + off, 0);
  }
}

DI void phase_5(const Ctx& x, int P, int hf, int t8) {
  const int mw = x.w & 1, kw = x.w >> 1, g = x.g, c = x.c;
  const int rb0 = 8 * P + 4 * hf + 2 * mw, rowblk = rb0 + (kw & 1);
  const int gp = (t8 >> 1) * 5 + (t8 & 1) * 3 + (kw >> 1), gs = (t8 >> 1) * 5 + 2;
  const int op = ft_pair(rowblk, 20, gp, g, c), os = ft_single(rowblk, 20, gs, t8 & 1, g, c);
  const v4u xp = ld16(x.rs, O_XB + op, 0);
  const v2u xs = ld8(x.rs, O_XB + os, 0);
  f32x4 o[3];
  gemm64<20>(x, O_G + (rb0 * 20 + kw) * 1024 + x.lane * 16, 0, o);
  const v4u r = pack8(o[0] + unlo(xp), o[1] + unhi(xp));
  st16(x.rs, r, O_X1 + op, 0);
  const v4u ones = {0x3f803f80u, 0x3f803f80u, 0x3f803f80u, 0x3f803f80u};
  float s1 = dot8(r, ones, 0.f), s2 = dot8(r, r, 0.f);
  if (kw < 2) {
    const v2u q = pack4(o[2] + un4(xs));
    st8(x.rs, q, O_X1 + os, 0);
    s1 = dot2(q.y, ones.x, dot2(q.x, ones.x, s1));
    s2 = dot2(q.y, q.y, dot2(q.x, q.x, s2));
  }
  s1 = xsum16_32(s1); s2 = xsum16_32(s2);
  if (g == 0)
    st8(x.rs, v2u{__builtin_bit_cast(u32, s1), __builtin_bit_cast(u32, s2)}, O_ST1 + (rowblk * 16 + c) * 128 + (t8 * 2 + (kw >> 1)) * 8, 0);
}

DI void phase_6(const Ctx& x, int P, int t) {
  const int mw = x.w & 3, kw = x.w >> 2, g = x.g, c = x.c;
  const int rb0 = 8 * P + 2 * mw, rowblk = rb0 + kw;
  const f32x4 p0 = __builtin_bit_cast(f32x4, ld16(x.rs, O_ST1 + (rowblk * 16 + c) * 128 + g * 32, 0));
  const f32x4 p1 = __builtin_bit_cast(f32x4, ld16(x.rs, O_ST1 + (rowblk * 16 + c) * 128 + g * 32 + 16, 0));
  f32x4 acc[2][7];
  float dummy;
  gemm128<7, false>(x, O_X1 + (rb0 * 20 + kw) * 1024 + x.lane * 16, acc, dummy);
  const float m1 = xsum16_32((p0[0] + p0[2]) + (p1[0] + p1[2])) * (1.f / 640.f);
  const float q1 = xsum16_32((p0[1] + p0[3]) + (p1[1] + p1[3])) * (1.f / 640.f);
  const float r1 = __builtin_amdgcn_rsqf(fmaxf(q1 - m1 * m1, 0.f) + EPS), nm = -m1 * r1;
  const int g0 = (t >> 1) * 7 + (t & 1) * 4, gs = (t >> 1) * 7 + 3;
  LAS f32x4* cs = (LAS f32x4*)(x.lds + L_MISC) + g;
#pragma unroll
  for (int i = 0; i < 3; ++i) {
    const v4u r = pack8(gelu4(acc[0][2 * i] * r1 + cs[8 * i] * nm), gelu4(acc[0][2 * i + 1] * r1 + cs[8 * i + 4] * nm));
    if (g0 + i < 54) st16(x.rs, r, O_U + ft_pair(rowblk, 54, g0 + i, g, c), 0);
  }
  st8(x.rs, pack4(gelu4(acc[0][6] * r1 + cs[24] * nm)), O_U + ft_single(rowblk, 54, gs, t & 1, g, c), 0);
}
DI void colsum_6(const Ctx& x, int t) {
  if (x.tid < 112) {
    float s = 0.f;
#pragma unroll
    for (int k = 0; k < 20; ++k) s += __builtin_bit_cast(float, ld4(x.rs, O_CS3 + (t * 112 + x.tid) * 4, k * 7168));
    *(LAS float*)(x.lds + L_MISC + x.tid * 4) = s;
  }
}

DI void phase_7(const Ctx& x, int P, int hf, int t8, u16* out) {
  const int mw = x.w & 1, kw = x.w >> 1, g = x.g, c = x.c;
  const int rb0 = 8 * P + 4 * hf + 2 * mw, rowblk = rb0 + (kw & 1);
  const int gp = (t8 >> 1) * 5 + (t8 & 1) * 3 + (kw >> 1), gs = (t8 >> 1) * 5 + 2;
  const int op = ft_pair(rowblk, 20, gp, g, c), os = ft_single(rowblk, 20, gs, t8 & 1, g, c);
  const v4u xp = ld16(x.rs, O_X1 + op, 0), hp = ld16(x.rs, O_HH + op, 0);
  const v2u xs = ld8(x.rs, O_X1 + os, 0), hs = ld8(x.rs, O_HH + os, 0);
  f32x4 o[3];
  gemm64<54>(x, O_U + (rb0 * 54 + kw) * 1024 + x.lane * 16, O_B7 + t8 * 276480, o);
  char* orow = (char*)out + (rowblk * 16 + c) * 1280;
  *(v4u*)(orow + gp * 64 + g * 16) = pack8(o[0] + unlo(xp) + unlo(hp), o[1] + unhi(xp) + unhi(hp));
  if (kw < 2) *(v2u*)(orow + gs * 64 + (t8 & 1) * 32 + g * 8) = pack4(o[2] + un4(xs) + un4(hs));
}

template <int LO, int HI>
__global__ __launch_bounds__(512, 2) void fused(const u16* __restrict__ xin, const u16* __restrict__ wr, const u16* __restrict__ wi,
                                                const u16* __restrict__ w1, const u16* __restrict__ w2, const u16* __restrict__ w3,
                                                const u16* __restrict__ w4, u16* __restrict__ out, char* ws) {
  extern __shared__ __attribute__((aligned(16))) char smem[];
  Ctx x;
  x.ws = ws; x.rs = __builtin_amdgcn_make_buffer_rsrc(ws, 0, WS_END, 0x00020000); x.lds = (LAS char*)smem;
  x.tid = threadIdx.x; x.lane = x.tid & 63; x.w = __builtin_amdgcn_readfirstlane(x.tid >> 6); x.g = x.lane >> 4; x.c = x.lane & 15;
  GAS u64* ep = (GAS u64*)(ws + O_EPOCH + LO * 256);
  x.tag = __hip_atomic_load(ep, __ATOMIC_RELAXED, __HIP_MEMORY_SCOPE_AGENT) * 6364136223846793005ull + 1442695040888963407ull;
  const int b = blockIdx.x, xcd = b & 7, i = b >> 3;
  const int P = 2 * xcd + (i >> 4), t = i & 15, hf = t >> 3, t8 = t & 7;

  if (LO <= 0) {
    if (b < 160) { phase_a(x, xin, xcd, i & 3, i >> 2); if (HI > 0) publish(x, O_FA + ((i >> 2) * 32 + xcd * 4 + (i & 3)) * 8); }
    else side_jobs(x, b - 160, wr, wi, w1, w2, w3, w4);
  }
  if (LO <= 1 && HI >= 1) {
    if (b < 160) { phase_b<(LO < 1)>(x, b); if (HI > 1) publish(x, O_FB + b * 8); }
  }
  if (LO <= 2 && HI >= 2) {
    if (LO < 1) {
      if (x.w == 0) {
        poll(x, O_FS + x.lane * 8, true);
        poll(x, O_FS + (64 + x.lane) * 8, x.lane < 32);
        __builtin_amdgcn_fence(__ATOMIC_ACQUIRE, "agent");
        VMWAIT(0);
      }
      BAR();
    }
    dma_lin(x, O_BC + t * 102400, 0, 100);
    if (LO < 2) {
      if (x.w == 0) {
        poll(x, O_FB + x.lane * 8, true);
        poll(x, O_FB + (64 + x.lane) * 8, true);
        poll(x, O_FB + (128 + x.lane) * 8, x.lane < 32);
      }
      BAR();
    }
    phase_c(x, P, t);
    if (HI > 2) publish(x, O_FC + (P * 16 + t) * 8);
  }
  if (LO <= 3 && HI >= 3) {
    dma_lin(x, O_B5 + t8 * 102400, 0, 100);
    if (LO < 3) {
      if (x.w == 0) poll(x, O_FC + (x.lane < 16 ? P * 16 + x.lane : ((P + (hf ? 1 : 15)) & 15) * 16 + (x.lane & 7)) * 8, x.lane < 24);
      BAR();
    }
    phase_5(x, P, hf, t8);
    if (HI > 3) publish(x, O_F5 + (P * 16 + t) * 8);
  }
  if (LO <= 4 && HI >= 4) {
    dma_lin(x, O_B6 + t * 143360, 0, 140);
    colsum_6(x, t);
    if (LO < 4) {
      if (x.w == 0) poll(x, O_F5 + (P * 16 + x.c) * 8, x.lane < 16);
      BAR();
    }
    phase_6(x, P, t);
    if (HI > 4) publish(x, O_F6 + (P * 16 + t) * 8);
  }
  if (LO <= 5 && HI >= 5) {
    dma_lin(x, O_B7 + t8 * 276480, 0, 140);
    if (LO < 5) {
      if (x.w == 0) poll(x, O_F6 + (P * 16 + x.c) * 8, x.lane < 16);
      BAR();
    }
    phase_7(x, P, hf, t8, out);
  }
  if (LO == 0 && HI == 5 && b == 0 && x.tid == 0) __hip_atomic_store(ep, x.tag, __ATOMIC_RELAXED, __HIP_MEMORY_SCOPE_AGENT);
}

template <int LO, int HI>
static void launch(void* const* d_in, void* d_out, void* d_ws, hipStream_t stream) {
  (void)hipFuncSetAttribute((const void*)fused<LO, HI>, hipFuncAttributeMaxDynamicSharedMemorySize, LDS_BYTES);
  fused<LO, HI><<<256, 512, LDS_BYTES, stream>>>((const u16*)d_in[0], (const u16*)d_in[1], (const u16*)d_in[2], (const u16*)d_in[4],
                                                 (const u16*)d_in[5], (const u16*)d_in[6], (const u16*)d_in[7], (u16*)d_out, (char*)d_ws);
}

extern "C" void kernel_launch(void* const* d_in, const int* in_sizes, int n_in, void* d_out, int out_size, void* d_ws,
                              size_t ws_size, hipStream_t stream) {
  launch<0, 5>(d_in, d_out, d_ws, stream);
}
```

```cpp
#include <hip/hip_runtime.h>
#include <stdint.h>

typedef unsigned u32;
typedef unsigned long long u64;
typedef unsigned short u16;
typedef u32 v4u __attribute__((ext_vector_type(4)));
typedef u32 v2u __attribute__((ext_vector_type(2)));
typedef float f32x4 __attribute__((ext_vector_type(4)));
typedef float f32x2 __attribute__((ext_vector_type(2)));
typedef __bf16 bf16x8 __attribute__((ext_vector_type(8)));
typedef __bf16 bf16x2 __attribute__((ext_vector_type(2)));
typedef short s16x4 __attribute__((ext_vector_type(4)));
typedef __amdgpu_buffer_rsrc_t rsrc_t;
#define LAS __attribute__((address_space(3)))
#define GAS __attribute__((address_space(1)))
#define DI __device__ __forceinline__

constexpr int ACT = 2048 * 640 * 2;
constexpr int O_EPOCH = 0;
constexpr int O_FLAG = 4096;
enum { F_A = 0, F_W = 160, F_C = 240, F_5 = 496, F_6 = 752, F_S = 1008, F_B = F_S + 8 * 256, F_END = F_B + 8 * 160 };
__host__ __device__ constexpr int flag_at(int i) { return O_FLAG + i * 256; }
constexpr int O_SIG = 2 << 20;
static_assert(flag_at(F_END) <= O_SIG, "flag area");
constexpr int O_MU = O_SIG + 5 * 8192;
constexpr int O_CS3 = O_MU + 8192;
constexpr int O_ST1 = O_CS3 + 20 * 1792 * 4;
constexpr int O_ZT = O_ST1 + 2048 * 128;
constexpr int O_WT = O_ZT + ACT;
constexpr int O_HB = O_WT + ACT;
constexpr int O_BC = O_HB + ACT;
constexpr int O_B5 = O_BC + 16 * 102400 + 8192;
constexpr int O_B6 = O_B5 + 8 * 102400 + 8192;
constexpr int O_B7 = O_B6 + 16 * 143360 + 8192;
constexpr int O_G = O_B7 + 8 * 276480 + 8192;
constexpr int O_XB = O_G + ACT;
constexpr int O_X1 = O_XB + ACT;
constexpr int O_U = O_X1 + ACT;
constexpr int WS_END = O_U + 2048 * 1728 * 2;

constexpr int LDS_BYTES = 160 * 1024;
constexpr int L_MISC = 156 * 1024;
constexpr int L_CTL = 159 * 1024;
constexpr int L_TJ = 100 * 1024;
constexpr int L_XS = 116 * 1024;
constexpr int L_XB = 132 * 1024;
enum { C_SB = 4, C_GO = 8, C_LAND = 12, C_LGO = 16, C_LDONE = 20, C_SB0 = 24, C_SB1 = 28, C_PUB = 32 };
constexpr int NTHR = 768;
constexpr float EPS = 1e-5f;
constexpr int SC1 = 16;

DI u32 pk(float a, float b) { f32x2 v = {a, b}; return __builtin_bit_cast(u32, __builtin_convertvector(v, bf16x2)); }
DI float blo(u32 v) { return __builtin_bit_cast(float, v << 16); }
DI float bhi(u32 v) { return __builtin_bit_cast(float, v & 0xffff0000u); }
DI f32x4 mfma(v4u a, v4u b, f32x4 c) {
  return __builtin_amdgcn_mfma_f32_16x16x32_bf16(__builtin_bit_cast(bf16x8, a), __builtin_bit_cast(bf16x8, b), c, 0, 0, 0);
}
DI float dot2(u32 a, u32 b, float c) {
  return __builtin_amdgcn_fdot2_f32_bf16(__builtin_bit_cast(bf16x2, a), __builtin_bit_cast(bf16x2, b), c, false);
}
DI float dot8(v4u a, v4u b, float c) { return dot2(a.w, b.w, dot2(a.z, b.z, dot2(a.y, b.y, dot2(a.x, b.x, c)))); }
DI v2u trread(LAS char* p) { return __builtin_bit_cast(v2u, __builtin_amdgcn_ds_read_tr16_b64_v4i16((LAS s16x4*)p)); }
DI float cosr(float rev) { return __builtin_amdgcn_cosf(rev); }
DI float sinr(float rev) { return __builtin_amdgcn_sinf(rev); }
DI float gelu(float x) {
  const float k0 = -2.302208198f, k1 = -0.1029432397f;
  float e = __builtin_amdgcn_exp2f(x * (k0 + k1 * x * x));
  return x * __builtin_amdgcn_rcpf(1.f + e);
}
DI void dma16(const void* gsrc, u32 lds_dst) {
  u32 keep;
  asm volatile("s_mov_b32 %0, m0\n\ts_mov_b32 m0, %2\n\ts_nop 0\n\tglobal_load_lds_dwordx4 %1, off\n\ts_mov_b32 m0, %0"
               : "=&s"(keep) : "v"(gsrc), "s"(lds_dst) : "memory");
}
DI void dma16_sc1(const void* gsrc, u32 lds_dst) {
  u32 keep;
  asm volatile("s_mov_b32 %0, m0\n\ts_mov_b32 m0, %2\n\ts_nop 0\n\tglobal_load_lds_dwordx4 %1, off sc1\n\ts_mov_b32 m0, %0"
               : "=&s"(keep) : "v"(gsrc), "s"(lds_dst) : "memory");
}
#define VMWAIT(n) asm volatile("s_waitcnt vmcnt(" #n ")" ::: "memory")
#define BAR() asm volatile("s_waitcnt lgkmcnt(0)\n\ts_barrier" ::: "memory")
#define SCHED() __builtin_amdgcn_sched_barrier(0)
DI v4u ld16(rsrc_t r, int voff, int soff) { return __builtin_amdgcn_raw_buffer_load_b128(r, voff, soff, SC1); }
DI v2u ld8(rsrc_t r, int voff, int soff) { return __builtin_amdgcn_raw_buffer_load_b64(r, voff, soff, SC1); }
DI u32 ld4(rsrc_t r, int voff, int soff) { return __builtin_amdgcn_raw_buffer_load_b32(r, voff, soff, SC1); }
DI void st16(rsrc_t r, v4u v, int voff, int soff) { __builtin_amdgcn_raw_buffer_store_b128(v, r, voff + soff, 0, SC1); }
DI void st8(rsrc_t r, v2u v, int voff, int soff) { __builtin_amdgcn_raw_buffer_store_b64(v, r, voff, soff, SC1); }
DI void st4(rsrc_t r, u32 v, int voff, int soff) { __builtin_amdgcn_raw_buffer_store_b32(v, r, voff, soff, SC1); }
DI float xsum16_32(float v) {
  float u = v;
  asm volatile("s_nop 1\n\tv_permlane16_swap_b32 %0, %1" : "+v"(u), "+v"(v));
  v += u; u = v;
  asm volatile("s_nop 1\n\tv_permlane32_swap_b32 %0, %1" : "+v"(u), "+v"(v));
  return v + u;
}
template <int CTRL> DI float dppadd(float v) {
  return v + __builtin_bit_cast(float, __builtin_amdgcn_mov_dpp(__builtin_bit_cast(int, v), CTRL, 0xf, 0xf, true));
}
DI float xsum8(float v) { return dppadd<0x141>(dppadd<0x4E>(dppadd<0xB1>(v))); }
#define PIN(v) asm volatile("" : "+v"(v))

#define STAMP(i)
#define STAMPW(i)
#define STAMPL(i)
struct Ctx {
  char* ws; rsrc_t rs; LAS char* lds; u64 epoch;
  int tid, lane, w, g, c;
  DI u64 tag() const { u64 e = epoch; asm volatile("" : "+v"(e)); return e + 0x9E3779B97F4A7C15ull; }
};
DI void refresh(Ctx& x) {
  int l = x.lane;
  asm volatile("" : "+v"(l));
  x.lane = l; x.g = l >> 4; x.c = l & 15;
}
DI LAS char* pinp(LAS char* p) { asm volatile("" : "+v"(p)); return p; }
DI u32 lds_ld(LAS char* p) { return __hip_atomic_load((LAS u32*)p, __ATOMIC_RELAXED, __HIP_MEMORY_SCOPE_WORKGROUP); }
#define LSLP 1
#define PSLP 1
DI void lds_wait_ge(LAS char* p, u32 want) {
#pragma unroll 1
  for (int spin = 0; spin < (1 << 20); ++spin) {
    if (__builtin_amdgcn_readfirstlane(lds_ld(p)) >= want) break;
    __builtin_amdgcn_s_sleep(LSLP);
  }
  asm volatile("" ::: "memory");
}
DI void lds_inc(const Ctx& x, int word) {
  if (x.lane == 0) __hip_atomic_fetch_add((LAS u32*)(x.lds + L_CTL + word), 1u, __ATOMIC_RELAXED, __HIP_MEMORY_SCOPE_WORKGROUP);
}
template <int REP = 1, int STR = 0, int NW = 8> DI bool publish(const Ctx& x, int idx, int flag, int stamp) {
  __builtin_amdgcn_s_waitcnt(0x0070);
  asm volatile("s_waitcnt vmcnt(0) lgkmcnt(0)" ::: "memory");
  u32 old = 0;
  if (x.lane == 0) old = __hip_atomic_fetch_add((LAS u32*)(x.lds + L_CTL + C_PUB + 4 * idx), 1u, __ATOMIC_RELAXED, __HIP_MEMORY_SCOPE_WORKGROUP);
  const bool last = __builtin_amdgcn_readfirstlane(old) == NW - 1;
  if (last) {
    if (x.lane < 8 * REP)
      __hip_atomic_store((GAS u64*)(x.ws + flag_at(flag + (x.lane >> 3) * STR) + (x.lane & 7) * 8), x.tag(), __ATOMIC_RELAXED, __HIP_MEMORY_SCOPE_AGENT);
    STAMPW(stamp);
  }
  asm volatile("" ::: "memory");
  return last;
}
DI void soft_barrier(const Ctx& x, int word, u32 target) {
  asm volatile("s_waitcnt lgkmcnt(0)" ::: "memory");
  lds_inc(x, word);
#pragma unroll 1
  for (int spin = 0; spin < (1 << 20); ++spin)
    if (__builtin_amdgcn_readfirstlane(lds_ld(x.lds + L_CTL + word)) >= target) break;
  asm volatile("" ::: "memory");
}
template <int N, int SLP = PSLP> DI void poll(const Ctx& x, const int (&off)[N]) {
  const u64 tag = x.tag();
#pragma unroll 1
  for (int spin = 0; spin < (1 << 16); ++spin) {
    u64 v[N];
#pragma unroll
    for (int i = 0; i < N; ++i) v[i] = __hip_atomic_load((GAS u64*)(x.ws + flag_at(off[i])), __ATOMIC_RELAXED, __HIP_MEMORY_SCOPE_AGENT);
    bool ok = true;
#pragma unroll
    for (int i = 0; i < N; ++i) ok &= v[i] == tag;
    if (__all(ok)) break;
    __builtin_amdgcn_s_sleep(SLP);
  }
  __builtin_amdgcn_fence(__ATOMIC_ACQUIRE, "wavefront");
  asm volatile("" ::: "memory");
}
DI void lds_st(LAS char* p, u32 v) { __hip_atomic_store((LAS u32*)p, v, __ATOMIC_RELAXED, __HIP_MEMORY_SCOPE_WORKGROUP); }
template <int N, int DELAY> DI void wait_flags(const Ctx& x, bool poller, const int (&off)[N], u32 gen) {
  if (poller) {
    if (DELAY) __builtin_amdgcn_s_sleep(DELAY);
    poll<N>(x, off);
    lds_st(x.lds + L_CTL + C_GO, gen);
  } else lds_wait_ge(x.lds + L_CTL + C_GO, gen);
}
#define LANDED(n) do { VMWAIT(n); lds_inc(x, C_LAND); } while (0)
DI void wait_landed(const Ctx& x, u32 n) { lds_wait_ge(x.lds + L_CTL + C_LAND, 4 * n); }

template <int GP> DI void group_dst(int gam, int h, int& tile, int& nb, bool& single) {
  int sup = gam / (2 * GP + 1), e = gam - sup * (2 * GP + 1);
  single = (e == GP);
  tile = 2 * sup + (e > GP ? 1 : (single ? h : 0));
  nb = single ? 2 * GP : 2 * (e > GP ? e - GP - 1 : e) + h;
}

DI v4u pack8(const float (&v)[8]) { return v4u{pk(v[0], v[1]), pk(v[2], v[3]), pk(v[4], v[5]), pk(v[6], v[7])}; }
DI v4u pack8(f32x4 a, f32x4 b) { return v4u{pk(a[0], a[1]), pk(a[2], a[3]), pk(b[0], b[1]), pk(b[2], b[3])}; }
DI void gen_q(int kap, int g, v4u (&qc)[10], v4u (&qs)[10]) {
  const int k = kap ? kap : 320;
  const float ac = kap ? 0.05590169944f : 0.03952847075f, as = kap ? -0.05590169944f : 0.f;
  const float kf = (float)k * (1.f / 640.f), a0 = (float)(8 * g) * kf;
  float c0[8], c1[8], s0[8], s1[8];
#pragma unroll
  for (int j = 0; j < 8; ++j) {
    const float u = a0 + (float)j * kf, v = a0 + (float)(32 + j) * kf;
    c0[j] = ac * cosr(u); s0[j] = as * sinr(u); c1[j] = ac * cosr(v); s1[j] = as * sinr(v);
  }
  const float c2 = 2.f * cosr(32.f * kf);
#pragma unroll
  for (int ks = 0; ks < 10; ks += 2) {
    qc[ks] = pack8(c0); qs[ks] = pack8(s0); qc[ks + 1] = pack8(c1); qs[ks + 1] = pack8(s1);
    if (ks + 2 < 10) {
#pragma unroll
      for (int j = 0; j < 8; ++j) {
        c0[j] = c2 * c1[j] - c0[j]; c1[j] = c2 * c0[j] - c1[j];
        s0[j] = c2 * s1[j] - s0[j]; s1[j] = c2 * s0[j] - s1[j];
      }
    }
  }
#pragma unroll
  for (int i = 0; i < 10; ++i) { PIN(qc[i]); PIN(qs[i]); }
}

DI void phase_a(const Ctx& x, const u16* xin, int mm, int q4, int nt) {
  const int w = x.w, lane = x.lane, g = x.g, c = x.c, u = w >> 2;
  const int lam = 8 * w + (lane >> 3);
  const int srow = 256 * mm + 32 * (4 * ((lam >> 4) & 1) + (lam & 3)) + 8 * q4 + 4 * (lam >> 5) + ((lam >> 2) & 3);
  {
    const char* src = (const char*)xin + srow * 1280 + (((lane & 7) ^ (lam & 7)) << 4);
#pragma unroll
    for (int kc = 0; kc < 10; ++kc) dma16(src + kc * 128, (u32)(uintptr_t)x.lds + (kc * 8 + w) * 1024);
  }
  const int kap = 64 * nt + 16 * (w & 3) + c;
  v4u qc[10], qs[10];
  gen_q(kap, g, qc, qs);
  f32x4 acc[2][2][2];
#pragma unroll
  for (int i = 0; i < 8; ++i) acc[i >> 2][(i >> 1) & 1][i & 1] = f32x4{0.f, 0.f, 0.f, 0.f};
  LAS char* a0 = x.lds + u * 4096 + (c >> 3) * 1024 + (c & 7) * 128 + ((g ^ (c & 7)) << 4);
  LAS char* a1 = x.lds + u * 4096 + (c >> 3) * 1024 + (c & 7) * 128 + (((g + 4) ^ (c & 7)) << 4);
  v4u f[4];
  SCHED();
  STAMP(36);
  VMWAIT(7); BAR(); STAMP(1);
#pragma unroll
  for (int i = 0; i < 4; ++i) f[i] = *(LAS v4u*)((i < 2 ? a0 : a1) + (i & 1) * 2048);
#pragma unroll
  for (int kc = 0; kc < 10; ++kc) {
    if (kc == 2) { VMWAIT(4); asm volatile("s_barrier" ::: "memory"); }
    if (kc == 5) { VMWAIT(0); asm volatile("s_barrier" ::: "memory"); }
    if (kc == 4) { a0 = pinp(a0 + 40960); a1 = pinp(a1 + 40960); }
    SCHED();
#pragma unroll
    for (int i = 0; i < 4; ++i) {
      const int qi = (2 * kc + (i >> 1)) % 10;
      acc[kc / 5][i & 1][0] = mfma(f[i], qc[qi], acc[kc / 5][i & 1][0]);
      acc[kc / 5][i & 1][1] = mfma(f[i], qs[qi], acc[kc / 5][i & 1][1]);
      if (kc < 9) f[i] = *(LAS v4u*)((i < 2 ? a0 : a1) + ((kc + 1) % 5) * 8192 + (i & 1) * 2048);
      SCHED();
    }
  }
  STAMP(37);
  const float sg = (c & 1) ? -1.f : 1.f;
#pragma unroll
  for (int i = 0; i < 4; ++i) acc[0][i >> 1][i & 1] += sg * acc[1][i >> 1][i & 1];
  BAR();
  LAS char* rp = pinp(x.lds + L_MISC + 1024 + (32 * u + 4 * g) * 4);
  const f32x4 r0 = *(LAS f32x4*)rp, r1 = *(LAS f32x4*)(rp + 64);
  const int off = O_ZT + kap * 8192 + ((q4 >> 1) * 2 + (mm >> 2)) * 1024 + ((mm & 3) * 16 + 8 * (q4 & 1) + 4 * u + g) * 16;
  st16(x.rs, pack8(acc[0][0][0] * r0, acc[0][1][0] * r1), off, 0);
  st16(x.rs, pack8(acc[0][0][1] * r0, acc[0][1][1] * r1), off, 4096);
}

DI void phase_a_stats(const Ctx& x, int mm, int q4, int nt) {
  const int lw = x.w & 3;
  LAS char* own = x.lds + 2 * lw * 1024 + x.lane * 16;
  float s1[2] = {0.f, 0.f}, s2[2] = {0.f, 0.f};
  const v4u ones = {0x3f803f80u, 0x3f803f80u, 0x3f803f80u, 0x3f803f80u};
#pragma unroll
  for (int kc = 0; kc < 10; ++kc) {
    if (kc == 0 || kc == 3 || kc == 6) BAR();
    if (kc == 5) own = pinp(own + 40960);
#pragma unroll
    for (int i = 0; i < 2; ++i) {
      const v4u o = *(LAS v4u*)(own + (kc % 5) * 8192 + i * 1024);
      s1[i] = dot8(o, ones, s1[i]); s2[i] = dot8(o, o, s2[i]);
    }
    if (kc == 2 || kc == 5) { PIN(s1[0]); PIN(s1[1]); PIN(s2[0]); PIN(s2[1]); }
  }
  const bool odd = x.lane & 1;
  const float t10 = xsum8(s1[0]), t11 = xsum8(s1[1]), t20 = xsum8(s2[0]), t21 = xsum8(s2[1]);
  const float mu = (odd ? t11 : t10) * (1.f / 640.f);
  const float var = fmaxf((odd ? t21 : t20) * (1.f / 640.f) - mu * mu, 0.f) + EPS;
  const float rstd = __builtin_amdgcn_rsqf(var);
  if ((x.lane & 7) < 2) {
    const int lam = 16 * lw + 8 * (x.lane & 1) + (x.lane >> 3), o = ((mm * 4 + q4) * 64 + lam) * 4;
    *(LAS float*)(x.lds + L_MISC + 1024 + lam * 4) = rstd;
    st4(x.rs, __builtin_bit_cast(u32, var * rstd), O_SIG + nt * 8192 + o, 0);
    if (nt == 0) st4(x.rs, __builtin_bit_cast(u32, mu), O_MU + o, 0);
  }
  BAR();
}
DI int stat_at(int n1, int n2) {
  return ((n1 >> 3) * 4 + (n2 >> 3)) * 64 + (2 * ((n2 >> 2) & 1) + ((n1 >> 2) & 1)) * 16 + 4 * (n2 & 3) + (n1 & 3);
}

DI void side_wt(const Ctx& x, const u16* wr, const u16* wi, int v) {
  const int sg = v / 5, cb = v - sg * 5;
  const int rho = (sg * 4 + (x.w & 3)) * 8 + (x.lane & 7), k0 = cb * 64 + (x.lane >> 3) * 8;
  const int f0 = 16 * (rho >> 7) + (rho & 15) + 64 * (16 * ((rho >> 6) & 1) + 4 * ((rho >> 4) & 3));
  typedef u32 v4ua __attribute__((ext_vector_type(4), aligned(4)));
  v4u a[4], b[4];
  u32 ea[4], eb[4], na[4], nb[4];
  const u32 sh = (f0 & 1) * 16;
#pragma unroll
  for (int r = 0; r < 4; ++r) {
    const int e = (f0 + 64 * r) * 321, d = (e + k0) >> 1;
    a[r] = *(const v4ua*)((const u32*)wr + d); ea[r] = ((const u32*)wr)[d + 4];
    b[r] = *(const v4ua*)((const u32*)wi + d); eb[r] = ((const u32*)wi)[d + 4];
    na[r] = wr[e + 320]; nb[r] = wi[e + 320];
  }
  SCHED();
  BAR();
  SCHED();
#pragma unroll
  for (int r = 0; r < 4; ++r) { PIN(na[r]); PIN(nb[r]); }
#pragma unroll
  for (int r = 0; r < 4; ++r)
#pragma unroll
    for (int j = 0; j < 4; ++j) {
      a[r][j] = __builtin_amdgcn_alignbit(j < 3 ? a[r][(j + 1) & 3] : ea[r], a[r][j], sh);
      b[r][j] = __builtin_amdgcn_alignbit(j < 3 ? b[r][(j + 1) & 3] : eb[r], b[r][j], sh);
    }
  if (k0 == 0) {
#pragma unroll
    for (int r = 0; r < 4; ++r) { a[r].x = (a[r].x & 0xffff0000u) | na[r]; b[r].x = (b[r].x & 0xffff0000u) | nb[r]; }
  }
#pragma unroll
  for (int j = 0; j < 8; ++j) {
    v4u o;
#pragma unroll
    for (int r = 0; r < 4; ++r) o[r] = __builtin_amdgcn_perm(b[r][j >> 1], a[r][j >> 1], (j & 1) ? 0x07060302u : 0x05040100u);
    st16(x.rs, o, O_WT + k0 * 8192 + rho * 16, j * 8192);
  }
}
template <int GP> DI int blk_addr(const Ctx& x, int gam0, int rb, int& tile, int& nb) {
  const int p = x.c & 3, r0 = 8 * x.g + (x.c >> 2);
  bool single;
  group_dst<GP>(gam0 + (rb >> 1), rb & 1, tile, nb, single);
  const int ch = (rb >> 1) * 4 + (single ? 2 * (rb & 1) + (p >> 1) : p), hb = single ? (p & 1) : (rb & 1);
  return r0 * 128 + ((ch ^ (((r0 >> 1) & 1) << 2)) << 4) + hb * 8;
}
DI void tile_dma(const Ctx& x, int j, int lds_off, const u16* w2, const u16* w3, const u16* w4) {
  const int row = x.lane >> 3, ch = ((x.lane & 7) ^ (((row >> 1) & 1) << 2)) << 4;
  const bool is3 = j < 540;
  const int jj = is3 ? j : j < 740 ? j - 540 : j - 740, ks = is3 ? jj / 27 : jj / 10, ct = jj - ks * (is3 ? 27 : 10), rs = is3 ? 3456 : 1280;
  const char* src = (const char*)(is3 ? w3 : j < 740 ? w2 : w4) + (ks * 32 + row) * rs + ct * 128 + ch;
#pragma unroll
  for (int i = 0; i < 4; ++i) dma16(src + i * 8 * rs, (u32)(uintptr_t)x.lds + lds_off + i * 1024);
}
template <int GP> DI void tile_out2(const Ctx& x, int lds_off, int ks, int ct, int dst, int tile_bytes) {
  const v4u ones = {0x3f803f80u, 0x3f803f80u, 0x3f803f80u, 0x3f803f80u};
  v4u f[4];
  int tile[4], nb[4];
#pragma unroll
  for (int rb = 0; rb < 4; ++rb) {
    LAS char* ad = x.lds + lds_off + blk_addr<GP>(x, 2 * ct, rb, tile[rb], nb[rb]);
    const v2u lo = trread(ad), hi = trread(ad + 512);
    f[rb] = v4u{lo.x, lo.y, hi.x, hi.y};
  }
#pragma unroll
  for (int rb = 0; rb < 4; ++rb) {
    st16(x.rs, f[rb], x.lane * 16, dst + tile[rb] * tile_bytes + (ks * (2 * GP + 1) + nb[rb]) * 1024);
    if (GP == 3) {
      const f32x4 s = mfma(f[rb], ones, f32x4{0.f, 0.f, 0.f, 0.f});
      if (x.c == 0) st16(x.rs, __builtin_bit_cast(v4u, s), x.g * 16, O_CS3 + (ks * 1792 + tile[rb] * 112 + nb[rb] * 16) * 4);
    }
  }
}
DI void tile_out(const Ctx& x, int j, int lds_off) {
  if (j < 540) tile_out2<3>(x, lds_off, j / 27, j % 27, O_B6, 143360);
  else {
    const int jj = j < 740 ? j - 540 : j - 740;
    tile_out2<2>(x, lds_off, jj / 10, jj % 10, j < 740 ? O_B5 : O_B7, j < 740 ? 102400 : 276480);
  }
}
DI void side_qc(const Ctx& x, int job, int njobs) {
#pragma unroll 1
  for (int pc = job * 8 + x.w; pc < 800; pc += njobs * 8) {
    const int ks = pc / 40, blk = pc - ks * 40;
    int tile, nb; bool single;
    group_dst<2>(blk >> 1, blk & 1, tile, nb, single);
    const int n = (blk >> 1) * 32 + (single ? 16 * (blk & 1) + x.c : 8 * (x.c >> 2) + 4 * (blk & 1) + (x.c & 3));
    const float a0 = (float)((n * (16 * ks + 4 * x.g)) % 640) * (1.f / 640.f), da = (float)n * (1.f / 640.f);
    v4u f;
#pragma unroll
    for (int i = 0; i < 4; ++i) {
      const float a = a0 + (float)i * da;
      f[i] = pk(0.05590169944f * cosr(a), -0.05590169944f * sinr(a));
    }
    if (ks == 0 && x.g == 0) f[0] = pk((n & 1) ? -0.03952847075f : 0.03952847075f, 0.f);
    st16(x.rs, f, x.lane * 16, O_BC + (8 + tile) * 102400 + (ks * 5 + nb) * 1024);
  }
}
DI void side_w1p(const Ctx& x, const u16* w1, int ct, int nt) {
  {
    const int row = 8 * x.w + (x.lane >> 3);
    const char* src = (const char*)w1 + row * 1280 + ct * 128 + (((x.lane & 7) ^ (((row >> 1) & 1) << 2)) << 4);
#pragma unroll
    for (int i = 0; i < 10; ++i) dma16(src + i * 64 * 1280, (u32)(uintptr_t)x.lds + (i * 8 + x.w) * 1024);
  }
  BAR();
  v4u qc[10], qs[10];
  gen_q(64 * nt + 16 * (x.w & 3) + x.c, x.g, qc, qs);
  f32x4 acc[2][2][2];
#pragma unroll
  for (int i = 0; i < 8; ++i) acc[i >> 2][(i >> 1) & 1][i & 1] = f32x4{0.f, 0.f, 0.f, 0.f};
  int addr[2], tile[2], nb[2];
#pragma unroll
  for (int rb = 0; rb < 2; ++rb) addr[rb] = blk_addr<2>(x, 2 * ct, 2 * (x.w >> 2) + rb, tile[rb], nb[rb]);
  SCHED();
  VMWAIT(0);
  soft_barrier(x, C_SB, 8);
  STAMP(1);
  v2u lo[4], hi[4];
#pragma unroll
  for (int i = 0; i < 4; ++i) { lo[i] = trread(x.lds + addr[i & 1] + (i >> 1) * 4096); hi[i] = trread(x.lds + addr[i & 1] + (i >> 1) * 4096 + 512); }
#pragma unroll
  for (int kc = 0; kc < 10; ++kc) {
    if (kc == 4) { addr[0] += 40960; addr[1] += 40960; PIN(addr[0]); PIN(addr[1]); }
#pragma unroll
    for (int i = 0; i < 4; ++i) {
      const int qi = (2 * kc + (i >> 1)) % 10;
      const v4u f = {lo[i].x, lo[i].y, hi[i].x, hi[i].y};
      acc[kc / 5][i & 1][0] = mfma(qc[qi], f, acc[kc / 5][i & 1][0]);
      acc[kc / 5][i & 1][1] = mfma(qs[qi], f, acc[kc / 5][i & 1][1]);
      if (kc < 9) {
        LAS char* ad = x.lds + addr[i & 1] + ((kc + 1) % 5) * 8192 + (i >> 1) * 4096;
        lo[i] = trread(ad); hi[i] = trread(ad + 512);
      }
      SCHED();
    }
  }
  const f32x4 sg = {1.f, -1.f, 1.f, -1.f};
#pragma unroll
  for (int rb = 0; rb < 2; ++rb) {
    const f32x4 re = acc[0][rb][0] + sg * acc[1][rb][0], im = acc[0][rb][1] + sg * acc[1][rb][1];
    st16(x.rs, v4u{pk(re[0], im[0]), pk(re[1], im[1]), pk(re[2], im[2]), pk(re[3], im[3])}, x.lane * 16,
         O_BC + tile[rb] * 102400 + ((4 * nt + (x.w & 3)) * 5 + nb[rb]) * 1024);
  }
}
DI void side_jobs(const Ctx& x, int v, const u16* w1, const u16* w2, const u16* w3, const u16* w4) {
  if (v < 46) {
    const int j = 640 + (v * 8 + x.w) * 2;
    if (v < 40) { tile_dma(x, j, x.w * 8192, w2, w3, w4); tile_dma(x, j + 1, x.w * 8192 + 4096, w2, w3, w4); }
    BAR();
    side_qc(x, 45 - v, 46);
    VMWAIT(0);
    STAMP(1);
    if (v < 40) { tile_out(x, j, x.w * 8192); tile_out(x, j + 1, x.w * 8192 + 4096); }
  } else {
    side_w1p(x, w1, (v - 46) / 5, (v - 46) % 5);
  }
  STAMP(5);
  publish<8, 256>(x, 1, F_S + 160 + v, 3);
}

DI v4u negv(v4u a) { return a ^ 0x80008000u; }
DI void phase_b(const Ctx& x, int p) {
  const int w = x.w, lane = x.lane, g = x.g, c = x.c, wq = w & 3, col = w >> 2;
  v4u C64[2], S64[2], N64[2], C32[2], S32[2], N32[2];
  f32x4 tc[2], ts[2];
  {
    const float f1 = (float)(16 * wq + c);
#pragma unroll
    for (int ks = 0; ks < 2; ++ks) {
      float cv[8], sv[8];
#pragma unroll
      for (int j = 0; j < 8; ++j) { const float a = f1 * (float)(32 * ks + 8 * g + j) * (1.f / 64.f); cv[j] = cosr(a); sv[j] = sinr(a); }
      C64[ks] = v4u{pk(cv[0], cv[1]), pk(cv[2], cv[3]), pk(cv[4], cv[5]), pk(cv[6], cv[7])};
      S64[ks] = v4u{pk(sv[0], sv[1]), pk(sv[2], sv[3]), pk(sv[4], sv[5]), pk(sv[6], sv[7])};
      N64[ks] = negv(S64[ks]);
#pragma unroll
      for (int j = 0; j < 8; ++j) {
        const float a = (float)(16 * ks + c) * (float)(16 * (j >> 2) + 4 * g + (j & 3)) * (1.f / 32.f);
        cv[j] = cosr(a); sv[j] = sinr(a);
      }
      C32[ks] = v4u{pk(cv[0], cv[1]), pk(cv[2], cv[3]), pk(cv[4], cv[5]), pk(cv[6], cv[7])};
      S32[ks] = v4u{pk(sv[0], sv[1]), pk(sv[2], sv[3]), pk(sv[4], sv[5]), pk(sv[6], sv[7])};
      N32[ks] = negv(S32[ks]);
#pragma unroll
      for (int r = 0; r < 4; ++r) {
        const float a = f1 * (float)(16 * ks + 4 * g + r) * (1.f / 2048.f);
        tc[ks][r] = 0.02209708691f * cosr(a); ts[ks][r] = 0.02209708691f * sinr(a);
      }
    }
  }
  const int nt = p >> 5, kap = 2 * p + col;
#pragma unroll
  for (int i = 0; i < 2; ++i) { PIN(C64[i]); PIN(S64[i]); PIN(N64[i]); PIN(C32[i]); PIN(S32[i]); PIN(N32[i]); PIN(tc[i]); PIN(ts[i]); }
  STAMP(4);
  wait_flags<1, 0>(x, w == 0, {F_W + nt * 16 + c}, 1);
  v4u wv[2];
#pragma unroll
  for (int t = 0; t < 2; ++t) wv[t] = ld16(x.rs, O_WT + kap * 8192 + (wq * 2 + t) * 1024 + lane * 16, 0);
  STAMP(5);
  wait_flags<1, 0>(x, w == 0, {F_A + nt * 32 + (lane & 31)}, 2);
  STAMP(6);
  LAS char* xs = pinp(x.lds + L_XS + col * 8192 + lane * 16);
  {
    const v4u l0 = ld16(x.rs, O_ZT + p * 16384 + w * 2048 + lane * 16, 0), l1 = ld16(x.rs, O_ZT + p * 16384 + w * 2048 + 1024 + lane * 16, 0);
    *(LAS v4u*)(xs + wq * 2048) = l0;
    *(LAS v4u*)(xs + wq * 2048 + 1024) = l1;
  }
  const f32x4 sg = __builtin_bit_cast(f32x4, ld16(x.rs, O_SIG + nt * 8192 + stat_at(16 * wq + 4 * g, 16 * col + c) * 4, 0));
  soft_barrier(x, col ? C_SB1 : C_SB0, 4);
  STAMP(50);
  v4u xr[2][2], xi[2][2];
#pragma unroll
  for (int i = 0; i < 4; ++i) {
    xr[i >> 1][i & 1] = *(LAS v4u*)(xs + i * 1024);
    xi[i >> 1][i & 1] = *(LAS v4u*)(xs + 4096 + i * 1024);
  }
  SCHED();
  const f32x4 zero = {0.f, 0.f, 0.f, 0.f};
  f32x4 ar[2], ai[2], br[2], bi[2];
#pragma unroll
  for (int b = 0; b < 2; ++b) {
    ar[b] = mfma(xr[b][0], C64[0], zero); ai[b] = mfma(xi[b][0], C64[0], zero);
    ar[b] = mfma(xi[b][0], S64[0], ar[b]); ai[b] = mfma(xr[b][0], N64[0], ai[b]);
    ar[b] = mfma(xr[b][1], C64[1], ar[b]); ai[b] = mfma(xi[b][1], C64[1], ai[b]);
    ar[b] = mfma(xi[b][1], S64[1], ar[b]); ai[b] = mfma(xr[b][1], N64[1], ai[b]);
  }
#pragma unroll
  for (int b = 0; b < 2; ++b) { br[b] = ar[b] * tc[b] + ai[b] * ts[b]; bi[b] = ai[b] * tc[b] - ar[b] * ts[b]; }
  v4u pr = pack8(br[0], br[1]), pi = pack8(bi[0], bi[1]);
  STAMP(49);
#pragma unroll
  for (int t = 0; t < 2; ++t) {
    ar[t] = mfma(C32[t], pr, zero); ai[t] = mfma(C32[t], pi, zero);
    ar[t] = mfma(S32[t], pi, ar[t]); ai[t] = mfma(N32[t], pr, ai[t]);
  }
#pragma unroll
  for (int t = 0; t < 2; ++t)
#pragma unroll
    for (int r = 0; r < 4; ++r) {
      const float wre = blo(wv[t][r]), wim = bhi(wv[t][r]);
      br[t][r] = ar[t][r] * wre - ai[t][r] * wim;
      bi[t][r] = ar[t][r] * wim + ai[t][r] * wre;
    }
  pr = pack8(br[0], br[1]); pi = pack8(bi[0], bi[1]);
#pragma unroll
  for (int t = 0; t < 2; ++t) {
    ar[t] = mfma(C32[t], pr, zero); ai[t] = mfma(C32[t], pi, zero);
    ar[t] = mfma(N32[t], pi, ar[t]); ai[t] = mfma(S32[t], pr, ai[t]);
  }
  {
    LAS char* img = pinp(x.lds + L_XB + col * 9216 + (16 * wq + c) * 72 + g * 8);
#pragma unroll
    for (int t = 0; t < 2; ++t) {
      f32x4 ur = ar[t] * tc[t] - ai[t] * ts[t], ui = ai[t] * tc[t] + ar[t] * ts[t];
      *(LAS v2u*)(img + t * 32) = v2u{pk(ur[0], ur[1]), pk(ur[2], ur[3])};
      *(LAS v2u*)(img + 4608 + t * 32) = v2u{pk(ui[0], ui[1]), pk(ui[2], ui[3])};
    }
  }
  STAMP(38);
  soft_barrier(x, C_SB, 8);
  STAMP(39);
  LAS char* rd = pinp(x.lds + L_XB + (8 * g + (c >> 2)) * 72 + col * 32 + (c & 3) * 8);
#pragma unroll
  for (int cc = 0; cc < 2; ++cc) {
    ar[cc] = zero; ai[cc] = zero;
#pragma unroll
    for (int ks = 0; ks < 2; ++ks) {
      v2u r0 = trread(rd + cc * 9216 + ks * 2304), r1 = trread(rd + cc * 9216 + ks * 2304 + 288);
      v2u i0 = trread(rd + cc * 9216 + 4608 + ks * 2304), i1 = trread(rd + cc * 9216 + 4608 + ks * 2304 + 288);
      const v4u ur = {r0.x, r0.y, r1.x, r1.y}, ui = {i0.x, i0.y, i1.x, i1.y};
      ar[cc] = mfma(C64[ks], ur, ar[cc]); ai[cc] = mfma(C64[ks], ui, ai[cc]);
      ar[cc] = mfma(N64[ks], ui, ar[cc]); ai[cc] = mfma(S64[ks], ur, ai[cc]);
    }
  }
  v2u z[4];
  {
    LAS char* zp = pinp(x.lds + L_XS + (col * 2 + (wq >> 1)) * 1024 + ((2 * (wq & 1) + (g >> 1)) * 16 + c) * 16 + (g & 1) * 8);
#pragma unroll
    for (int i = 0; i < 4; ++i) z[i] = *(LAS v2u*)(zp + i * 4096);
  }
  if (p == 0) ai[0] = zero;
  u32 o[8];
#pragma unroll
  for (int r = 0; r < 4; ++r) {
    float zz[4];
#pragma unroll
    for (int i = 0; i < 4; ++i) zz[i] = (r & 1) ? bhi(z[i][r >> 1]) : blo(z[i][r >> 1]);
    o[2 * r] = pk(sg[r] * zz[0] + ar[0][r], sg[r] * zz[1] + ai[0][r]);
    o[2 * r + 1] = pk(sg[r] * zz[2] + ar[1][r], sg[r] * zz[3] + ai[1][r]);
  }
  const int ob = O_HB + p * 16384 + (((8 * wq + 2 * g) * 2 + col) * 16 + c) * 16;
  st16(x.rs, v4u{o[0], o[1], o[2], o[3]}, ob, 0);
  st16(x.rs, v4u{o[4], o[5], o[6], o[7]}, ob + 512, 0);
}

#define GATE 0
#define GATEW(i) do { if (GATE == 1) lds_wait_ge(x.lds + L_CTL + C_PUB + 4 * (i), 8); if (GATE == 2) lds_wait_ge(x.lds + L_CTL + C_GO, (i) + 2); } while (0)
#define TOUCH 0
#define DLY 6
#define DLYB 10
#define PACE 6
#define STR2(x) #x
#define STR(x) STR2(x)
DI void dma_lin(const Ctx& x, int ws_off, int lds_off, int p0, int p1) {
  const char* src = x.ws + ws_off + x.lane * 16;
#pragma unroll 1
  for (int pc = p0 + (x.w & 3); pc < p1; pc += 4) {
    dma16_sc1(src + pc * 1024, (u32)(uintptr_t)x.lds + lds_off + pc * 1024);
    asm volatile("s_waitcnt vmcnt(" STR(PACE) ")" ::: "memory");
  }
}
DI void l2_touch(const Ctx& x, int ws_off, int p0, int p1) {
  const char* src = x.ws + ws_off + x.lane * 16;
#pragma unroll 1
  for (int pc = p0 + (x.w & 3); pc < p1; pc += 4) {
    dma16_sc1(src + pc * 1024, (u32)(uintptr_t)x.lds + L_MISC + 2048);
    VMWAIT(4);
  }
  VMWAIT(0);
}
DI int ft_pair(int rowblk, int KS, int gam, int g, int c) { return (rowblk * KS + gam) * 1024 + (g * 16 + c) * 16; }
DI int ft_single(int rowblk, int KS, int gam, int h, int g, int c) {
  return (rowblk * KS + gam) * 1024 + ((2 * h + (g >> 1)) * 16 + c) * 16 + (g & 1) * 8;
}

constexpr int L_EX128 = 100 * 1024, L_EX64 = 92 * 1024;
template <int NB, bool HPLANES>
DI void gemm128(const Ctx& x, int aoff, u32 land, f32x4 (&acc)[2][NB], float& ssq) {
  constexpr int LA = 4, EXW = NB == 5 ? 6 : 7;
  const int kw = x.w >> 2;
  v4u a[10][2];
  auto lda = [&](int it) {
    if (HPLANES) { a[it][0] = ld16(x.rs, aoff, it * 262144); a[it][1] = ld16(x.rs, aoff, it * 262144 + 16384); }
    else { a[it][0] = ld16(x.rs, aoff, it * 2048); a[it][1] = ld16(x.rs, aoff, it * 2048 + 20480); }
  };
#pragma unroll
  for (int it = 0; it < LA; ++it) lda(it);
  SCHED();
  wait_landed(x, land);
  STAMP(NB == 5 ? 26 : 28);
#pragma unroll
  for (int i = 0; i < 2; ++i)
#pragma unroll
    for (int n = 0; n < NB; ++n) acc[i][n] = f32x4{0.f, 0.f, 0.f, 0.f};
  f32x4 gr0 = {0.f, 0.f, 0.f, 0.f}, gr1 = gr0;
  LAS char* bp = x.lds + kw * NB * 1024 + x.lane * 16;
  v4u f[NB];
#pragma unroll
  for (int n = 0; n < NB; ++n) f[n] = *(LAS v4u*)(bp + n * 1024);
#pragma unroll
  for (int it = 0; it < 10; ++it) {
    SCHED();
    if (it + LA < 10) lda(it + LA);
    v4u a0 = a[it][0], a1 = a[it][1];
    if (HPLANES) {
      a0 = v4u{a[it][0].x, a[it][0].y, a[it][1].x, a[it][1].y};
      a1 = v4u{a[it][0].z, a[it][0].w, a[it][1].z, a[it][1].w};
    }
    SCHED();
#pragma unroll
    for (int n = 0; n < NB; ++n) {
      acc[0][n] = mfma(f[n], a0, acc[0][n]);
      acc[1][n] = mfma(f[n], a1, acc[1][n]);
      if (n == 0 && (it & 3) == 3) bp = pinp(bp + 8 * NB * 1024);
      if (it < 9) f[n] = *(LAS v4u*)(bp + (((it + 1) & 3) * 2 * NB + n) * 1024);
      if (HPLANES && n == 0) { gr0 = mfma(a0, a0, gr0); gr1 = mfma(a1, a1, gr1); }
      SCHED();
    }
  }
  float s0 = 0.f, s1 = 0.f;
  if (HPLANES) {
    const int r = x.c & 3;
    const bool dg = (x.c >> 2) == x.g;
    s0 = dg ? (r == 0 ? gr0[0] : r == 1 ? gr0[1] : r == 2 ? gr0[2] : gr0[3]) : 0.f;
    s1 = dg ? (r == 0 ? gr1[0] : r == 1 ? gr1[1] : r == 2 ? gr1[2] : gr1[3]) : 0.f;
  }
  BAR();
  STAMP(NB == 5 ? 12 : 20);
  LAS char* mine = pinp(x.lds + L_EX128 + x.w * EXW * 1024 + x.lane * 16);
  LAS char* other = pinp(x.lds + L_EX128 + (x.w ^ 4) * EXW * 1024 + x.lane * 16);
  if (kw) {
#pragma unroll
    for (int n = 0; n < NB; ++n) { *(LAS f32x4*)(mine + n * 1024) = acc[0][n]; acc[0][n] = acc[1][n]; }
    if (HPLANES) { *(LAS float*)(mine + NB * 1024) = s0; s0 = s1; }
  } else {
#pragma unroll
    for (int n = 0; n < NB; ++n) *(LAS f32x4*)(mine + n * 1024) = acc[1][n];
    if (HPLANES) *(LAS float*)(mine + NB * 1024) = s1;
  }
  BAR();
  STAMP(NB == 5 ? 32 : 34);
  if (HPLANES) ssq = xsum16_32(s0 + *(LAS float*)(other + NB * 1024));
#pragma unroll
  for (int n = 0; n < NB; ++n) acc[0][n] += *(LAS f32x4*)(other + n * 1024);
}

template <int KS>
DI void gemm64(const Ctx& x, int a0off, int a1off, u32 land, f32x4 (&o)[3]) {
  constexpr int NIT = (KS + 3) / 4, LA = KS == 20 ? 5 : 4;
  const int kw = x.w >> 1;
  v4u a[NIT][2];
  constexpr int AS = KS == 20 ? 1024 : 4096, BS = KS == 20 ? 5120 : 20480;
  auto lda = [&](int it) { a[it][0] = ld16(x.rs, a0off, it * AS); a[it][1] = ld16(x.rs, a1off, it * AS); };
#pragma unroll
  for (int it = 0; it < LA; ++it) lda(it);
  SCHED();
  wait_landed(x, land);
  STAMP(KS == 20 ? 27 : 29);
  f32x4 acc[2][5];
#pragma unroll
  for (int i = 0; i < 2; ++i)
#pragma unroll
    for (int n = 0; n < 5; ++n) acc[i][n] = f32x4{0.f, 0.f, 0.f, 0.f};
  LAS char* bs = x.lds + kw * (KS == 20 ? 25600 : 5120) + x.lane * 16;
  LAS char* bq[3] = {bs + (kw >> 1) * 2048, bs + ((kw >> 1) ^ 1) * 2048, bs + 4096};
  v4u f[5];
#pragma unroll
  for (int n = 0; n < 5; ++n) f[n] = *(LAS v4u*)(bq[n >> 1] + (n & 1) * 1024);
#pragma unroll
  for (int it = 0; it < NIT; ++it) {
    SCHED();
    if (KS == 54 && (it == 3 || it == 6 || it == 10)) BAR();
    if (it + LA < NIT) lda(it + LA);
    const int st = (it + 1) % 7;
    if (KS == 54 && st % 3 == 0) {
#pragma unroll
      for (int n = 0; n < 3; ++n) bq[n] = pinp(bq[n] + (st ? 61440 : -122880));
    }
    SCHED();
    if (it * 4 + 3 < KS || kw < KS - it * 4) {
#pragma unroll
      for (int n = 0; n < 5; ++n) {
        acc[0][n] = mfma(f[n], a[it][0], acc[0][n]);
        acc[1][n] = mfma(f[n], a[it][1], acc[1][n]);
        if (it + 1 < NIT) f[n] = *(LAS v4u*)(bq[n >> 1] + (KS == 54 ? st % 3 : st) * BS + (n & 1) * 1024);
        SCHED();
      }
    }
  }
  BAR();
  STAMP(KS == 20 ? 16 : 24);
  LAS char* mine = pinp(x.lds + L_EX64 + x.w * 8192 + x.lane * 16 - 2048);
#pragma unroll
  for (int n = 2; n < 5; ++n) *(LAS f32x4*)(mine + n * 1024) = acc[0][n];
#pragma unroll
  for (int n = 0; n < 5; ++n) *(LAS f32x4*)(mine + (5 + n) * 1024) = acc[1][n];
  BAR();
  STAMP(KS == 20 ? 33 : 35);
  o[0] = acc[0][0]; o[1] = acc[0][1]; o[2] = acc[0][4];
#pragma unroll
  for (int m = 1; m < 4; ++m) {
    LAS char* s = pinp(x.lds + L_EX64 + (x.w ^ (2 * m)) * 8192 + x.lane * 16 - 2048) + (m & 1) * 5120;
    o[0] += *(LAS f32x4*)(s + (m >> 1) * 2048);
    o[1] += *(LAS f32x4*)(s + (m >> 1) * 2048 + 1024);
    o[2] += *(LAS f32x4*)(s + 4096);
  }
}

DI f32x4 gelu4(f32x4 v) { return f32x4{gelu(v[0]), gelu(v[1]), gelu(v[2]), gelu(v[3])}; }
DI v2u pack4(f32x4 a) { return v2u{pk(a[0], a[1]), pk(a[2], a[3])}; }
DI f32x4 unlo(v4u v) { return f32x4{blo(v.x), bhi(v.x), blo(v.y), bhi(v.y)}; }
DI f32x4 unhi(v4u v) { return f32x4{blo(v.z), bhi(v.z), blo(v.w), bhi(v.w)}; }
DI f32x4 un4(v2u v) { return f32x4{blo(v.x), bhi(v.x), blo(v.y), bhi(v.y)}; }

DI void phase_c(const Ctx& x, int P, int t) {
  const int mw = x.w & 3, kw = x.w >> 2, g = x.g, c = x.c;
  const int np = 2 * P + (mw >> 1), b = mw & 1;
  const int rowblk = 4 * np + b + 2 * kw;
  const float mu = __builtin_bit_cast(float, ld4(x.rs, O_MU + stat_at(rowblk >> 1, 16 * (rowblk & 1) + c) * 4, 0));
  f32x4 acc[2][5];
  float ssq;
  gemm128<5, true>(x, O_HB + (8 * kw + 2 * g) * 16384 + ((np * 2 + b) * 16 + c) * 16, 1, acc, ssq);
  const float vh = ssq * (1.f / 640.f);
  float sc = __builtin_amdgcn_rsqf(vh + EPS);
  if (g == 0) *(LAS f32x2*)(x.lds + L_MISC + 1024 + ((rowblk & 7) * 16 + c) * 8) = f32x2{(vh + EPS) * sc, mu};
  const int t8 = t & 7, g0 = (t8 >> 1) * 5 + (t8 & 1) * 3, gs = (t8 >> 1) * 5 + 2;
  if (t < 8) {
    sc *= __builtin_amdgcn_rsqf(vh * sc * sc + EPS);
#pragma unroll
    for (int n = 0; n < 5; ++n) acc[0][n] = gelu4(acc[0][n] * sc);
  } else {
#pragma unroll
    for (int n = 0; n < 5; ++n) acc[0][n] *= sc;
  }
  const int dst = t < 8 ? O_G : O_XB;
#pragma unroll
  for (int i = 0; i < 2; ++i) st16(x.rs, pack8(acc[0][2 * i], acc[0][2 * i + 1]), dst + ft_pair(rowblk, 20, g0 + i, g, c), 0);
  st8(x.rs, pack4(acc[0][4]), dst + ft_single(rowblk, 20, gs, t8 & 1, g, c), 0);
}

DI void phase_5(const Ctx& x, int P, int hf, int t8, u32 (&res)[6]) {
  const int mw = x.w & 1, kw = x.w >> 1, g = x.g, c = x.c;
  const int rb0 = 8 * P + 4 * hf + 2 * mw, rowblk = rb0 + (kw & 1);
  const int gp = (t8 >> 1) * 5 + (t8 & 1) * 3 + (kw >> 1), gs = (t8 >> 1) * 5 + 2;
  const int op = ft_pair(rowblk, 20, gp, g, c), os = ft_single(rowblk, 20, gs, t8 & 1, g, c);
  const int sh = (kw & 1 ? -1 : 1) * (1 + (kw >> 1));
  int ao[2];
#pragma unroll
  for (int i = 0; i < 2; ++i) {
    const int r = (rb0 + ((kw & 1) ^ i)) * 16 + c - sh;
    ao[i] = (u32)r < 2048u ? O_G + ft_pair(r >> 4, 20, 5 * kw, g, r & 15) : 0x7f000000;
  }
  const v4u xp = ld16(x.rs, O_XB + op, 0);
  const v2u xs = ld8(x.rs, O_XB + os, 0);
  f32x4 o[3];
  gemm64<20>(x, ao[0], ao[1], 2, o);
  const f32x2 st = *(LAS f32x2*)(x.lds + L_MISC + 1024 + ((rowblk & 7) * 16 + c) * 8);
  const f32x4 b0 = unlo(xp), b1 = unhi(xp), b2 = un4(xs);
  o[0] += b0; o[1] += b1; o[2] += b2;
  const v4u r = pack8(o[0], o[1]);
  st16(x.rs, r, O_X1 + op, 0);
  const v4u ones = {0x3f803f80u, 0x3f803f80u, 0x3f803f80u, 0x3f803f80u};
  float s1 = dot8(r, ones, 0.f), s2 = dot8(r, r, 0.f);
  if (kw < 2) {
    const v2u q = pack4(o[2]);
    st8(x.rs, q, O_X1 + os, 0);
    s1 = dot2(q.y, ones.x, dot2(q.x, ones.x, s1));
    s2 = dot2(q.y, q.y, dot2(q.x, q.x, s2));
  }
  s1 = xsum16_32(s1); s2 = xsum16_32(s2);
  if (g == 0)
    st8(x.rs, v2u{__builtin_bit_cast(u32, s1), __builtin_bit_cast(u32, s2)}, O_ST1 + (rowblk * 16 + t8 * 2 + (kw >> 1)) * 128 + c * 8, 0);
  const v4u rp = pack8(o[0] + b0 * st[0] + st[1], o[1] + b1 * st[0] + st[1]);
  const v2u rq = pack4(o[2] + b2 * st[0] + st[1]);
  res[0] = rp.x; res[1] = rp.y; res[2] = rp.z; res[3] = rp.w; res[4] = rq.x; res[5] = rq.y;
#pragma unroll
  for (int i = 0; i < 6; ++i) PIN(res[i]);
}

DI void phase_6(const Ctx& x, int P, int t) {
  const int mw = x.w & 3, kw = x.w >> 2, g = x.g, c = x.c;
  const int rb0 = 8 * P + 2 * mw, rowblk = rb0 + kw;
  f32x2 p[4];
#pragma unroll
  for (int i = 0; i < 4; ++i) p[i] = __builtin_bit_cast(f32x2, ld8(x.rs, O_ST1 + rowblk * 2048 + x.lane * 8 + i * 512, 0));
  f32x4 acc[2][7];
  float dummy;
  gemm128<7, false>(x, O_X1 + (rb0 * 20 + kw) * 1024 + x.lane * 16, 3, acc, dummy);
  const f32x2 ps = (p[0] + p[1]) + (p[2] + p[3]);
  const float m1 = xsum16_32(ps[0]) * (1.f / 640.f), q1 = xsum16_32(ps[1]) * (1.f / 640.f);
  const float r1 = __builtin_amdgcn_rsqf(fmaxf(q1 - m1 * m1, 0.f) + EPS), nm = -m1 * r1;
  const int g0 = (t >> 1) * 7 + (t & 1) * 4, gs = (t >> 1) * 7 + 3;
  LAS f32x4* cs = (LAS f32x4*)pinp(x.lds + L_MISC + g * 16);
#pragma unroll
  for (int i = 0; i < 3; ++i) {
    const v4u r = pack8(gelu4(acc[0][2 * i] * r1 + cs[8 * i] * nm), gelu4(acc[0][2 * i + 1] * r1 + cs[8 * i + 4] * nm));
    if (g0 + i < 54) st16(x.rs, r, O_U + ft_pair(rowblk, 54, g0 + i, g, c), 0);
  }
  st8(x.rs, pack4(gelu4(acc[0][6] * r1 + cs[24] * nm)), O_U + ft_single(rowblk, 54, gs, t & 1, g, c), 0);
}
DI void colsum_6(const Ctx& x, int t) {
  if (x.lane < 56) {
    f32x2 s = {0.f, 0.f};
#pragma unroll
    for (int k = 0; k < 20; ++k) s += __builtin_bit_cast(f32x2, ld8(x.rs, O_CS3 + t * 448 + x.lane * 8, k * 7168));
    *(LAS f32x2*)(x.lds + L_MISC + x.lane * 8) = s;
  }
}

DI void phase_7(const Ctx& x, int P, int hf, int t8, const u32 (&res)[6], u16* out) {
  const int mw = x.w & 1, kw = x.w >> 1, g = x.g, c = x.c;
  const int rb0 = 8 * P + 4 * hf + 2 * mw, rowblk = rb0 + (kw & 1);
  const int gp = (t8 >> 1) * 5 + (t8 & 1) * 3 + (kw >> 1), gs = (t8 >> 1) * 5 + 2;
  f32x4 o[3];
  gemm64<54>(x, O_U + (rowblk * 54 + kw) * 1024 + x.lane * 16, O_U + ((rowblk ^ 1) * 54 + kw) * 1024 + x.lane * 16, 4, o);
  char* orow = (char*)out + (rowblk * 16 + c) * 1280;
  *(v4u*)(orow + gp * 64 + g * 16) = pack8(o[0] + un4(v2u{res[0], res[1]}), o[1] + un4(v2u{res[2], res[3]}));
  if (kw < 2) *(v2u*)(orow + gs * 64 + (t8 & 1) * 32 + g * 8) = pack4(o[2] + un4(v2u{res[4], res[5]}));
}

#define MAP 1
__global__ __launch_bounds__(NTHR, 3) void fused(const u16* __restrict__ xin, const u16* __restrict__ wr, const u16* __restrict__ wi,
                                                 const u16* __restrict__ w1, const u16* __restrict__ w2, const u16* __restrict__ w3,
                                                 const u16* __restrict__ w4, u16* __restrict__ out, char* ws) {
  extern __shared__ __attribute__((aligned(16))) char smem[];
  Ctx x;
  x.ws = ws; x.rs = __builtin_amdgcn_make_buffer_rsrc(ws, 0, WS_END, 0x00020000); x.lds = (LAS char*)smem;
  x.tid = threadIdx.x; x.lane = x.tid & 63; x.w = __builtin_amdgcn_readfirstlane(x.tid >> 6); x.g = x.lane >> 4; x.c = x.lane & 15;
  GAS u64* ep = (GAS u64*)(ws + O_EPOCH);
  x.epoch = __hip_atomic_load(ep, __ATOMIC_RELAXED, __HIP_MEMORY_SCOPE_AGENT);
  const int b = blockIdx.x, xcd = b & 7, i = b >> 3;
  const int P = 4 * (xcd >> 1) + (i >> 3), t = 8 * (xcd & 1) + (i & 7), hf = (i >> 2) & 1, t8 = 4 * (xcd & 1) + (i & 3);
  STAMP(0);
  if (x.w >= 8) {
    if (x.w == 8 && x.lane < 16) lds_st(x.lds + L_CTL + x.lane * 4, 0);
    if (b < 160) {
      const int lw = x.w & 3;
      tile_dma(x, b * 4 + lw, L_TJ + lw * 4096, w2, w3, w4);
      phase_a_stats(x, xcd, i & 3, i >> 2);
      publish<1, 0, 12>(x, 0, F_A + (i >> 2) * 32 + xcd * 4 + (i & 3), 3);
      STAMPL(46);
      tile_out(x, b * 4 + lw, L_TJ + lw * 4096);
      publish<8, 256, 4>(x, 5, F_S + b, 47);
    } else {
      const int v = b - 160;
      if (v < 80) {
        side_wt(x, wr, wi, v);
        STAMPL(46);
        publish<1, 0, 4>(x, 0, F_W + (v % 5) * 16 + v / 5, 2);
      } else { VMWAIT(0); BAR(); }
      lds_wait_ge(x.lds + L_CTL + C_PUB + 4, 8);
    }
    if (x.w == 8) {
      const int fo = F_S + xcd * 256 + x.lane;
      poll<4, 16>(x, {fo, fo + 64, fo + 128, fo + 192});
      lds_st(x.lds + L_CTL + C_LGO, 1);
      STAMPW(9);
    } else lds_wait_ge(x.lds + L_CTL + C_LGO, 1);
    dma_lin(x, O_BC + t * 102400, 0, 0, 100);
    LANDED(0);
    STAMPL(40);
    if (x.w == 8) colsum_6(x, t);
    STAMPL(44);
    BAR();
    dma_lin(x, O_B5 + t8 * 102400, 0, 0, 4);
    BAR();
    GATEW(2);
    dma_lin(x, O_B5 + t8 * 102400, 0, 4, 100);
    STAMPL(45);
    LANDED(0);
    STAMPL(41);
    BAR();
    dma_lin(x, O_B6 + t * 143360, 0, 0, 4);
    BAR();
    GATEW(3);
    dma_lin(x, O_B6 + t * 143360, 0, 4, 92);
    lds_wait_ge(x.lds + L_CTL + C_PUB + 12, 8);
    dma_lin(x, O_B6 + t * 143360, 0, 92, 140);
    LANDED(0);
    STAMPL(42);
    BAR();
    dma_lin(x, O_B7 + t8 * 276480, 0, 0, 4);
    BAR();
    GATEW(4);
    dma_lin(x, O_B7 + t8 * 276480, 0, 4, 100);
    lds_wait_ge(x.lds + L_CTL + C_PUB + 16, 8);
    dma_lin(x, O_B7 + t8 * 276480, 0, 100, 140);
    LANDED(0);
    STAMPL(43);
    BAR();
    dma_lin(x, O_B7 + t8 * 276480 + 140 * 1024, 0, 0, 80);
    VMWAIT(0);
    BAR();
    dma_lin(x, O_B7 + t8 * 276480 + 140 * 1024, 0, 80, 130);
    VMWAIT(0);
    BAR();
    return;
  }
  bool last;
  if (b < 160) {
    phase_a(x, xin, xcd, i & 3, i >> 2);
    STAMP(2);
    publish<1, 0, 12>(x, 0, F_A + (i >> 2) * 32 + xcd * 4 + (i & 3), 3);
    refresh(x);
    phase_b(x, b);
    STAMP(7);
    last = publish<8, 160>(x, 1, F_B + b, 8);
  } else {
    side_jobs(x, b - 160, w1, w2, w3, w4);
    last = x.w == 0;
    if (last) poll<1, 8>(x, {F_B + xcd * 160 + i});
  }
  wait_flags<3, DLYB>(x, last, {F_B + xcd * 160 + x.lane, F_B + xcd * 160 + 64 + x.lane, F_B + xcd * 160 + 128 + (x.lane & 31)}, 3);
  STAMP(11);
  refresh(x);
  phase_c(x, P, t);
  STAMP(13);
  last = publish(x, 2, F_C + P * 16 + t, 14);
  wait_flags<1, DLY>(x, last, {F_C + (x.lane < 32 ? P * 16 + x.c : ((P + (hf ? 1 : 15)) & 15) * 16 + (x.lane & 7))}, 4);
  STAMP(15);
  refresh(x);
  u32 res[6];
  phase_5(x, P, hf, t8, res);
  STAMP(17);
  last = publish(x, 3, F_5 + P * 16 + t, 18);
  wait_flags<1, DLY>(x, last, {F_5 + P * 16 + x.c}, 5);
  STAMP(19);
  refresh(x);
  phase_6(x, P, t);
  STAMP(21);
  last = publish(x, 4, F_6 + P * 16 + t, 22);
  wait_flags<1, DLY>(x, last, {F_6 + P * 16 + x.c}, 6);
  STAMP(23);
  refresh(x);
  phase_7(x, P, hf, t8, res, out);
  STAMP(25);
  if (b == 0 && x.tid == 0) __hip_atomic_store(ep, x.tag(), __ATOMIC_RELAXED, __HIP_MEMORY_SCOPE_AGENT);
}

extern "C" void kernel_launch(void* const* d_in, const int* in_sizes, int n_in, void* d_out, int out_size, void* d_ws,
                              size_t ws_size, hipStream_t stream) {
  (void)hipFuncSetAttribute((const void*)fused, hipFuncAttributeMaxDynamicSharedMemorySize, LDS_BYTES);
  fused<<<256, NTHR, LDS_BYTES, stream>>>((const u16*)d_in[0], (const u16*)d_in[1], (const u16*)d_in[2], (const u16*)d_in[4],
                                          (const u16*)d_in[5], (const u16*)d_in[6], (const u16*)d_in[7], (u16*)d_out, (char*)d_ws);
}
```

```cpp
#include <hip/hip_runtime.h>
#include <stdint.h>

typedef unsigned u32;
typedef unsigned long long u64;
typedef unsigned short u16;
typedef u32 v4u __attribute__((ext_vector_type(4)));
typedef u32 v2u __attribute__((ext_vector_type(2)));
typedef float f32x4 __attribute__((ext_vector_type(4)));
typedef float f32x2 __attribute__((ext_vector_type(2)));
typedef __bf16 bf16x8 __attribute__((ext_vector_type(8)));
typedef __bf16 bf16x2 __attribute__((ext_vector_type(2)));
typedef short s16x4 __attribute__((ext_vector_type(4)));
typedef __amdgpu_buffer_rsrc_t rsrc_t;
#define LAS __attribute__((address_space(3)))
#define GAS __attribute__((address_space(1)))
#define DI __device__ __forceinline__

constexpr int ACT = 2048 * 640 * 2;
constexpr int O_EPOCH = 0;
constexpr int O_FLAG = 4096;
enum { F_A = 0, F_W = 160, F_C = 240, F_5 = 496, F_6 = 752, F_S = 1008, F_T = F_S + 8 * 96, F_B = F_T + 8 * 96, F_END = F_B + 8 * 160 };
__host__ __device__ constexpr int flag_at(int i) { return O_FLAG + i * 256; }
constexpr int O_SIG = 2 << 20;
static_assert(flag_at(F_END) <= O_SIG, "flag area");
constexpr int O_MU = O_SIG + 5 * 8192;
constexpr int O_CS3 = O_MU + 8192;
constexpr int O_ST1 = O_CS3 + 20 * 1792 * 4;
constexpr int O_ZT = O_ST1 + 2048 * 128;
constexpr int O_WT = O_ZT + ACT;
constexpr int O_HB = O_WT + ACT;
constexpr int O_BC = O_HB + ACT;
constexpr int O_B5 = O_BC + 16 * 102400 + 8192;
constexpr int O_B6 = O_B5 + 8 * 102400 + 8192;
constexpr int O_B7 = O_B6 + 16 * 143360 + 8192;
constexpr int O_G = O_B7 + 8 * 276480 + 8192;
constexpr int O_XB = O_G + ACT;
constexpr int O_X1 = O_XB + ACT;
constexpr int O_U = O_X1 + ACT;
constexpr int WS_END = O_U + 2048 * 1728 * 2;

constexpr int LDS_BYTES = 160 * 1024;
constexpr int L_MISC = 156 * 1024;
constexpr int L_CTL = 159 * 1024;
constexpr int L_TJ = 84 * 1024;
constexpr int L_XS = 116 * 1024;
constexpr int L_XB = 132 * 1024;
enum { C_SB = 4, C_GO = 8, C_LAND = 12, C_LGO = 16, C_GOW = 20, C_SB0 = 24, C_SB1 = 28, C_PUB = 32 };
constexpr int NTHR = 768;
constexpr float EPS = 1e-5f;
constexpr int SC1 = 16;

DI u32 pk(float a, float b) { f32x2 v = {a, b}; return __builtin_bit_cast(u32, __builtin_convertvector(v, bf16x2)); }
DI float blo(u32 v) { return __builtin_bit_cast(float, v << 16); }
DI float bhi(u32 v) { return __builtin_bit_cast(float, v & 0xffff0000u); }
DI f32x4 mfma(v4u a, v4u b, f32x4 c) {
  return __builtin_amdgcn_mfma_f32_16x16x32_bf16(__builtin_bit_cast(bf16x8, a), __builtin_bit_cast(bf16x8, b), c, 0, 0, 0);
}
DI float dot2(u32 a, u32 b, float c) {
  return __builtin_amdgcn_fdot2_f32_bf16(__builtin_bit_cast(bf16x2, a), __builtin_bit_cast(bf16x2, b), c, false);
}
DI float dot8(v4u a, v4u b, float c) { return dot2(a.w, b.w, dot2(a.z, b.z, dot2(a.y, b.y, dot2(a.x, b.x, c)))); }
DI v2u trread(LAS char* p) { return __builtin_bit_cast(v2u, __builtin_amdgcn_ds_read_tr16_b64_v4i16((LAS s16x4*)p)); }
DI float cosr(float rev) { return __builtin_amdgcn_cosf(rev); }
DI float sinr(float rev) { return __builtin_amdgcn_sinf(rev); }
DI float gelu(float x) {
  const float k0 = -2.302208198f, k1 = -0.1029432397f;
  float e = __builtin_amdgcn_exp2f(x * (k0 + k1 * x * x));
  return x * __builtin_amdgcn_rcpf(1.f + e);
}
DI void dma16(const void* gsrc, u32 lds_dst) {
  u32 keep;
  asm volatile("s_mov_b32 %0, m0\n\ts_mov_b32 m0, %2\n\ts_nop 0\n\tglobal_load_lds_dwordx4 %1, off nt\n\ts_mov_b32 m0, %0"
               : "=&s"(keep) : "v"(gsrc), "s"(lds_dst) : "memory");
}
DI void dma16_sc1(const void* gsrc, u32 lds_dst) {
  u32 keep;
  asm volatile("s_mov_b32 %0, m0\n\ts_mov_b32 m0, %2\n\ts_nop 0\n\tglobal_load_lds_dwordx4 %1, off sc1\n\ts_mov_b32 m0, %0"
               : "=&s"(keep) : "v"(gsrc), "s"(lds_dst) : "memory");
}
#define VMWAIT(n) asm volatile("s_waitcnt vmcnt(" #n ")" ::: "memory")
#define BAR() asm volatile("s_waitcnt lgkmcnt(0)\n\ts_barrier" ::: "memory")
#define SCHED() __builtin_amdgcn_sched_barrier(0)
DI v4u ld16(rsrc_t r, int voff, int soff) { return __builtin_amdgcn_raw_buffer_load_b128(r, voff, soff, SC1); }
DI v2u ld8(rsrc_t r, int voff, int soff) { return __builtin_amdgcn_raw_buffer_load_b64(r, voff, soff, SC1); }
DI u32 ld4(rsrc_t r, int voff, int soff) { return __builtin_amdgcn_raw_buffer_load_b32(r, voff, soff, SC1); }
DI void st16(rsrc_t r, v4u v, int voff, int soff) { __builtin_amdgcn_raw_buffer_store_b128(v, r, voff + soff, 0, SC1); }
DI void st8(rsrc_t r, v2u v, int voff, int soff) { __builtin_amdgcn_raw_buffer_store_b64(v, r, voff, soff, SC1); }
DI void st4(rsrc_t r, u32 v, int voff, int soff) { __builtin_amdgcn_raw_buffer_store_b32(v, r, voff, soff, SC1); }
DI float xsum16_32(float v) {
  float u = v;
  asm volatile("s_nop 1\n\tv_permlane16_swap_b32 %0, %1" : "+v"(u), "+v"(v));
  v += u; u = v;
  asm volatile("s_nop 1\n\tv_permlane32_swap_b32 %0, %1" : "+v"(u), "+v"(v));
  return v + u;
}
template <int CTRL> DI float dppadd(float v) {
  return v + __builtin_bit_cast(float, __builtin_amdgcn_mov_dpp(__builtin_bit_cast(int, v), CTRL, 0xf, 0xf, true));
}
DI float xsum8(float v) { return dppadd<0x141>(dppadd<0x4E>(dppadd<0xB1>(v))); }
#define PIN(v) asm volatile("" : "+v"(v))

#define STAMP(i)
#define STAMPA(i)
#define STAMPW(i)
#define STAMPL(i)
struct Ctx {
  char* ws; rsrc_t rs; LAS char* lds; u64 epoch;
  int tid, lane, w, g, c;
  DI u64 tag() const { u64 e = epoch; asm volatile("" : "+v"(e)); return e + 0x9E3779B97F4A7C15ull; }
};
DI void refresh(Ctx& x) {
  int l = x.lane;
  asm volatile("" : "+v"(l));
  x.lane = l; x.g = l >> 4; x.c = l & 15;
}
DI LAS char* pinp(LAS char* p) { asm volatile("" : "+v"(p)); return p; }
DI u32 lds_ld(LAS char* p) { return __hip_atomic_load((LAS u32*)p, __ATOMIC_RELAXED, __HIP_MEMORY_SCOPE_WORKGROUP); }
#define LSLP 1
#define PSLP 1
DI void lds_wait_ge(LAS char* p, u32 want) {
#pragma unroll 1
  for (int spin = 0; spin < (1 << 20); ++spin) {
    if (__builtin_amdgcn_readfirstlane(lds_ld(p)) >= want) break;
    __builtin_amdgcn_s_sleep(LSLP);
  }
  asm volatile("" ::: "memory");
}
DI void lds_inc(const Ctx& x, int word) {
  if (x.lane == 0) __hip_atomic_fetch_add((LAS u32*)(x.lds + L_CTL + word), 1u, __ATOMIC_RELAXED, __HIP_MEMORY_SCOPE_WORKGROUP);
}
template <int REP = 1, int STR = 0> DI bool publish(const Ctx& x, int idx, int flag, int stamp, int NW = 8) {
  __builtin_amdgcn_s_waitcnt(0x0070);
  asm volatile("s_waitcnt vmcnt(0) lgkmcnt(0)" ::: "memory");
  u32 old = 0;
  if (x.lane == 0) old = __hip_atomic_fetch_add((LAS u32*)(x.lds + L_CTL + C_PUB + 4 * idx), 1u, __ATOMIC_RELAXED, __HIP_MEMORY_SCOPE_WORKGROUP);
  const bool last = __builtin_amdgcn_readfirstlane(old) == NW - 1;
  if (last) {
    if (x.lane < 8 * REP)
      __hip_atomic_store((GAS u64*)(x.ws + flag_at(flag + (x.lane >> 3) * STR) + (x.lane & 7) * 8), x.tag(), __ATOMIC_RELAXED, __HIP_MEMORY_SCOPE_AGENT);
    STAMPW(stamp);
  }
  asm volatile("" ::: "memory");
  return last;
}
DI void soft_barrier(const Ctx& x, int word, u32 target) {
  asm volatile("s_waitcnt lgkmcnt(0)" ::: "memory");
  lds_inc(x, word);
#pragma unroll 1
  for (int spin = 0; spin < (1 << 20); ++spin)
    if (__builtin_amdgcn_readfirstlane(lds_ld(x.lds + L_CTL + word)) >= target) break;
  asm volatile("" ::: "memory");
}
template <int N, int SLP = PSLP> DI void poll(const Ctx& x, const int (&off)[N]) {
  const u64 tag = x.tag();
#pragma unroll 1
  for (int spin = 0; spin < (1 << 16); ++spin) {
    u64 v[N];
#pragma unroll
    for (int i = 0; i < N; ++i) v[i] = __hip_atomic_load((GAS u64*)(x.ws + flag_at(off[i])), __ATOMIC_RELAXED, __HIP_MEMORY_SCOPE_AGENT);
    bool ok = true;
#pragma unroll
    for (int i = 0; i < N; ++i) ok &= v[i] == tag;
    if (__all(ok)) break;
    __builtin_amdgcn_s_sleep(SLP);
  }
  __builtin_amdgcn_fence(__ATOMIC_ACQUIRE, "wavefront");
  asm volatile("" ::: "memory");
}
DI void lds_st(LAS char* p, u32 v) { __hip_atomic_store((LAS u32*)p, v, __ATOMIC_RELAXED, __HIP_MEMORY_SCOPE_WORKGROUP); }
template <int N, int DELAY> DI void wait_flags(const Ctx& x, bool poller, const int (&off)[N], u32 gen) {
  if (poller) {
    if (DELAY) __builtin_amdgcn_s_sleep(DELAY);
    poll<N>(x, off);
    lds_st(x.lds + L_CTL + C_GO, gen);
  } else lds_wait_ge(x.lds + L_CTL + C_GO, gen);
}
#define LANDED(n) do { VMWAIT(n); lds_inc(x, C_LAND); } while (0)
DI void wait_landed(const Ctx& x, u32 n) { lds_wait_ge(x.lds + L_CTL + C_LAND, 4 * n); }

template <int GP> DI void group_dst(int gam, int h, int& tile, int& nb, bool& single) {
  int sup = gam / (2 * GP + 1), e = gam - sup * (2 * GP + 1);
  single = (e == GP);
  tile = 2 * sup + (e > GP ? 1 : (single ? h : 0));
  nb = single ? 2 * GP : 2 * (e > GP ? e - GP - 1 : e) + h;
}

DI v4u pack8(const float (&v)[8]) { return v4u{pk(v[0], v[1]), pk(v[2], v[3]), pk(v[4], v[5]), pk(v[6], v[7])}; }
DI v4u pack8(f32x4 a, f32x4 b) { return v4u{pk(a[0], a[1]), pk(a[2], a[3]), pk(b[0], b[1]), pk(b[2], b[3])}; }
DI void gen_q(int kap, int g, int n0, v4u (&qc)[10], v4u (&qs)[10]) {
  const int k = kap ? kap : 320;
  const float ac = kap ? 0.05590169944f : 0.03952847075f, as = kap ? -0.05590169944f : 0.f;
  const float kf = (float)k * (1.f / 640.f), a0 = (float)(((n0 + 8 * g) * k) % 640) * (1.f / 640.f);
  float c0[8], c1[8], s0[8], s1[8];
#pragma unroll
  for (int j = 0; j < 8; ++j) {
    const float u = a0 + (float)j * kf, v = a0 + (float)(32 + j) * kf;
    c0[j] = ac * cosr(u); s0[j] = as * sinr(u); c1[j] = ac * cosr(v); s1[j] = as * sinr(v);
  }
  const float c2 = 2.f * cosr(32.f * kf);
#pragma unroll
  for (int ks = 0; ks < 10; ks += 2) {
    qc[ks] = pack8(c0); qs[ks] = pack8(s0); qc[ks + 1] = pack8(c1); qs[ks + 1] = pack8(s1);
    if (ks + 2 < 10) {
#pragma unroll
      for (int j = 0; j < 8; ++j) {
        c0[j] = c2 * c1[j] - c0[j]; c1[j] = c2 * c0[j] - c1[j];
        s0[j] = c2 * s1[j] - s0[j]; s1[j] = c2 * s0[j] - s1[j];
      }
    }
  }
#pragma unroll
  for (int i = 0; i < 10; ++i) { PIN(qc[i]); PIN(qs[i]); }
}

#define XF 4
DI void phase_a(const Ctx& x, const u16* xin, int mm, int q4, int nt) {
  const int w = x.w, lane = x.lane, g = x.g, c = x.c;
  const int lam = 8 * w + (lane >> 3);
  const int srow = 256 * mm + 32 * (4 * ((lam >> 4) & 1) + (lam & 3)) + 8 * q4 + 4 * (lam >> 5) + ((lam >> 2) & 3);
  const char* src = (const char*)xin + srow * 1280 + (((lane & 7) ^ (lam & 7)) << 4);
  auto fetch = [&](int k0, int k1) {
#pragma unroll
    for (int kc = k0; kc < k1; ++kc) {
      const int kr = kc + 2 * nt;
      dma16(src + (kr < 10 ? kr : kr - 10) * 128, (u32)(uintptr_t)x.lds + (kc * 8 + w) * 1024);
    }
  };
  fetch(0, XF);
  v4u q[10];
  {
    const int slot = 128 * nt + 16 * w + c, k = slot ? slot >> 1 : 320;
    const float amp = slot > 1 ? 0.05590169944f : slot ? 0.f : 0.03952847075f;
    const float kf = (float)k * (1.f / 640.f), a0 = (float)(((128 * nt + 8 * g) * k) % 640) * (1.f / 640.f) + ((c & 1) ? 0.25f : 0.f);
    float v0[8], v1[8];
#pragma unroll
    for (int j = 0; j < 8; ++j) { v0[j] = amp * cosr(a0 + (float)j * kf); v1[j] = amp * cosr(a0 + (float)(32 + j) * kf); }
    const float c2 = 2.f * cosr(32.f * kf);
#pragma unroll
    for (int ks = 0; ks < 10; ks += 2) {
      q[ks] = pack8(v0); q[ks + 1] = pack8(v1);
      if (ks + 2 < 10) {
#pragma unroll
        for (int j = 0; j < 8; ++j) { v0[j] = c2 * v1[j] - v0[j]; v1[j] = c2 * v0[j] - v1[j]; }
      }
    }
#pragma unroll
    for (int i = 0; i < 10; ++i) PIN(q[i]);
  }
  fetch(XF, 10);
  f32x4 acc[2][4];
#pragma unroll
  for (int i = 0; i < 8; ++i) acc[i >> 2][i & 3] = f32x4{0.f, 0.f, 0.f, 0.f};
  LAS char* a0 = x.lds + (c >> 3) * 1024 + (c & 7) * 128 + ((g ^ (c & 7)) << 4);
  LAS char* a1 = x.lds + (c >> 3) * 1024 + (c & 7) * 128 + (((g + 4) ^ (c & 7)) << 4);
  v4u f[8];
  SCHED();
  STAMP(36);
  VMWAIT(8); BAR(); STAMP(1);
#pragma unroll
  for (int i = 0; i < 8; ++i) f[i] = *(LAS v4u*)((i < 4 ? a0 : a1) + (i & 3) * 2048);
#pragma unroll
  for (int kc = 0; kc < 10; ++kc) {
    if (kc == 1) { VMWAIT(5); asm volatile("s_barrier" ::: "memory"); }
    if (kc == 4) { VMWAIT(0); asm volatile("s_barrier" ::: "memory"); }
    if (kc == 4) { a0 = pinp(a0 + 40960); a1 = pinp(a1 + 40960); }
    SCHED();
#pragma unroll
    for (int i = 0; i < 8; ++i) {
      acc[kc / 5][i & 3] = mfma(f[i], q[(2 * kc + (i >> 2)) % 10], acc[kc / 5][i & 3]);
      if (kc < 9) f[i] = *(LAS v4u*)((i < 4 ? a0 : a1) + ((kc + 1) % 5) * 8192 + (i & 3) * 2048);
      SCHED();
    }
  }
  STAMP(37);
  STAMPA(L_CTL + 256 + (x.w == 7 ? 58 * 4 : 256));
  const float sg = (c & 2) ? -1.f : 1.f;
#pragma unroll
  for (int i = 0; i < 4; ++i) acc[0][i] += sg * acc[1][i];
  BAR();
  LAS char* rp = pinp(x.lds + L_MISC + 1024 + 4 * g * 4);
  const int off = O_ZT + ((nt * 32 + mm * 4 + q4) * 32 + 4 * w + (c >> 2)) * 512 + (c & 3) * 64 + g * 16;
#pragma unroll
  for (int u = 0; u < 2; ++u)
    st16(x.rs, pack8(acc[0][2 * u] * *(LAS f32x4*)(rp + u * 128), acc[0][2 * u + 1] * *(LAS f32x4*)(rp + u * 128 + 64)), off, u * 256);
  if (w == 0 || (w == 1 && nt == 0))
    st4(x.rs, *(LAS u32*)(x.lds + L_MISC + 1280 + w * 256 + lane * 4), (w ? O_MU : O_SIG + nt * 8192) + ((mm * 4 + q4) * 64 + lane) * 4, 0);
}

DI void phase_a_stats(const Ctx& x) {
  const int lw = x.w & 3, g = x.g, c = x.c;
  LAS char* a0 = x.lds + lw * 2048 + (c >> 3) * 1024 + (c & 7) * 128 + ((g ^ (c & 7)) << 4);
  LAS char* a1 = x.lds + lw * 2048 + (c >> 3) * 1024 + (c & 7) * 128 + (((g + 4) ^ (c & 7)) << 4);
  const v4u ones = {0x3f803f80u, 0x3f803f80u, 0x3f803f80u, 0x3f803f80u};
  f32x4 gr = {0.f, 0.f, 0.f, 0.f}, sm = gr;
  __builtin_amdgcn_s_setprio(2);
#pragma unroll
  for (int st = 0; st < 3; ++st) {
    constexpr int lo[4] = {0, 2, 5, 10};
    STAMPL(54 + st);
    BAR();
    STAMPL(51 + st);
    if (st == 2) { a0 = pinp(a0 + 40960); a1 = pinp(a1 + 40960); }
    v4u f[10];
#pragma unroll
    for (int kc = lo[st]; kc < lo[st + 1]; ++kc) {
      f[2 * (kc - lo[st])] = *(LAS v4u*)(a0 + (kc % 5) * 8192);
      f[2 * (kc - lo[st]) + 1] = *(LAS v4u*)(a1 + (kc % 5) * 8192);
    }
#pragma unroll
    for (int j = 0; j < 2 * (lo[st + 1] - lo[st]); ++j) { gr = mfma(f[j], f[j], gr); sm = mfma(ones, f[j], sm); }
    SCHED();
  }
  STAMPL(57);
  const int r = c & 3;
  const float q = xsum16_32((c >> 2) == g ? (r == 0 ? gr[0] : r == 1 ? gr[1] : r == 2 ? gr[2] : gr[3]) : 0.f);
  const float mu = sm[0] * (1.f / 640.f);
  const float var = fmaxf(q * (1.f / 640.f) - mu * mu, 0.f) + EPS;
  const float rstd = __builtin_amdgcn_rsqf(var);
  if (g < 3) *(LAS float*)(x.lds + L_MISC + 1024 + g * 256 + (16 * lw + c) * 4) = g == 0 ? rstd : g == 1 ? var * rstd : mu;
  __builtin_amdgcn_s_setprio(0);
  STAMPL(47);
  BAR();
}
DI int stat_at(int n1, int n2) {
  return ((n1 >> 3) * 4 + (n2 >> 3)) * 64 + (2 * ((n2 >> 2) & 1) + ((n1 >> 2) & 1)) * 16 + 4 * (n2 & 3) + (n1 & 3);
}

DI void side_wt(const Ctx& x, const u16* wr, const u16* wi, int v, int lo, int sb, int pub) {
  const int fq = v >> 3, fg = v & 7, lw = x.w & 3;
#pragma unroll
  for (int i = 0; i < 2; ++i) {
    const char* src = (const char*)(i ? wi : wr) + (4 * fq + lw) * 41088 + fg * 5136;
#pragma unroll
    for (int pc = 0; pc < 6; ++pc) dma16(src + min(pc * 64 + x.lane, 320) * 16, (u32)(uintptr_t)x.lds + lo + (lw * 2 + i) * 6144 + pc * 1024);
  }
  VMWAIT(0);
  soft_barrier(x, sb, 4);
  STAMPL(48);
  const int c8 = x.lane & 7, kk = x.lane >> 3;
  const int rho = (fg >> 1) * 128 + fq * 16 + (fg & 1) * 8 + c8;
#pragma unroll 2
  for (int it = 0; it < 10; ++it) {
    const int j = it * 4 + lw, kap = (j >> 3) * 64 + kk * 8 + (j & 7);
    LAS const u16* sp = (LAS const u16*)(x.lds + lo) + c8 * 321 + (kap ? kap : 320);
    v4u o;
#pragma unroll
    for (int r = 0; r < 4; ++r) o[r] = (u32)sp[r * 6144] | ((u32)sp[r * 6144 + 3072] << 16);
    st16(x.rs, o, O_WT + kap * 8192 + rho * 16, 0);
  }
  STAMPL(46);
  publish(x, pub, F_W + v, 2, 4);
}
template <int GP> DI int blk_addr(const Ctx& x, int gam0, int rb, int& tile, int& nb) {
  const int p = x.c & 3, r0 = 8 * x.g + (x.c >> 2);
  bool single;
  group_dst<GP>(gam0 + (rb >> 1), rb & 1, tile, nb, single);
  const int ch = (rb >> 1) * 4 + (single ? 2 * (rb & 1) + (p >> 1) : p), hb = single ? (p & 1) : (rb & 1);
  return r0 * 128 + ((ch ^ (((r0 >> 1) & 1) << 2)) << 4) + hb * 8;
}
DI void tile_pos(int j, int& ks, int& ct) {
  const bool is3 = j < 540;
  const int jj = j - (is3 ? 0 : j < 740 ? 540 : 740);
  ks = (jj * (is3 ? 2428 : 6554)) >> 16;
  ct = jj - ks * (is3 ? 27 : 10);
}
DI void tile_dma(const Ctx& x, int j, int lds_off, const u16* w2, const u16* w3, const u16* w4) {
  const int row = x.lane >> 3, ch = ((x.lane & 7) ^ (((row >> 1) & 1) << 2)) << 4, rs = j < 540 ? 3456 : 1280;
  int ks, ct;
  tile_pos(j, ks, ct);
  const char* src = (const char*)(j < 540 ? w3 : j < 740 ? w2 : w4) + (ks * 32 * rs + ct * 128) + (row * rs + ch);
#pragma unroll
  for (int i = 0; i < 4; ++i) dma16(src + i * 8 * rs, (u32)(uintptr_t)x.lds + lds_off + i * 1024);
}
DI void tile_out(const Ctx& x, int j, int lds_off) {
  const bool is3 = j < 540;
  const int GP = is3 ? 3 : 2, tb = is3 ? 143360 : j < 740 ? 102400 : 276480, base = is3 ? O_B6 : j < 740 ? O_B5 : O_B7;
  int ks, ct;
  tile_pos(j, ks, ct);
  const int p = x.c & 3, r0 = 8 * x.g + (x.c >> 2), sw = ((r0 >> 1) & 1) << 6;
  LAS char* lp = x.lds + lds_off + r0 * 128;
  const v4u ones = {0x3f803f80u, 0x3f803f80u, 0x3f803f80u, 0x3f803f80u};
  v4u f[4];
  int dst[4];
#pragma unroll
  for (int rb = 0; rb < 4; ++rb) {
    const int gam = 2 * ct + (rb >> 1), h = rb & 1, sup = (gam * (is3 ? 37 : 52)) >> 8, e = gam - sup * (2 * GP + 1);
    const bool single = e == GP, hi = e > GP;
    dst[rb] = (2 * sup + (int)(hi || (single && h))) * tb + (ks * (2 * GP + 1) + (single ? 2 * GP : 2 * (e - (hi ? GP + 1 : 0)) + h)) * 1024;
    LAS char* ad = lp + (((rb >> 1) * 64 + (single ? h * 32 + p * 8 : p * 16 + h * 8)) ^ sw);
    const v2u lo = trread(ad), hi2 = trread(ad + 512);
    f[rb] = v4u{lo.x, lo.y, hi2.x, hi2.y};
  }
#pragma unroll
  for (int rb = 0; rb < 4; ++rb) st16(x.rs, f[rb], x.lane * 16, base + dst[rb]);
  if (is3) {
#pragma unroll
    for (int rb = 0; rb < 4; ++rb) {
      const f32x4 s = mfma(f[rb], ones, f32x4{0.f, 0.f, 0.f, 0.f});
      if (x.c == 0) st16(x.rs, __builtin_bit_cast(v4u, s), x.g * 16, O_CS3 + (dst[rb] >> 4));
    }
  }
}
DI void side_qc(const Ctx& x, int job, int njobs) {
#pragma unroll 1
  for (int pc = job; pc < 800; pc += njobs) {
    const int ks = pc / 40, blk = pc - ks * 40;
    int tile, nb; bool single;
    group_dst<2>(blk >> 1, blk & 1, tile, nb, single);
    const int n = (blk >> 1) * 32 + (single ? 16 * (blk & 1) + x.c : 8 * (x.c >> 2) + 4 * (blk & 1) + (x.c & 3));
    const float a0 = (float)((n * (16 * ks + 4 * x.g)) % 640) * (1.f / 640.f), da = (float)n * (1.f / 640.f);
    v4u f;
#pragma unroll
    for (int i = 0; i < 4; ++i) {
      const float a = a0 + (float)i * da;
      f[i] = pk(0.05590169944f * cosr(a), -0.05590169944f * sinr(a));
    }
    if (ks == 0 && x.g == 0) f[0] = pk((n & 1) ? -0.03952847075f : 0.03952847075f, 0.f);
    st16(x.rs, f, x.lane * 16, O_BC + (8 + tile) * 102400 + (ks * 5 + nb) * 1024);
  }
}
DI void side_w1p(const Ctx& x, const u16* w1, int ct, int nt) {
  {
    const int row = 8 * x.w + (x.lane >> 3);
    const char* src = (const char*)w1 + row * 1280 + ct * 128 + (((x.lane & 7) ^ (((row >> 1) & 1) << 2)) << 4);
#pragma unroll
    for (int i = 0; i < 10; ++i) {
      const int kr = i + 2 * nt;
      dma16(src + (kr < 10 ? kr : kr - 10) * 64 * 1280, (u32)(uintptr_t)x.lds + (i * 8 + x.w) * 1024);
    }
  }
  v4u qc[10], qs[10];
  gen_q(64 * nt + 16 * (x.w & 3) + x.c, x.g, 128 * nt, qc, qs);
  f32x4 acc[2][2][2];
#pragma unroll
  for (int i = 0; i < 8; ++i) acc[i >> 2][(i >> 1) & 1][i & 1] = f32x4{0.f, 0.f, 0.f, 0.f};
  int addr[2], tile[2], nb[2];
#pragma unroll
  for (int rb = 0; rb < 2; ++rb) addr[rb] = blk_addr<2>(x, 2 * ct, 2 * (x.w >> 2) + rb, tile[rb], nb[rb]);
  SCHED();
  VMWAIT(0);
  soft_barrier(x, C_SB, 8);
  STAMP(1);
  v2u lo[4], hi[4];
#pragma unroll
  for (int i = 0; i < 4; ++i) { lo[i] = trread(x.lds + addr[i & 1] + (i >> 1) * 4096); hi[i] = trread(x.lds + addr[i & 1] + (i >> 1) * 4096 + 512); }
#pragma unroll
  for (int kc = 0; kc < 10; ++kc) {
    if (kc == 4) { addr[0] += 40960; addr[1] += 40960; PIN(addr[0]); PIN(addr[1]); }
#pragma unroll
    for (int i = 0; i < 4; ++i) {
      const int qi = (2 * kc + (i >> 1)) % 10;
      const v4u f = {lo[i].x, lo[i].y, hi[i].x, hi[i].y};
      acc[kc / 5][i & 1][0] = mfma(qc[qi], f, acc[kc / 5][i & 1][0]);
      acc[kc / 5][i & 1][1] = mfma(qs[qi], f, acc[kc / 5][i & 1][1]);
      if (kc < 9) {
        LAS char* ad = x.lds + addr[i & 1] + ((kc + 1) % 5) * 8192 + (i >> 1) * 4096;
        lo[i] = trread(ad); hi[i] = trread(ad + 512);
      }
      SCHED();
    }
  }
  const f32x4 sg = {1.f, -1.f, 1.f, -1.f};
#pragma unroll
  for (int rb = 0; rb < 2; ++rb) {
    const f32x4 re = acc[0][rb][0] + sg * acc[1][rb][0], im = acc[0][rb][1] + sg * acc[1][rb][1];
    st16(x.rs, v4u{pk(re[0], im[0]), pk(re[1], im[1]), pk(re[2], im[2]), pk(re[3], im[3])}, x.lane * 16,
         O_BC + tile[rb] * 102400 + ((4 * nt + (x.w & 3)) * 5 + nb[rb]) * 1024);
  }
}
#define TGATE 0
DI void side_jobs(const Ctx& x, int xcd, int sl, const u16* wr, const u16* wi, const u16* w1, const u16* w2, const u16* w3, const u16* w4) {
  const int y = sl < 2 ? sl * 8 + xcd : (sl < 7 && xcd >= 2) ? 16 + (sl - 2) * 6 + (xcd - 2) : -1;
  const int ct = sl >= 7 ? xcd : 8 + xcd, nt = sl >= 7 ? sl - 7 : sl - 2;
  const u64 t0 = __builtin_amdgcn_s_memrealtime();
  BAR();
  if (y < 0 && x.w < 8) {
    side_w1p(x, w1, ct, nt);
    STAMP(5);
    publish<8, 96>(x, 1, F_S + sl * 8 + xcd, 3);
    return;
  }
  const bool two = y >= 18;
  if (y >= 0 && (x.w >= 8 || (!two && x.w < 4))) {
    const bool ld = x.w >= 8;
    side_wt(x, wr, wi, ld ? y : 46 + y, ld ? 0 : 49152, ld ? C_SB0 : C_SB1, ld ? 0 : 5);
  } else {
    if (y >= 0) {
      side_qc(x, two ? 72 + (y - 18) * 8 + x.w : y * 4 + x.w - 4, 296);
      publish<8, 96>(x, 1, F_S + sl * 8 + xcd, 3, two ? 8 : 4);
    }
    lds_wait_ge(x.lds + L_CTL + (y >= 0 ? C_PUB : C_SB), y >= 0 ? 4 : 8);
    const int n = y < 0 ? 4 : two ? 2 : 1;
    const int j = y < 0 ? ((ct * 5 + nt) * 4 + (x.w & 3)) * 4 : two ? 800 + ((y - 18) * 8 + x.w) * 2 : min(1248 + y * 4 + x.w - 4, 1279);
    const int lo = y < 0 ? L_TJ + (x.w & 3) * 16384 : two ? 49152 + x.w * 8192 : 81920 + x.w * 4096;
    STAMPW(59);
#pragma unroll 1
    for (int k = 0; k < n; ++k) tile_dma(x, j + k, lo + k * 4096, w2, w3, w4);
    VMWAIT(0);
    STAMPW(61);
#pragma unroll 1
    for (int k = 0; k < 64 && (u32)(__builtin_amdgcn_s_memrealtime() - t0) < TGATE; ++k) __builtin_amdgcn_s_sleep(4);
#pragma unroll 1
    for (int k = 0; k < n; ++k) tile_out(x, j + k, lo + k * 4096);
    STAMPW(62);
  }
  publish<8, 96>(x, 6, F_T + sl * 8 + xcd, 47, y < 0 ? 4 : 12);
  if (x.w >= 8) {
    lds_wait_ge(x.lds + L_CTL + C_PUB + 24, y < 0 ? 4 : 12);
    if (y < 0) lds_wait_ge(x.lds + L_CTL + C_PUB + 4, 8);
  }
}

DI v4u negv(v4u a) { return a ^ 0x80008000u; }
DI void phase_b(const Ctx& x, int p) {
  const int w = x.w, lane = x.lane, g = x.g, c = x.c, wq = w & 3, col = w >> 2;
  v4u C64[2], S64[2], N64[2], C32[2], S32[2], N32[2];
  f32x4 tc[2], ts[2];
  {
    const float f1 = (float)(16 * wq + c);
#pragma unroll
    for (int ks = 0; ks < 2; ++ks) {
      float cv[8], sv[8];
#pragma unroll
      for (int j = 0; j < 8; ++j) { const float a = f1 * (float)(32 * ks + 8 * g + j) * (1.f / 64.f); cv[j] = cosr(a); sv[j] = sinr(a); }
      C64[ks] = v4u{pk(cv[0], cv[1]), pk(cv[2], cv[3]), pk(cv[4], cv[5]), pk(cv[6], cv[7])};
      S64[ks] = v4u{pk(sv[0], sv[1]), pk(sv[2], sv[3]), pk(sv[4], sv[5]), pk(sv[6], sv[7])};
      N64[ks] = negv(S64[ks]);
#pragma unroll
      for (int j = 0; j < 8; ++j) {
        const float a = (float)(16 * ks + c) * (float)(16 * (j >> 2) + 4 * g + (j & 3)) * (1.f / 32.f);
        cv[j] = cosr(a); sv[j] = sinr(a);
      }
      C32[ks] = v4u{pk(cv[0], cv[1]), pk(cv[2], cv[3]), pk(cv[4], cv[5]), pk(cv[6], cv[7])};
      S32[ks] = v4u{pk(sv[0], sv[1]), pk(sv[2], sv[3]), pk(sv[4], sv[5]), pk(sv[6], sv[7])};
      N32[ks] = negv(S32[ks]);
#pragma unroll
      for (int r = 0; r < 4; ++r) {
        const float a = f1 * (float)(16 * ks + 4 * g + r) * (1.f / 2048.f);
        tc[ks][r] = 0.02209708691f * cosr(a); ts[ks][r] = 0.02209708691f * sinr(a);
      }
    }
  }
  const int nt = p >> 5, kap = 2 * p + col;
#pragma unroll
  for (int i = 0; i < 2; ++i) { PIN(C64[i]); PIN(S64[i]); PIN(N64[i]); PIN(C32[i]); PIN(S32[i]); PIN(N32[i]); PIN(tc[i]); PIN(ts[i]); }
  STAMP(4);
  lds_wait_ge(x.lds + L_CTL + C_GOW, 1);
  v4u wv[2];
#pragma unroll
  for (int t = 0; t < 2; ++t) wv[t] = ld16(x.rs, O_WT + kap * 8192 + (wq * 2 + t) * 1024 + lane * 16, 0);
  const int zo = O_ZT + ((nt * 32 + 4 * w + (lane >> 5)) * 32 + (p & 31)) * 512 + (lane & 31) * 16;
  LAS char* xd = pinp(x.lds + L_XS + ((lane >> 2) & 3) * 4096 + (w >> 2) * 1024 + ((w & 3) * 16 + 4 * (lane >> 4) + (lane & 3)) * 16);
  LAS char* xs = pinp(x.lds + L_XS + col * 8192 + lane * 16);
  const int so = O_SIG + nt * 8192 + stat_at(16 * wq + 4 * g, 16 * col + c) * 4;
  STAMP(5);
  lds_wait_ge(x.lds + L_CTL + C_GO, 2);
  STAMP(6);
  const v4u l0 = ld16(x.rs, zo, 0), l1 = ld16(x.rs, zo, 32768);
  const f32x4 sg = __builtin_bit_cast(f32x4, ld16(x.rs, so, 0));
  *(LAS v4u*)xd = l0;
  *(LAS v4u*)(xd + 2048) = l1;
  soft_barrier(x, C_SB0, 8);
  STAMP(50);
  v4u xr[2][2], xi[2][2];
#pragma unroll
  for (int i = 0; i < 4; ++i) {
    xr[i >> 1][i & 1] = *(LAS v4u*)(xs + i * 1024);
    xi[i >> 1][i & 1] = *(LAS v4u*)(xs + 4096 + i * 1024);
  }
  SCHED();
  const f32x4 zero = {0.f, 0.f, 0.f, 0.f};
  f32x4 ar[2], ai[2], br[2], bi[2];
#pragma unroll
  for (int b = 0; b < 2; ++b) {
    ar[b] = mfma(xr[b][0], C64[0], zero); ai[b] = mfma(xi[b][0], C64[0], zero);
    ar[b] = mfma(xi[b][0], S64[0], ar[b]); ai[b] = mfma(xr[b][0], N64[0], ai[b]);
    ar[b] = mfma(xr[b][1], C64[1], ar[b]); ai[b] = mfma(xi[b][1], C64[1], ai[b]);
    ar[b] = mfma(xi[b][1], S64[1], ar[b]); ai[b] = mfma(xr[b][1], N64[1], ai[b]);
  }
#pragma unroll
  for (int b = 0; b < 2; ++b) { br[b] = ar[b] * tc[b] + ai[b] * ts[b]; bi[b] = ai[b] * tc[b] - ar[b] * ts[b]; }
  v4u pr = pack8(br[0], br[1]), pi = pack8(bi[0], bi[1]);
  STAMP(49);
#pragma unroll
  for (int t = 0; t < 2; ++t) {
    ar[t] = mfma(C32[t], pr, zero); ai[t] = mfma(C32[t], pi, zero);
    ar[t] = mfma(S32[t], pi, ar[t]); ai[t] = mfma(N32[t], pr, ai[t]);
  }
#pragma unroll
  for (int t = 0; t < 2; ++t)
#pragma unroll
    for (int r = 0; r < 4; ++r) {
      const float wre = blo(wv[t][r]), wim = bhi(wv[t][r]);
      br[t][r] = ar[t][r] * wre - ai[t][r] * wim;
      bi[t][r] = ar[t][r] * wim + ai[t][r] * wre;
    }
  pr = pack8(br[0], br[1]); pi = pack8(bi[0], bi[1]);
#pragma unroll
  for (int t = 0; t < 2; ++t) {
    ar[t] = mfma(C32[t], pr, zero); ai[t] = mfma(C32[t], pi, zero);
    ar[t] = mfma(N32[t], pi, ar[t]); ai[t] = mfma(S32[t], pr, ai[t]);
  }
  {
    LAS char* img = pinp(x.lds + L_XB + col * 9216 + (16 * wq + c) * 72 + g * 8);
#pragma unroll
    for (int t = 0; t < 2; ++t) {
      f32x4 ur = ar[t] * tc[t] - ai[t] * ts[t], ui = ai[t] * tc[t] + ar[t] * ts[t];
      *(LAS v2u*)(img + t * 32) = v2u{pk(ur[0], ur[1]), pk(ur[2], ur[3])};
      *(LAS v2u*)(img + 4608 + t * 32) = v2u{pk(ui[0], ui[1]), pk(ui[2], ui[3])};
    }
  }
  STAMP(38);
  soft_barrier(x, C_SB, 8);
  STAMP(39);
  LAS char* rd = pinp(x.lds + L_XB + (8 * g + (c >> 2)) * 72 + col * 32 + (c & 3) * 8);
#pragma unroll
  for (int cc = 0; cc < 2; ++cc) {
    ar[cc] = zero; ai[cc] = zero;
#pragma unroll
    for (int ks = 0; ks < 2; ++ks) {
      v2u r0 = trread(rd + cc * 9216 + ks * 2304), r1 = trread(rd + cc * 9216 + ks * 2304 + 288);
      v2u i0 = trread(rd + cc * 9216 + 4608 + ks * 2304), i1 = trread(rd + cc * 9216 + 4608 + ks * 2304 + 288);
      const v4u ur = {r0.x, r0.y, r1.x, r1.y}, ui = {i0.x, i0.y, i1.x, i1.y};
      ar[cc] = mfma(C64[ks], ur, ar[cc]); ai[cc] = mfma(C64[ks], ui, ai[cc]);
      ar[cc] = mfma(N64[ks], ui, ar[cc]); ai[cc] = mfma(S64[ks], ur, ai[cc]);
    }
  }
  v2u z[4];
  {
    LAS char* zp = pinp(x.lds + L_XS + (col * 2 + (wq >> 1)) * 1024 + ((2 * (wq & 1) + (g >> 1)) * 16 + c) * 16 + (g & 1) * 8);
#pragma unroll
    for (int i = 0; i < 4; ++i) z[i] = *(LAS v2u*)(zp + i * 4096);
  }
  if (p == 0) ai[0] = zero;
  u32 o[8];
#pragma unroll
  for (int r = 0; r < 4; ++r) {
    float zz[4];
#pragma unroll
    for (int i = 0; i < 4; ++i) zz[i] = (r & 1) ? bhi(z[i][r >> 1]) : blo(z[i][r >> 1]);
    o[2 * r] = pk(sg[r] * zz[0] + ar[0][r], sg[r] * zz[1] + ai[0][r]);
    o[2 * r + 1] = pk(sg[r] * zz[2] + ar[1][r], sg[r] * zz[3] + ai[1][r]);
  }
  const int ob = O_HB + p * 16384 + (((8 * wq + 2 * g) * 2 + col) * 16 + c) * 16;
  st16(x.rs, v4u{o[0], o[1], o[2], o[3]}, ob, 0);
  st16(x.rs, v4u{o[4], o[5], o[6], o[7]}, ob + 512, 0);
}

#define GATE 0
#define GATEW(i) do { if (GATE == 1) lds_wait_ge(x.lds + L_CTL + C_PUB + 4 * (i), 8); if (GATE == 2) lds_wait_ge(x.lds + L_CTL + C_GO, (i) + 2); } while (0)
#define DLY 6
#define DLYA 6
#define DLYB 10
#define PACE 6
#define STR2(x) #x
#define STR(x) STR2(x)
DI void dma_lin(const Ctx& x, int ws_off, int lds_off, int p0, int p1) {
  const char* src = x.ws + ws_off + x.lane * 16;
#pragma unroll 1
  for (int pc = p0 + (x.w & 3); pc < p1; pc += 4) {
    dma16_sc1(src + pc * 1024, (u32)(uintptr_t)x.lds + lds_off + pc * 1024);
    asm volatile("s_waitcnt vmcnt(" STR(PACE) ")" ::: "memory");
  }
}
DI int ft_pair(int rowblk, int KS, int gam, int g, int c) { return (rowblk * KS + gam) * 1024 + (g * 16 + c) * 16; }
DI int ft_single(int rowblk, int KS, int gam, int h, int g, int c) {
  return (rowblk * KS + gam) * 1024 + ((2 * h + (g >> 1)) * 16 + c) * 16 + (g & 1) * 8;
}

constexpr int L_EX128 = 100 * 1024, L_EX64 = 92 * 1024;
template <int NB, bool HPLANES>
DI void gemm128(const Ctx& x, int aoff, u32 land, f32x4 (&acc)[2][NB], float& ssq) {
  constexpr int LA = 4, EXW = NB == 5 ? 6 : 7;
  const int kw = x.w >> 2;
  v4u a[10][2];
  auto lda = [&](int it) {
    if (HPLANES) { a[it][0] = ld16(x.rs, aoff, it * 262144); a[it][1] = ld16(x.rs, aoff, it * 262144 + 16384); }
    else { a[it][0] = ld16(x.rs, aoff, it * 2048); a[it][1] = ld16(x.rs, aoff, it * 2048 + 20480); }
  };
#pragma unroll
  for (int it = 0; it < LA; ++it) lda(it);
  SCHED();
  wait_landed(x, land);
  STAMP(NB == 5 ? 26 : 28);
#pragma unroll
  for (int i = 0; i < 2; ++i)
#pragma unroll
    for (int n = 0; n < NB; ++n) acc[i][n] = f32x4{0.f, 0.f, 0.f, 0.f};
  f32x4 gr0 = {0.f, 0.f, 0.f, 0.f}, gr1 = gr0;
  LAS char* bp = x.lds + kw * NB * 1024 + x.lane * 16;
  v4u f[NB];
#pragma unroll
  for (int n = 0; n < NB; ++n) f[n] = *(LAS v4u*)(bp + n * 1024);
#pragma unroll
  for (int it = 0; it < 10; ++it) {
    SCHED();
    if (it + LA < 10) lda(it + LA);
    v4u a0 = a[it][0], a1 = a[it][1];
    if (HPLANES) {
      a0 = v4u{a[it][0].x, a[it][0].y, a[it][1].x, a[it][1].y};
      a1 = v4u{a[it][0].z, a[it][0].w, a[it][1].z, a[it][1].w};
    }
    SCHED();
#pragma unroll
    for (int n = 0; n < NB; ++n) {
      acc[0][n] = mfma(f[n], a0, acc[0][n]);
      acc[1][n] = mfma(f[n], a1, acc[1][n]);
      if (n == 0 && (it & 3) == 3) bp = pinp(bp + 8 * NB * 1024);
      if (it < 9) f[n] = *(LAS v4u*)(bp + (((it + 1) & 3) * 2 * NB + n) * 1024);
      if (HPLANES && n == 0) { gr0 = mfma(a0, a0, gr0); gr1 = mfma(a1, a1, gr1); }
      SCHED();
    }
  }
  float s0 = 0.f, s1 = 0.f;
  if (HPLANES) {
    const int r = x.c & 3;
    const bool dg = (x.c >> 2) == x.g;
    s0 = dg ? (r == 0 ? gr0[0] : r == 1 ? gr0[1] : r == 2 ? gr0[2] : gr0[3]) : 0.f;
    s1 = dg ? (r == 0 ? gr1[0] : r == 1 ? gr1[1] : r == 2 ? gr1[2] : gr1[3]) : 0.f;
  }
  BAR();
  STAMP(NB == 5 ? 12 : 20);
  LAS char* mine = pinp(x.lds + L_EX128 + x.w * EXW * 1024 + x.lane * 16);
  LAS char* other = pinp(x.lds + L_EX128 + (x.w ^ 4) * EXW * 1024 + x.lane * 16);
  if (kw) {
#pragma unroll
    for (int n = 0; n < NB; ++n) { *(LAS f32x4*)(mine + n * 1024) = acc[0][n]; acc[0][n] = acc[1][n]; }
    if (HPLANES) { *(LAS float*)(mine + NB * 1024) = s0; s0 = s1; }
  } else {
#pragma unroll
    for (int n = 0; n < NB; ++n) *(LAS f32x4*)(mine + n * 1024) = acc[1][n];
    if (HPLANES) *(LAS float*)(mine + NB * 1024) = s1;
  }
  BAR();
  STAMP(NB == 5 ? 32 : 34);
  if (HPLANES) ssq = xsum16_32(s0 + *(LAS float*)(other + NB * 1024));
#pragma unroll
  for (int n = 0; n < NB; ++n) acc[0][n] += *(LAS f32x4*)(other + n * 1024);
}

template <int KS>
DI void gemm64(const Ctx& x, int a0off, int a1off, u32 land, f32x4 (&o)[3]) {
  constexpr int NIT = (KS + 3) / 4, LA = KS == 20 ? 5 : 4;
  const int kw = x.w >> 1;
  v4u a[NIT][2];
  constexpr int AS = KS == 20 ? 1024 : 4096, BS = KS == 20 ? 5120 : 20480;
  auto lda = [&](int it) { a[it][0] = ld16(x.rs, a0off, it * AS); a[it][1] = ld16(x.rs, a1off, it * AS); };
#pragma unroll
  for (int it = 0; it < LA; ++it) lda(it);
  SCHED();
  wait_landed(x, land);
  STAMP(KS == 20 ? 27 : 29);
  f32x4 acc[2][5];
#pragma unroll
  for (int i = 0; i < 2; ++i)
#pragma unroll
    for (int n = 0; n < 5; ++n) acc[i][n] = f32x4{0.f, 0.f, 0.f, 0.f};
  LAS char* bs = x.lds + kw * (KS == 20 ? 25600 : 5120) + x.lane * 16;
  LAS char* bq[3] = {bs + (kw >> 1) * 2048, bs + ((kw >> 1) ^ 1) * 2048, bs + 4096};
  v4u f[5];
#pragma unroll
  for (int n = 0; n < 5; ++n) f[n] = *(LAS v4u*)(bq[n >> 1] + (n & 1) * 1024);
#pragma unroll
  for (int it = 0; it < NIT; ++it) {
    SCHED();
    if (KS == 54 && (it == 3 || it == 6 || it == 10)) BAR();
    if (it + LA < NIT) lda(it + LA);
    const int st = (it + 1) % 7;
    if (KS == 54 && st % 3 == 0) {
#pragma unroll
      for (int n = 0; n < 3; ++n) bq[n] = pinp(bq[n] + (st ? 61440 : -122880));
    }
    SCHED();
    if (it * 4 + 3 < KS || kw < KS - it * 4) {
#pragma unroll
      for (int n = 0; n < 5; ++n) {
        acc[0][n] = mfma(f[n], a[it][0], acc[0][n]);
        acc[1][n] = mfma(f[n], a[it][1], acc[1][n]);
        if (it + 1 < NIT) f[n] = *(LAS v4u*)(bq[n >> 1] + (KS == 54 ? st % 3 : st) * BS + (n & 1) * 1024);
        SCHED();
      }
    }
  }
  BAR();
  STAMP(KS == 20 ? 16 : 24);
  LAS char* mine = pinp(x.lds + L_EX64 + x.w * 8192 + x.lane * 16 - 2048);
#pragma unroll
  for (int n = 2; n < 5; ++n) *(LAS f32x4*)(mine + n * 1024) = acc[0][n];
#pragma unroll
  for (int n = 0; n < 5; ++n) *(LAS f32x4*)(mine + (5 + n) * 1024) = acc[1][n];
  BAR();
  STAMP(KS == 20 ? 33 : 35);
  o[0] = acc[0][0]; o[1] = acc[0][1]; o[2] = acc[0][4];
#pragma unroll
  for (int m = 1; m < 4; ++m) {
    LAS char* s = pinp(x.lds + L_EX64 + (x.w ^ (2 * m)) * 8192 + x.lane * 16 - 2048) + (m & 1) * 5120;
    o[0] += *(LAS f32x4*)(s + (m >> 1) * 2048);
    o[1] += *(LAS f32x4*)(s + (m >> 1) * 2048 + 1024);
    o[2] += *(LAS f32x4*)(s + 4096);
  }
}

DI f32x4 gelu4(f32x4 v) { return f32x4{gelu(v[0]), gelu(v[1]), gelu(v[2]), gelu(v[3])}; }
DI v2u pack4(f32x4 a) { return v2u{pk(a[0], a[1]), pk(a[2], a[3])}; }
DI f32x4 unlo(v4u v) { return f32x4{blo(v.x), bhi(v.x), blo(v.y), bhi(v.y)}; }
DI f32x4 unhi(v4u v) { return f32x4{blo(v.z), bhi(v.z), blo(v.w), bhi(v.w)}; }
DI f32x4 un4(v2u v) { return f32x4{blo(v.x), bhi(v.x), blo(v.y), bhi(v.y)}; }

DI void phase_c(const Ctx& x, int P, int t) {
  const int mw = x.w & 3, kw = x.w >> 2, g = x.g, c = x.c;
  const int np = 2 * P + (mw >> 1), b = mw & 1;
  const int rowblk = 4 * np + b + 2 * kw;
  const float mu = __builtin_bit_cast(float, ld4(x.rs, O_MU + stat_at(rowblk >> 1, 16 * (rowblk & 1) + c) * 4, 0));
  f32x4 acc[2][5];
  float ssq;
  gemm128<5, true>(x, O_HB + (8 * kw + 2 * g) * 16384 + ((np * 2 + b) * 16 + c) * 16, 1, acc, ssq);
  const float vh = ssq * (1.f / 640.f);
  float sc = __builtin_amdgcn_rsqf(vh + EPS);
  if (g == 0) *(LAS f32x2*)(x.lds + L_MISC + 1024 + ((rowblk & 7) * 16 + c) * 8) = f32x2{(vh + EPS) * sc, mu};
  const int t8 = t & 7, g0 = (t8 >> 1) * 5 + (t8 & 1) * 3, gs = (t8 >> 1) * 5 + 2;
  if (t < 8) {
    sc *= __builtin_amdgcn_rsqf(vh * sc * sc + EPS);
#pragma unroll
    for (int n = 0; n < 5; ++n) acc[0][n] = gelu4(acc[0][n] * sc);
  } else {
#pragma unroll
    for (int n = 0; n < 5; ++n) acc[0][n] *= sc;
  }
  const int dst = t < 8 ? O_G : O_XB;
#pragma unroll
  for (int i = 0; i < 2; ++i) st16(x.rs, pack8(acc[0][2 * i], acc[0][2 * i + 1]), dst + ft_pair(rowblk, 20, g0 + i, g, c), 0);
  st8(x.rs, pack4(acc[0][4]), dst + ft_single(rowblk, 20, gs, t8 & 1, g, c), 0);
}

DI void phase_5(const Ctx& x, int P, int hf, int t8, u32 (&res)[6]) {
  const int mw = x.w & 1, kw = x.w >> 1, g = x.g, c = x.c;
  const int rb0 = 8 * P + 4 * hf + 2 * mw, rowblk = rb0 + (kw & 1);
  const int gp = (t8 >> 1) * 5 + (t8 & 1) * 3 + (kw >> 1), gs = (t8 >> 1) * 5 + 2;
  const int op = ft_pair(rowblk, 20, gp, g, c), os = ft_single(rowblk, 20, gs, t8 & 1, g, c);
  const int sh = (kw & 1 ? -1 : 1) * (1 + (kw >> 1));
  int ao[2];
#pragma unroll
  for (int i = 0; i < 2; ++i) {
    const int r = (rb0 + ((kw & 1) ^ i)) * 16 + c - sh;
    ao[i] = (u32)r < 2048u ? O_G + ft_pair(r >> 4, 20, 5 * kw, g, r & 15) : 0x7f000000;
  }
  const v4u xp = ld16(x.rs, O_XB + op, 0);
  const v2u xs = ld8(x.rs, O_XB + os, 0);
  f32x4 o[3];
  gemm64<20>(x, ao[0], ao[1], 2, o);
  const f32x2 st = *(LAS f32x2*)(x.lds + L_MISC + 1024 + ((rowblk & 7) * 16 + c) * 8);
  const f32x4 b0 = unlo(xp), b1 = unhi(xp), b2 = un4(xs);
  o[0] += b0; o[1] += b1; o[2] += b2;
  const v4u r = pack8(o[0], o[1]);
  st16(x.rs, r, O_X1 + op, 0);
  const v4u ones = {0x3f803f80u, 0x3f803f80u, 0x3f803f80u, 0x3f803f80u};
  float s1 = dot8(r, ones, 0.f), s2 = dot8(r, r, 0.f);
  if (kw < 2) {
    const v2u q = pack4(o[2]);
    st8(x.rs, q, O_X1 + os, 0);
    s1 = dot2(q.y, ones.x, dot2(q.x, ones.x, s1));
    s2 = dot2(q.y, q.y, dot2(q.x, q.x, s2));
  }
  s1 = xsum16_32(s1); s2 = xsum16_32(s2);
  if (g == 0)
    st8(x.rs, v2u{__builtin_bit_cast(u32, s1), __builtin_bit_cast(u32, s2)}, O_ST1 + (rowblk * 16 + t8 * 2 + (kw >> 1)) * 128 + c * 8, 0);
  const v4u rp = pack8(o[0] + b0 * st[0] + st[1], o[1] + b1 * st[0] + st[1]);
  const v2u rq = pack4(o[2] + b2 * st[0] + st[1]);
  res[0] = rp.x; res[1] = rp.y; res[2] = rp.z; res[3] = rp.w; res[4] = rq.x; res[5] = rq.y;
#pragma unroll
  for (int i = 0; i < 6; ++i) PIN(res[i]);
}

DI void phase_6(const Ctx& x, int P, int t) {
  const int mw = x.w & 3, kw = x.w >> 2, g = x.g, c = x.c;
  const int rb0 = 8 * P + 2 * mw, rowblk = rb0 + kw;
  f32x2 p[4];
#pragma unroll
  for (int i = 0; i < 4; ++i) p[i] = __builtin_bit_cast(f32x2, ld8(x.rs, O_ST1 + rowblk * 2048 + x.lane * 8 + i * 512, 0));
  f32x4 acc[2][7];
  float dummy;
  gemm128<7, false>(x, O_X1 + (rb0 * 20 + kw) * 1024 + x.lane * 16, 3, acc, dummy);
  const f32x2 ps = (p[0] + p[1]) + (p[2] + p[3]);
  const float m1 = xsum16_32(ps[0]) * (1.f / 640.f), q1 = xsum16_32(ps[1]) * (1.f / 640.f);
  const float r1 = __builtin_amdgcn_rsqf(fmaxf(q1 - m1 * m1, 0.f) + EPS), nm = -m1 * r1;
  const int g0 = (t >> 1) * 7 + (t & 1) * 4, gs = (t >> 1) * 7 + 3;
  LAS f32x4* cs = (LAS f32x4*)pinp(x.lds + L_MISC + g * 16);
#pragma unroll
  for (int i = 0; i < 3; ++i) {
    const v4u r = pack8(gelu4(acc[0][2 * i] * r1 + cs[8 * i] * nm), gelu4(acc[0][2 * i + 1] * r1 + cs[8 * i + 4] * nm));
    if (g0 + i < 54) st16(x.rs, r, O_U + ft_pair(rowblk, 54, g0 + i, g, c), 0);
  }
  st8(x.rs, pack4(gelu4(acc[0][6] * r1 + cs[24] * nm)), O_U + ft_single(rowblk, 54, gs, t & 1, g, c), 0);
}
DI void colsum_6(const Ctx& x, int t) {
  if (x.lane < 56) {
    f32x2 s = {0.f, 0.f};
#pragma unroll
    for (int k = 0; k < 20; ++k) s += __builtin_bit_cast(f32x2, ld8(x.rs, O_CS3 + t * 8960 + x.lane * 8 + k * 448, 0));
    *(LAS f32x2*)(x.lds + L_MISC + x.lane * 8) = s;
  }
}

DI void phase_7(const Ctx& x, int P, int hf, int t8, const u32 (&res)[6], u16* out) {
  const int mw = x.w & 1, kw = x.w >> 1, g = x.g, c = x.c;
  const int rb0 = 8 * P + 4 * hf + 2 * mw, rowblk = rb0 + (kw & 1);
  const int gp = (t8 >> 1) * 5 + (t8 & 1) * 3 + (kw >> 1), gs = (t8 >> 1) * 5 + 2;
  f32x4 o[3];
  gemm64<54>(x, O_U + (rowblk * 54 + kw) * 1024 + x.lane * 16, O_U + ((rowblk ^ 1) * 54 + kw) * 1024 + x.lane * 16, 4, o);
  char* orow = (char*)out + (rowblk * 16 + c) * 1280;
  *(v4u*)(orow + gp * 64 + g * 16) = pack8(o[0] + un4(v2u{res[0], res[1]}), o[1] + un4(v2u{res[2], res[3]}));
  if (kw < 2) *(v2u*)(orow + gs * 64 + (t8 & 1) * 32 + g * 8) = pack4(o[2] + un4(v2u{res[4], res[5]}));
}

DI void wait_b(const Ctx& x, bool poller, int xcd) {
  wait_flags<3, DLYB>(x, poller, {F_B + xcd * 160 + x.lane, F_B + xcd * 160 + 64 + x.lane, F_B + xcd * 160 + 128 + (x.lane & 31)}, 3);
}
#define CODE_KB 44
#define MAP 1
__global__ __launch_bounds__(NTHR, 3) void fused(const u16* __restrict__ xin, const u16* __restrict__ wr, const u16* __restrict__ wi,
                                                 const u16* __restrict__ w1, const u16* __restrict__ w2, const u16* __restrict__ w3,
                                                 const u16* __restrict__ w4, u16* __restrict__ out, char* ws) {
  extern __shared__ __attribute__((aligned(16))) char smem[];
  if (blockIdx.x >= 240 && threadIdx.x >= 512) {
    u64 pc;
    asm volatile("s_getpc_b64 %0" : "=s"(pc));
    const char* code = (const char*)(pc & ~255ull) + (threadIdx.x - 512) * 16;
#pragma unroll 1
    for (int k = 0; k < CODE_KB / 4; ++k) dma16_sc1(code + k * 4096, (u32)(uintptr_t)(LAS char*)smem + L_MISC + 2048);
  }
  Ctx x;
  x.ws = ws; x.rs = __builtin_amdgcn_make_buffer_rsrc(ws, 0, WS_END, 0x00020000); x.lds = (LAS char*)smem;
  x.tid = threadIdx.x; x.lane = x.tid & 63; x.w = __builtin_amdgcn_readfirstlane(x.tid >> 6); x.g = x.lane >> 4; x.c = x.lane & 15;
  GAS u64* ep = (GAS u64*)(ws + O_EPOCH);
  x.epoch = __hip_atomic_load(ep, __ATOMIC_RELAXED, __HIP_MEMORY_SCOPE_AGENT);
  const int b = blockIdx.x, xcd = b & 7, i = b >> 3;
  const int P = 4 * (xcd >> 1) + (i >> 3), t = 8 * (xcd & 1) + (i & 7), hf = (i >> 2) & 1, t8 = 4 * (xcd & 1) + (i & 3);
  STAMP(0);
  if (x.w == 8 && x.lane < 16) lds_st(x.lds + L_CTL + x.lane * 4, 0);
  if (b >= 160) side_jobs(x, xcd, i - 20, wr, wi, w1, w2, w3, w4);
  if (x.w >= 8) {
    if (b < 160) {
      VMWAIT(0);
      phase_a_stats(x);
      if (x.w == 9) {
        lds_wait_ge(x.lds + L_CTL + C_PUB, 8);
        wait_flags<1, DLYA>(x, true, {F_A + (b >> 5) * 32 + (x.lane & 31)}, 2);
      }
      if (x.w == 10) { poll<1, 4>(x, {F_W + x.lane}); lds_st(x.lds + L_CTL + C_GOW, 1); }
      if (x.w == 8) lds_wait_ge(x.lds + L_CTL + C_GO, 2);
    }
    if (x.w == 8) {
      poll<2, 16>(x, {F_S + xcd * 96 + x.lane, F_S + xcd * 96 + 64 + (x.lane & 31)});
      if (b < 160) lds_wait_ge(x.lds + L_CTL + C_SB0, 8);
      lds_st(x.lds + L_CTL + C_LGO, 1);
      STAMPW(9);
    } else lds_wait_ge(x.lds + L_CTL + C_LGO, 1);
    dma_lin(x, O_BC + t * 102400, 0, 0, 100);
    LANDED(0);
    STAMPL(40);
    if (x.w == 8) {
      poll<2, 16>(x, {F_T + xcd * 96 + x.lane, F_T + xcd * 96 + 64 + (x.lane & 31)});
      colsum_6(x, t);
    }
    STAMPL(44);
    BAR();
    dma_lin(x, O_B5 + t8 * 102400, 0, 0, 4);
    BAR();
    GATEW(2);
    dma_lin(x, O_B5 + t8 * 102400, 0, 4, 100);
    STAMPL(45);
    LANDED(0);
    STAMPL(41);
    BAR();
    dma_lin(x, O_B6 + t * 143360, 0, 0, 4);
    BAR();
    GATEW(3);
    dma_lin(x, O_B6 + t * 143360, 0, 4, 92);
    lds_wait_ge(x.lds + L_CTL + C_PUB + 12, 8);
    dma_lin(x, O_B6 + t * 143360, 0, 92, 140);
    LANDED(0);
    STAMPL(42);
    BAR();
    dma_lin(x, O_B7 + t8 * 276480, 0, 0, 4);
    BAR();
    GATEW(4);
    dma_lin(x, O_B7 + t8 * 276480, 0, 4, 100);
    lds_wait_ge(x.lds + L_CTL + C_PUB + 16, 8);
    dma_lin(x, O_B7 + t8 * 276480, 0, 100, 140);
    LANDED(0);
    STAMPL(43);
    BAR();
    dma_lin(x, O_B7 + t8 * 276480 + 140 * 1024, 0, 0, 80);
    VMWAIT(0);
    BAR();
    dma_lin(x, O_B7 + t8 * 276480 + 140 * 1024, 0, 80, 130);
    VMWAIT(0);
    BAR();
    return;
  }
  bool last;
  if (b < 160) {
    phase_a(x, xin, xcd, i & 3, i >> 2);
    STAMP(2);
    publish(x, 0, F_A + (i >> 2) * 32 + xcd * 4 + (i & 3), 3);
    refresh(x);
    phase_b(x, b);
    STAMP(7);
    last = publish<8, 160>(x, 1, F_B + b, 8);
  } else {
    last = x.w == 0;
    if (last) poll<1, 8>(x, {F_B + xcd * 160 + i});
  }
  wait_b(x, last, xcd);
  STAMP(11);
  refresh(x);
  phase_c(x, P, t);
  STAMP(13);
  last = publish(x, 2, F_C + P * 16 + t, 14);
  wait_flags<1, DLY>(x, last, {F_C + (x.lane < 32 ? P * 16 + x.c : ((P + (hf ? 1 : 15)) & 15) * 16 + (x.lane & 7))}, 4);
  STAMP(15);
  refresh(x);
  u32 res[6];
  phase_5(x, P, hf, t8, res);
  STAMP(17);
  last = publish(x, 3, F_5 + P * 16 + t, 18);
  wait_flags<1, DLY>(x, last, {F_5 + P * 16 + x.c}, 5);
  STAMP(19);
  refresh(x);
  phase_6(x, P, t);
  STAMP(21);
  last = publish(x, 4, F_6 + P * 16 + t, 22);
  wait_flags<1, DLY>(x, last, {F_6 + P * 16 + x.c}, 6);
  STAMP(23);
  refresh(x);
  phase_7(x, P, hf, t8, res, out);
  STAMP(25);
  if (b == 0 && x.tid == 0) __hip_atomic_store(ep, x.tag(), __ATOMIC_RELAXED, __HIP_MEMORY_SCOPE_AGENT);
}

#define WSOFF 0
extern "C" void kernel_launch(void* const* d_in, const int* in_sizes, int n_in, void* d_out, int out_size, void* d_ws,
                              size_t ws_size, hipStream_t stream) {
  (void)hipFuncSetAttribute((const void*)fused, hipFuncAttributeMaxDynamicSharedMemorySize, LDS_BYTES);
  fused<<<256, NTHR, LDS_BYTES, stream>>>((const u16*)d_in[0], (const u16*)d_in[1], (const u16*)d_in[2], (const u16*)d_in[4],
                                          (const u16*)d_in[5], (const u16*)d_in[6], (const u16*)d_in[7], (u16*)d_out, (char*)d_ws + WSOFF);
}
```

```cpp
#define DIAGT 4500
#include <hip/hip_runtime.h>
#include <stdint.h>

typedef unsigned u32;
typedef unsigned long long u64;
typedef unsigned short u16;
typedef u32 v4u __attribute__((ext_vector_type(4)));
typedef u32 v2u __attribute__((ext_vector_type(2)));
typedef float f32x4 __attribute__((ext_vector_type(4)));
typedef float f32x2 __attribute__((ext_vector_type(2)));
typedef __bf16 bf16x8 __attribute__((ext_vector_type(8)));
typedef __bf16 bf16x2 __attribute__((ext_vector_type(2)));
typedef short s16x4 __attribute__((ext_vector_type(4)));
typedef __amdgpu_buffer_rsrc_t rsrc_t;
#define LAS __attribute__((address_space(3)))
#define GAS __attribute__((address_space(1)))
#define DI __device__ __forceinline__

constexpr int ACT = 2048 * 640 * 2;
constexpr int O_EPOCH = 0;
constexpr int O_FLAG = 4096;
enum { F_A = 0, F_W = 160, F_C = 240, F_5 = 496, F_6 = 752, F_S = 1008, F_T = F_S + 8 * 96, F_B = F_T + 8 * 96, F_END = F_B + 8 * 160 };
__host__ __device__ constexpr int flag_at(int i) { return O_FLAG + i * 256; }
constexpr int O_SIG = 2 << 20;
static_assert(flag_at(F_END) <= O_SIG, "flag area");
constexpr int O_MU = O_SIG + 5 * 8192;
constexpr int O_CS3 = O_MU + 8192;
constexpr int O_ST1 = O_CS3 + 20 * 1792 * 4;
constexpr int O_ZT = O_ST1 + 2048 * 128;
constexpr int O_WT = O_ZT + ACT;
constexpr int O_HB = O_WT + ACT;
constexpr int O_BC = O_HB + ACT;
constexpr int O_B5 = O_BC + 16 * 102400 + 8192;
constexpr int O_B6 = O_B5 + 8 * 102400 + 8192;
constexpr int O_B7 = O_B6 + 16 * 143360 + 8192;
constexpr int O_G = O_B7 + 8 * 276480 + 8192;
constexpr int O_XB = O_G + ACT;
constexpr int O_X1 = O_XB + ACT;
constexpr int O_U = O_X1 + ACT;
constexpr int WS_END = O_U + 2048 * 1728 * 2;

constexpr int LDS_BYTES = 160 * 1024;
constexpr int L_MISC = 156 * 1024;
constexpr int L_CTL = 159 * 1024;
constexpr int L_TJ = 84 * 1024;
constexpr int L_XS = 116 * 1024;
constexpr int L_XB = 132 * 1024;
enum { C_SB = 4, C_GO = 8, C_LAND = 12, C_LGO = 16, C_GOW = 20, C_SB0 = 24, C_SB1 = 28, C_PUB = 32 };
constexpr int NTHR = 768;
constexpr float EPS = 1e-5f;
constexpr int SC1 = 16;

DI u32 pk(float a, float b) { f32x2 v = {a, b}; return __builtin_bit_cast(u32, __builtin_convertvector(v, bf16x2)); }
DI float blo(u32 v) { return __builtin_bit_cast(float, v << 16); }
DI float bhi(u32 v) { return __builtin_bit_cast(float, v & 0xffff0000u); }
DI f32x4 mfma(v4u a, v4u b, f32x4 c) {
  return __builtin_amdgcn_mfma_f32_16x16x32_bf16(__builtin_bit_cast(bf16x8, a), __builtin_bit_cast(bf16x8, b), c, 0, 0, 0);
}
DI float dot2(u32 a, u32 b, float c) {
  return __builtin_amdgcn_fdot2_f32_bf16(__builtin_bit_cast(bf16x2, a), __builtin_bit_cast(bf16x2, b), c, false);
}
DI float dot8(v4u a, v4u b, float c) { return dot2(a.w, b.w, dot2(a.z, b.z, dot2(a.y, b.y, dot2(a.x, b.x, c)))); }
DI v2u trread(LAS char* p) { return __builtin_bit_cast(v2u, __builtin_amdgcn_ds_read_tr16_b64_v4i16((LAS s16x4*)p)); }
DI float cosr(float rev) { return __builtin_amdgcn_cosf(rev); }
DI float sinr(float rev) { return __builtin_amdgcn_sinf(rev); }
DI float gelu(float x) {
  const float k0 = -2.302208198f, k1 = -0.1029432397f;
  float e = __builtin_amdgcn_exp2f(x * (k0 + k1 * x * x));
  return x * __builtin_amdgcn_rcpf(1.f + e);
}
DI void dma16(const void* gsrc, u32 lds_dst) {
  u32 keep;
  asm volatile("s_mov_b32 %0, m0\n\ts_mov_b32 m0, %2\n\ts_nop 0\n\tglobal_load_lds_dwordx4 %1, off nt\n\ts_mov_b32 m0, %0"
               : "=&s"(keep) : "v"(gsrc), "s"(lds_dst) : "memory");
}
DI void dma16_sc1(const void* gsrc, u32 lds_dst) {
  u32 keep;
  asm volatile("s_mov_b32 %0, m0\n\ts_mov_b32 m0, %2\n\ts_nop 0\n\tglobal_load_lds_dwordx4 %1, off sc1\n\ts_mov_b32 m0, %0"
               : "=&s"(keep) : "v"(gsrc), "s"(lds_dst) : "memory");
}
#define VMWAIT(n) asm volatile("s_waitcnt vmcnt(" #n ")" ::: "memory")
#define BAR() asm volatile("s_waitcnt lgkmcnt(0)\n\ts_barrier" ::: "memory")
#define SCHED() __builtin_amdgcn_sched_barrier(0)
DI v4u ld16(rsrc_t r, int voff, int soff) { return __builtin_amdgcn_raw_buffer_load_b128(r, voff, soff, SC1); }
DI v2u ld8(rsrc_t r, int voff, int soff) { return __builtin_amdgcn_raw_buffer_load_b64(r, voff, soff, SC1); }
DI u32 ld4(rsrc_t r, int voff, int soff) { return __builtin_amdgcn_raw_buffer_load_b32(r, voff, soff, SC1); }
DI void st16(rsrc_t r, v4u v, int voff, int soff) { __builtin_amdgcn_raw_buffer_store_b128(v, r, voff + soff, 0, SC1); }
DI void st8(rsrc_t r, v2u v, int voff, int soff) { __builtin_amdgcn_raw_buffer_store_b64(v, r, voff, soff, SC1); }
DI void st4(rsrc_t r, u32 v, int voff, int soff) { __builtin_amdgcn_raw_buffer_store_b32(v, r, voff, soff, SC1); }
DI float xsum16_32(float v) {
  float u = v;
  asm volatile("s_nop 1\n\tv_permlane16_swap_b32 %0, %1" : "+v"(u), "+v"(v));
  v += u; u = v;
  asm volatile("s_nop 1\n\tv_permlane32_swap_b32 %0, %1" : "+v"(u), "+v"(v));
  return v + u;
}
template <int CTRL> DI float dppadd(float v) {
  return v + __builtin_bit_cast(float, __builtin_amdgcn_mov_dpp(__builtin_bit_cast(int, v), CTRL, 0xf, 0xf, true));
}
DI float xsum8(float v) { return dppadd<0x141>(dppadd<0x4E>(dppadd<0xB1>(v))); }
#define PIN(v) asm volatile("" : "+v"(v))

#define STAMP(i)
#define STAMPA(i)
#define STAMPW(i)
#define STAMPL(i)
struct Ctx {
  char* ws; rsrc_t rs; LAS char* lds; u64 epoch;
  int tid, lane, w, g, c;
  DI u64 tag() const { u64 e = epoch; asm volatile("" : "+v"(e)); return e + 0x9E3779B97F4A7C15ull; }
};
DI void refresh(Ctx& x) {
  int l = x.lane;
  asm volatile("" : "+v"(l));
  x.lane = l; x.g = l >> 4; x.c = l & 15;
}
DI LAS char* pinp(LAS char* p) { asm volatile("" : "+v"(p)); return p; }
DI u32 lds_ld(LAS char* p) { return __hip_atomic_load((LAS u32*)p, __ATOMIC_RELAXED, __HIP_MEMORY_SCOPE_WORKGROUP); }
#define LSLP 1
#define PSLP 1
DI void lds_wait_ge(LAS char* p, u32 want) {
#pragma unroll 1
  for (int spin = 0; spin < (1 << 20); ++spin) {
    if (__builtin_amdgcn_readfirstlane(lds_ld(p)) >= want) break;
    __builtin_amdgcn_s_sleep(LSLP);
  }
  asm volatile("" ::: "memory");
}
DI void lds_inc(const Ctx& x, int word) {
  if (x.lane == 0) __hip_atomic_fetch_add((LAS u32*)(x.lds + L_CTL + word), 1u, __ATOMIC_RELAXED, __HIP_MEMORY_SCOPE_WORKGROUP);
}
template <int REP = 1, int STR = 0> DI bool publish(const Ctx& x, int idx, int flag, int stamp, int NW = 8) {
  __builtin_amdgcn_s_waitcnt(0x0070);
  asm volatile("s_waitcnt vmcnt(0) lgkmcnt(0)" ::: "memory");
  u32 old = 0;
  if (x.lane == 0) old = __hip_atomic_fetch_add((LAS u32*)(x.lds + L_CTL + C_PUB + 4 * idx), 1u, __ATOMIC_RELAXED, __HIP_MEMORY_SCOPE_WORKGROUP);
  const bool last = __builtin_amdgcn_readfirstlane(old) == NW - 1;
  if (last) {
    if (x.lane < 8 * REP)
      __hip_atomic_store((GAS u64*)(x.ws + flag_at(flag + (x.lane >> 3) * STR) + (x.lane & 7) * 8), x.tag(), __ATOMIC_RELAXED, __HIP_MEMORY_SCOPE_AGENT);
    STAMPW(stamp);
  }
  asm volatile("" ::: "memory");
  return last;
}
DI void soft_barrier(const Ctx& x, int word, u32 target) {
  asm volatile("s_waitcnt lgkmcnt(0)" ::: "memory");
  lds_inc(x, word);
#pragma unroll 1
  for (int spin = 0; spin < (1 << 20); ++spin)
    if (__builtin_amdgcn_readfirstlane(lds_ld(x.lds + L_CTL + word)) >= target) break;
  asm volatile("" ::: "memory");
}
template <int N, int SLP = PSLP> DI void poll(const Ctx& x, const int (&off)[N]) {
  const u64 tag = x.tag();
#pragma unroll 1
  for (int spin = 0; spin < (1 << 16); ++spin) {
    u64 v[N];
#pragma unroll
    for (int i = 0; i < N; ++i) v[i] = __hip_atomic_load((GAS u64*)(x.ws + flag_at(off[i])), __ATOMIC_RELAXED, __HIP_MEMORY_SCOPE_AGENT);
    bool ok = true;
#pragma unroll
    for (int i = 0; i < N; ++i) ok &= v[i] == tag;
    if (__all(ok)) break;
    __builtin_amdgcn_s_sleep(SLP);
  }
  __builtin_amdgcn_fence(__ATOMIC_ACQUIRE, "wavefront");
  asm volatile("" ::: "memory");
}
DI void lds_st(LAS char* p, u32 v) { __hip_atomic_store((LAS u32*)p, v, __ATOMIC_RELAXED, __HIP_MEMORY_SCOPE_WORKGROUP); }
template <int N, int DELAY> DI void wait_flags(const Ctx& x, bool poller, const int (&off)[N], u32 gen) {
  if (poller) {
    if (DELAY) __builtin_amdgcn_s_sleep(DELAY);
    poll<N>(x, off);
    lds_st(x.lds + L_CTL + C_GO, gen);
  } else lds_wait_ge(x.lds + L_CTL + C_GO, gen);
}
#define LANDED(n) do { VMWAIT(n); lds_inc(x, C_LAND); } while (0)
DI void wait_landed(const Ctx& x, u32 n) { lds_wait_ge(x.lds + L_CTL + C_LAND, 4 * n); }

template <int GP> DI void group_dst(int gam, int h, int& tile, int& nb, bool& single) {
  int sup = gam / (2 * GP + 1), e = gam - sup * (2 * GP + 1);
  single = (e == GP);
  tile = 2 * sup + (e > GP ? 1 : (single ? h : 0));
  nb = single ? 2 * GP : 2 * (e > GP ? e - GP - 1 : e) + h;
}

DI v4u pack8(const float (&v)[8]) { return v4u{pk(v[0], v[1]), pk(v[2], v[3]), pk(v[4], v[5]), pk(v[6], v[7])}; }
DI v4u pack8(f32x4 a, f32x4 b) { return v4u{pk(a[0], a[1]), pk(a[2], a[3]), pk(b[0], b[1]), pk(b[2], b[3])}; }
DI void gen_q(int kap, int g, int n0, v4u (&qc)[10], v4u (&qs)[10]) {
  const int k = kap ? kap : 320;
  const float ac = kap ? 0.05590169944f : 0.03952847075f, as = kap ? -0.05590169944f : 0.f;
  const float kf = (float)k * (1.f / 640.f), a0 = (float)(((n0 + 8 * g) * k) % 640) * (1.f / 640.f);
  float c0[8], c1[8], s0[8], s1[8];
#pragma unroll
  for (int j = 0; j < 8; ++j) {
    const float u = a0 + (float)j * kf, v = a0 + (float)(32 + j) * kf;
    c0[j] = ac * cosr(u); s0[j] = as * sinr(u); c1[j] = ac * cosr(v); s1[j] = as * sinr(v);
  }
  const float c2 = 2.f * cosr(32.f * kf);
#pragma unroll
  for (int ks = 0; ks < 10; ks += 2) {
    qc[ks] = pack8(c0); qs[ks] = pack8(s0); qc[ks + 1] = pack8(c1); qs[ks + 1] = pack8(s1);
    if (ks + 2 < 10) {
#pragma unroll
      for (int j = 0; j < 8; ++j) {
        c0[j] = c2 * c1[j] - c0[j]; c1[j] = c2 * c0[j] - c1[j];
        s0[j] = c2 * s1[j] - s0[j]; s1[j] = c2 * s0[j] - s1[j];
      }
    }
  }
#pragma unroll
  for (int i = 0; i < 10; ++i) { PIN(qc[i]); PIN(qs[i]); }
}

#define XF 4
DI void phase_a(const Ctx& x, const u16* xin, int mm, int q4, int nt) {
  const int w = x.w, lane = x.lane, g = x.g, c = x.c;
  const int lam = 8 * w + (lane >> 3);
  const int srow = 256 * mm + 32 * (4 * ((lam >> 4) & 1) + (lam & 3)) + 8 * q4 + 4 * (lam >> 5) + ((lam >> 2) & 3);
  const char* src = (const char*)xin + srow * 1280 + (((lane & 7) ^ (lam & 7)) << 4);
  auto fetch = [&](int k0, int k1) {
#pragma unroll
    for (int kc = k0; kc < k1; ++kc) {
      const int kr = kc + 2 * nt;
      dma16(src + (kr < 10 ? kr : kr - 10) * 128, (u32)(uintptr_t)x.lds + (kc * 8 + w) * 1024);
    }
  };
  fetch(0, XF);
  v4u q[10];
  {
    const int slot = 128 * nt + 16 * w + c, k = slot ? slot >> 1 : 320;
    const float amp = slot > 1 ? 0.05590169944f : slot ? 0.f : 0.03952847075f;
    const float kf = (float)k * (1.f / 640.f), a0 = (float)(((128 * nt + 8 * g) * k) % 640) * (1.f / 640.f) + ((c & 1) ? 0.25f : 0.f);
    float v0[8], v1[8];
#pragma unroll
    for (int j = 0; j < 8; ++j) { v0[j] = amp * cosr(a0 + (float)j * kf); v1[j] = amp * cosr(a0 + (float)(32 + j) * kf); }
    const float c2 = 2.f * cosr(32.f * kf);
#pragma unroll
    for (int ks = 0; ks < 10; ks += 2) {
      q[ks] = pack8(v0); q[ks + 1] = pack8(v1);
      if (ks + 2 < 10) {
#pragma unroll
        for (int j = 0; j < 8; ++j) { v0[j] = c2 * v1[j] - v0[j]; v1[j] = c2 * v0[j] - v1[j]; }
      }
    }
#pragma unroll
    for (int i = 0; i < 10; ++i) PIN(q[i]);
  }
  fetch(XF, 10);
  f32x4 acc[2][4];
#pragma unroll
  for (int i = 0; i < 8; ++i) acc[i >> 2][i & 3] = f32x4{0.f, 0.f, 0.f, 0.f};
  LAS char* a0 = x.lds + (c >> 3) * 1024 + (c & 7) * 128 + ((g ^ (c & 7)) << 4);
  LAS char* a1 = x.lds + (c >> 3) * 1024 + (c & 7) * 128 + (((g + 4) ^ (c & 7)) << 4);
  v4u f[8];
  SCHED();
  STAMP(36);
  VMWAIT(8); BAR(); STAMP(1);
#pragma unroll
  for (int i = 0; i < 8; ++i) f[i] = *(LAS v4u*)((i < 4 ? a0 : a1) + (i & 3) * 2048);
#pragma unroll
  for (int kc = 0; kc < 10; ++kc) {
    if (kc == 1) { VMWAIT(5); asm volatile("s_barrier" ::: "memory"); }
    if (kc == 4) { VMWAIT(0); asm volatile("s_barrier" ::: "memory"); }
    if (kc == 4) { a0 = pinp(a0 + 40960); a1 = pinp(a1 + 40960); }
    SCHED();
#pragma unroll
    for (int i = 0; i < 8; ++i) {
      acc[kc / 5][i & 3] = mfma(f[i], q[(2 * kc + (i >> 2)) % 10], acc[kc / 5][i & 3]);
      if (kc < 9) f[i] = *(LAS v4u*)((i < 4 ? a0 : a1) + ((kc + 1) % 5) * 8192 + (i & 3) * 2048);
      SCHED();
    }
  }
  STAMP(37);
  STAMPA(L_CTL + 256 + (x.w == 7 ? 58 * 4 : 256));
  const float sg = (c & 2) ? -1.f : 1.f;
#pragma unroll
  for (int i = 0; i < 4; ++i) acc[0][i] += sg * acc[1][i];
  BAR();
  LAS char* rp = pinp(x.lds + L_MISC + 1024 + 4 * g * 4);
  const int off = O_ZT + ((nt * 32 + mm * 4 + q4) * 32 + 4 * w + (c >> 2)) * 512 + (c & 3) * 64 + g * 16;
#pragma unroll
  for (int u = 0; u < 2; ++u)
    st16(x.rs, pack8(acc[0][2 * u] * *(LAS f32x4*)(rp + u * 128), acc[0][2 * u + 1] * *(LAS f32x4*)(rp + u * 128 + 64)), off, u * 256);
  if (w == 0 || (w == 1 && nt == 0))
    st4(x.rs, *(LAS u32*)(x.lds + L_MISC + 1280 + w * 256 + lane * 4), (w ? O_MU : O_SIG + nt * 8192) + ((mm * 4 + q4) * 64 + lane) * 4, 0);
}

DI void phase_a_stats(const Ctx& x) {
  const int lw = x.w & 3, g = x.g, c = x.c;
  LAS char* a0 = x.lds + lw * 2048 + (c >> 3) * 1024 + (c & 7) * 128 + ((g ^ (c & 7)) << 4);
  LAS char* a1 = x.lds + lw * 2048 + (c >> 3) * 1024 + (c & 7) * 128 + (((g + 4) ^ (c & 7)) << 4);
  const v4u ones = {0x3f803f80u, 0x3f803f80u, 0x3f803f80u, 0x3f803f80u};
  f32x4 gr = {0.f, 0.f, 0.f, 0.f}, sm = gr;
  __builtin_amdgcn_s_setprio(2);
#pragma unroll
  for (int st = 0; st < 3; ++st) {
    constexpr int lo[4] = {0, 2, 5, 10};
    STAMPL(54 + st);
    BAR();
    STAMPL(51 + st);
    if (st == 2) { a0 = pinp(a0 + 40960); a1 = pinp(a1 + 40960); }
    v4u f[10];
#pragma unroll
    for (int kc = lo[st]; kc < lo[st + 1]; ++kc) {
      f[2 * (kc - lo[st])] = *(LAS v4u*)(a0 + (kc % 5) * 8192);
      f[2 * (kc - lo[st]) + 1] = *(LAS v4u*)(a1 + (kc % 5) * 8192);
    }
#pragma unroll
    for (int j = 0; j < 2 * (lo[st + 1] - lo[st]); ++j) { gr = mfma(f[j], f[j], gr); sm = mfma(ones, f[j], sm); }
    SCHED();
  }
  STAMPL(57);
  const int r = c & 3;
  const float q = xsum16_32((c >> 2) == g ? (r == 0 ? gr[0] : r == 1 ? gr[1] : r == 2 ? gr[2] : gr[3]) : 0.f);
  const float mu = sm[0] * (1.f / 640.f);
  const float var = fmaxf(q * (1.f / 640.f) - mu * mu, 0.f) + EPS;
  const float rstd = __builtin_amdgcn_rsqf(var);
  if (g < 3) *(LAS float*)(x.lds + L_MISC + 1024 + g * 256 + (16 * lw + c) * 4) = g == 0 ? rstd : g == 1 ? var * rstd : mu;
  __builtin_amdgcn_s_setprio(0);
  STAMPL(47);
  BAR();
}
DI int stat_at(int n1, int n2) {
  return ((n1 >> 3) * 4 + (n2 >> 3)) * 64 + (2 * ((n2 >> 2) & 1) + ((n1 >> 2) & 1)) * 16 + 4 * (n2 & 3) + (n1 & 3);
}

DI void side_wt(const Ctx& x, const u16* wr, const u16* wi, int v, int lo, int sb, int pub) {
  const int fq = v >> 3, fg = v & 7, lw = x.w & 3;
#pragma unroll
  for (int i = 0; i < 2; ++i) {
    const char* src = (const char*)(i ? wi : wr) + (4 * fq + lw) * 41088 + fg * 5136;
#pragma unroll
    for (int pc = 0; pc < 6; ++pc) dma16(src + min(pc * 64 + x.lane, 320) * 16, (u32)(uintptr_t)x.lds + lo + (lw * 2 + i) * 6144 + pc * 1024);
  }
  VMWAIT(0);
  soft_barrier(x, sb, 4);
  STAMPL(48);
  const int c8 = x.lane & 7, kk = x.lane >> 3;
  const int rho = (fg >> 1) * 128 + fq * 16 + (fg & 1) * 8 + c8;
#pragma unroll 2
  for (int it = 0; it < 10; ++it) {
    const int j = it * 4 + lw, kap = (j >> 3) * 64 + kk * 8 + (j & 7);
    LAS const u16* sp = (LAS const u16*)(x.lds + lo) + c8 * 321 + (kap ? kap : 320);
    v4u o;
#pragma unroll
    for (int r = 0; r < 4; ++r) o[r] = (u32)sp[r * 6144] | ((u32)sp[r * 6144 + 3072] << 16);
    st16(x.rs, o, O_WT + kap * 8192 + rho * 16, 0);
  }
  STAMPL(46);
  publish(x, pub, F_W + v, 2, 4);
}
template <int GP> DI int blk_addr(const Ctx& x, int gam0, int rb, int& tile, int& nb) {
  const int p = x.c & 3, r0 = 8 * x.g + (x.c >> 2);
  bool single;
  group_dst<GP>(gam0 + (rb >> 1), rb & 1, tile, nb, single);
  const int ch = (rb >> 1) * 4 + (single ? 2 * (rb & 1) + (p >> 1) : p), hb = single ? (p & 1) : (rb & 1);
  return r0 * 128 + ((ch ^ (((r0 >> 1) & 1) << 2)) << 4) + hb * 8;
}
DI void tile_pos(int j, int& ks, int& ct) {
  const bool is3 = j < 540;
  const int jj = j - (is3 ? 0 : j < 740 ? 540 : 740);
  ks = (jj * (is3 ? 2428 : 6554)) >> 16;
  ct = jj - ks * (is3 ? 27 : 10);
}
DI void tile_dma(const Ctx& x, int j, int lds_off, const u16* w2, const u16* w3, const u16* w4) {
  const int row = x.lane >> 3, ch = ((x.lane & 7) ^ (((row >> 1) & 1) << 2)) << 4, rs = j < 540 ? 3456 : 1280;
  int ks, ct;
  tile_pos(j, ks, ct);
  const char* src = (const char*)(j < 540 ? w3 : j < 740 ? w2 : w4) + (ks * 32 * rs + ct * 128) + (row * rs + ch);
#pragma unroll
  for (int i = 0; i < 4; ++i) dma16(src + i * 8 * rs, (u32)(uintptr_t)x.lds + lds_off + i * 1024);
}
DI void tile_out(const Ctx& x, int j, int lds_off) {
  const bool is3 = j < 540;
  const int GP = is3 ? 3 : 2, tb = is3 ? 143360 : j < 740 ? 102400 : 276480, base = is3 ? O_B6 : j < 740 ? O_B5 : O_B7;
  int ks, ct;
  tile_pos(j, ks, ct);
  const int p = x.c & 3, r0 = 8 * x.g + (x.c >> 2), sw = ((r0 >> 1) & 1) << 6;
  LAS char* lp = x.lds + lds_off + r0 * 128;
  const v4u ones = {0x3f803f80u, 0x3f803f80u, 0x3f803f80u, 0x3f803f80u};
  v4u f[4];
  int dst[4];
#pragma unroll
  for (int rb = 0; rb < 4; ++rb) {
    const int gam = 2 * ct + (rb >> 1), h = rb & 1, sup = (gam * (is3 ? 37 : 52)) >> 8, e = gam - sup * (2 * GP + 1);
    const bool single = e == GP, hi = e > GP;
    dst[rb] = (2 * sup + (int)(hi || (single && h))) * tb + (ks * (2 * GP + 1) + (single ? 2 * GP : 2 * (e - (hi ? GP + 1 : 0)) + h)) * 1024;
    LAS char* ad = lp + (((rb >> 1) * 64 + (single ? h * 32 + p * 8 : p * 16 + h * 8)) ^ sw);
    const v2u lo = trread(ad), hi2 = trread(ad + 512);
    f[rb] = v4u{lo.x, lo.y, hi2.x, hi2.y};
  }
#pragma unroll
  for (int rb = 0; rb < 4; ++rb) st16(x.rs, f[rb], x.lane * 16, base + dst[rb]);
  if (is3) {
#pragma unroll
    for (int rb = 0; rb < 4; ++rb) {
      const f32x4 s = mfma(f[rb], ones, f32x4{0.f, 0.f, 0.f, 0.f});
      if (x.c == 0) st16(x.rs, __builtin_bit_cast(v4u, s), x.g * 16, O_CS3 + (dst[rb] >> 4));
    }
  }
}
DI void side_qc(const Ctx& x, int job, int njobs) {
#pragma unroll 1
  for (int pc = job; pc < 800; pc += njobs) {
    const int ks = pc / 40, blk = pc - ks * 40;
    int tile, nb; bool single;
    group_dst<2>(blk >> 1, blk & 1, tile, nb, single);
    const int n = (blk >> 1) * 32 + (single ? 16 * (blk & 1) + x.c : 8 * (x.c >> 2) + 4 * (blk & 1) + (x.c & 3));
    const float a0 = (float)((n * (16 * ks + 4 * x.g)) % 640) * (1.f / 640.f), da = (float)n * (1.f / 640.f);
    v4u f;
#pragma unroll
    for (int i = 0; i < 4; ++i) {
      const float a = a0 + (float)i * da;
      f[i] = pk(0.05590169944f * cosr(a), -0.05590169944f * sinr(a));
    }
    if (ks == 0 && x.g == 0) f[0] = pk((n & 1) ? -0.03952847075f : 0.03952847075f, 0.f);
    st16(x.rs, f, x.lane * 16, O_BC + (8 + tile) * 102400 + (ks * 5 + nb) * 1024);
  }
}
DI void side_w1p(const Ctx& x, const u16* w1, int ct, int nt) {
  {
    const int row = 8 * x.w + (x.lane >> 3);
    const char* src = (const char*)w1 + row * 1280 + ct * 128 + (((x.lane & 7) ^ (((row >> 1) & 1) << 2)) << 4);
#pragma unroll
    for (int i = 0; i < 10; ++i) {
      const int kr = i + 2 * nt;
      dma16(src + (kr < 10 ? kr : kr - 10) * 64 * 1280, (u32)(uintptr_t)x.lds + (i * 8 + x.w) * 1024);
    }
  }
  v4u qc[10], qs[10];
  gen_q(64 * nt + 16 * (x.w & 3) + x.c, x.g, 128 * nt, qc, qs);
  f32x4 acc[2][2][2];
#pragma unroll
  for (int i = 0; i < 8; ++i) acc[i >> 2][(i >> 1) & 1][i & 1] = f32x4{0.f, 0.f, 0.f, 0.f};
  int addr[2], tile[2], nb[2];
#pragma unroll
  for (int rb = 0; rb < 2; ++rb) addr[rb] = blk_addr<2>(x, 2 * ct, 2 * (x.w >> 2) + rb, tile[rb], nb[rb]);
  SCHED();
  VMWAIT(0);
  soft_barrier(x, C_SB, 8);
  STAMP(1);
  v2u lo[4], hi[4];
#pragma unroll
  for (int i = 0; i < 4; ++i) { lo[i] = trread(x.lds + addr[i & 1] + (i >> 1) * 4096); hi[i] = trread(x.lds + addr[i & 1] + (i >> 1) * 4096 + 512); }
#pragma unroll
  for (int kc = 0; kc < 10; ++kc) {
    if (kc == 4) { addr[0] += 40960; addr[1] += 40960; PIN(addr[0]); PIN(addr[1]); }
#pragma unroll
    for (int i = 0; i < 4; ++i) {
      const int qi = (2 * kc + (i >> 1)) % 10;
      const v4u f = {lo[i].x, lo[i].y, hi[i].x, hi[i].y};
      acc[kc / 5][i & 1][0] = mfma(qc[qi], f, acc[kc / 5][i & 1][0]);
      acc[kc / 5][i & 1][1] = mfma(qs[qi], f, acc[kc / 5][i & 1][1]);
      if (kc < 9) {
        LAS char* ad = x.lds + addr[i & 1] + ((kc + 1) % 5) * 8192 + (i >> 1) * 4096;
        lo[i] = trread(ad); hi[i] = trread(ad + 512);
      }
      SCHED();
    }
  }
  const f32x4 sg = {1.f, -1.f, 1.f, -1.f};
#pragma unroll
  for (int rb = 0; rb < 2; ++rb) {
    const f32x4 re = acc[0][rb][0] + sg * acc[1][rb][0], im = acc[0][rb][1] + sg * acc[1][rb][1];
    st16(x.rs, v4u{pk(re[0], im[0]), pk(re[1], im[1]), pk(re[2], im[2]), pk(re[3], im[3])}, x.lane * 16,
         O_BC + tile[rb] * 102400 + ((4 * nt + (x.w & 3)) * 5 + nb[rb]) * 1024);
  }
}
#define TGATE 0
DI void side_jobs(const Ctx& x, int xcd, int sl, const u16* wr, const u16* wi, const u16* w1, const u16* w2, const u16* w3, const u16* w4) {
  const int y = sl < 2 ? sl * 8 + xcd : (sl < 7 && xcd >= 2) ? 16 + (sl - 2) * 6 + (xcd - 2) : -1;
  const int ct = sl >= 7 ? xcd : 8 + xcd, nt = sl >= 7 ? sl - 7 : sl - 2;
  const u64 t0 = __builtin_amdgcn_s_memrealtime();
  BAR();
  if (y < 0 && x.w < 8) {
    side_w1p(x, w1, ct, nt);
    STAMP(5);
    publish<8, 96>(x, 1, F_S + sl * 8 + xcd, 3);
    return;
  }
  const bool two = y >= 18;
  if (y >= 0 && (x.w >= 8 || (!two && x.w < 4))) {
    const bool ld = x.w >= 8;
    side_wt(x, wr, wi, ld ? y : 46 + y, ld ? 0 : 49152, ld ? C_SB0 : C_SB1, ld ? 0 : 5);
  } else {
    if (y >= 0) {
      side_qc(x, two ? 72 + (y - 18) * 8 + x.w : y * 4 + x.w - 4, 296);
      publish<8, 96>(x, 1, F_S + sl * 8 + xcd, 3, two ? 8 : 4);
    }
    lds_wait_ge(x.lds + L_CTL + (y >= 0 ? C_PUB : C_SB), y >= 0 ? 4 : 8);
    const int n = y < 0 ? 4 : two ? 2 : 1;
    const int j = y < 0 ? ((ct * 5 + nt) * 4 + (x.w & 3)) * 4 : two ? 800 + ((y - 18) * 8 + x.w) * 2 : min(1248 + y * 4 + x.w - 4, 1279);
    const int lo = y < 0 ? L_TJ + (x.w & 3) * 16384 : two ? 49152 + x.w * 8192 : 81920 + x.w * 4096;
    STAMPW(59);
#pragma unroll 1
    for (int k = 0; k < n; ++k) tile_dma(x, j + k, lo + k * 4096, w2, w3, w4);
    VMWAIT(0);
    STAMPW(61);
#pragma unroll 1
    for (int k = 0; k < 64 && (u32)(__builtin_amdgcn_s_memrealtime() - t0) < TGATE; ++k) __builtin_amdgcn_s_sleep(4);
#pragma unroll 1
    for (int k = 0; k < n; ++k) tile_out(x, j + k, lo + k * 4096);
    STAMPW(62);
  }
  publish<8, 96>(x, 6, F_T + sl * 8 + xcd, 47, y < 0 ? 4 : 12);
  if (x.w >= 8) {
    lds_wait_ge(x.lds + L_CTL + C_PUB + 24, y < 0 ? 4 : 12);
    if (y < 0) lds_wait_ge(x.lds + L_CTL + C_PUB + 4, 8);
  }
}

DI v4u negv(v4u a) { return a ^ 0x80008000u; }
DI void phase_b(const Ctx& x, int p) {
  const int w = x.w, lane = x.lane, g = x.g, c = x.c, wq = w & 3, col = w >> 2;
  v4u C64[2], S64[2], N64[2], C32[2], S32[2], N32[2];
  f32x4 tc[2], ts[2];
  {
    const float f1 = (float)(16 * wq + c);
#pragma unroll
    for (int ks = 0; ks < 2; ++ks) {
      float cv[8], sv[8];
#pragma unroll
      for (int j = 0; j < 8; ++j) { const float a = f1 * (float)(32 * ks + 8 * g + j) * (1.f / 64.f); cv[j] = cosr(a); sv[j] = sinr(a); }
      C64[ks] = v4u{pk(cv[0], cv[1]), pk(cv[2], cv[3]), pk(cv[4], cv[5]), pk(cv[6], cv[7])};
      S64[ks] = v4u{pk(sv[0], sv[1]), pk(sv[2], sv[3]), pk(sv[4], sv[5]), pk(sv[6], sv[7])};
      N64[ks] = negv(S64[ks]);
#pragma unroll
      for (int j = 0; j < 8; ++j) {
        const float a = (float)(16 * ks + c) * (float)(16 * (j >> 2) + 4 * g + (j & 3)) * (1.f / 32.f);
        cv[j] = cosr(a); sv[j] = sinr(a);
      }
      C32[ks] = v4u{pk(cv[0], cv[1]), pk(cv[2], cv[3]), pk(cv[4], cv[5]), pk(cv[6], cv[7])};
      S32[ks] = v4u{pk(sv[0], sv[1]), pk(sv[2], sv[3]), pk(sv[4], sv[5]), pk(sv[6], sv[7])};
      N32[ks] = negv(S32[ks]);
#pragma unroll
      for (int r = 0; r < 4; ++r) {
        const float a = f1 * (float)(16 * ks + 4 * g + r) * (1.f / 2048.f);
        tc[ks][r] = 0.02209708691f * cosr(a); ts[ks][r] = 0.02209708691f * sinr(a);
      }
    }
  }
  const int nt = p >> 5, kap = 2 * p + col;
#pragma unroll
  for (int i = 0; i < 2; ++i) { PIN(C64[i]); PIN(S64[i]); PIN(N64[i]); PIN(C32[i]); PIN(S32[i]); PIN(N32[i]); PIN(tc[i]); PIN(ts[i]); }
  STAMP(4);
  lds_wait_ge(x.lds + L_CTL + C_GOW, 1);
  v4u wv[2];
#pragma unroll
  for (int t = 0; t < 2; ++t) wv[t] = ld16(x.rs, O_WT + kap * 8192 + (wq * 2 + t) * 1024 + lane * 16, 0);
  const int zo = O_ZT + ((nt * 32 + 4 * w + (lane >> 5)) * 32 + (p & 31)) * 512 + (lane & 31) * 16;
  LAS char* xd = pinp(x.lds + L_XS + ((lane >> 2) & 3) * 4096 + (w >> 2) * 1024 + ((w & 3) * 16 + 4 * (lane >> 4) + (lane & 3)) * 16);
  LAS char* xs = pinp(x.lds + L_XS + col * 8192 + lane * 16);
  const int so = O_SIG + nt * 8192 + stat_at(16 * wq + 4 * g, 16 * col + c) * 4;
  STAMP(5);
  lds_wait_ge(x.lds + L_CTL + C_GO, 2);
  STAMP(6);
  const v4u l0 = ld16(x.rs, zo, 0), l1 = ld16(x.rs, zo, 32768);
  const f32x4 sg = __builtin_bit_cast(f32x4, ld16(x.rs, so, 0));
  *(LAS v4u*)xd = l0;
  *(LAS v4u*)(xd + 2048) = l1;
  soft_barrier(x, C_SB0, 8);
  STAMP(50);
  v4u xr[2][2], xi[2][2];
#pragma unroll
  for (int i = 0; i < 4; ++i) {
    xr[i >> 1][i & 1] = *(LAS v4u*)(xs + i * 1024);
    xi[i >> 1][i & 1] = *(LAS v4u*)(xs + 4096 + i * 1024);
  }
  SCHED();
  const f32x4 zero = {0.f, 0.f, 0.f, 0.f};
  f32x4 ar[2], ai[2], br[2], bi[2];
#pragma unroll
  for (int b = 0; b < 2; ++b) {
    ar[b] = mfma(xr[b][0], C64[0], zero); ai[b] = mfma(xi[b][0], C64[0], zero);
    ar[b] = mfma(xi[b][0], S64[0], ar[b]); ai[b] = mfma(xr[b][0], N64[0], ai[b]);
    ar[b] = mfma(xr[b][1], C64[1], ar[b]); ai[b] = mfma(xi[b][1], C64[1], ai[b]);
    ar[b] = mfma(xi[b][1], S64[1], ar[b]); ai[b] = mfma(xr[b][1], N64[1], ai[b]);
  }
#pragma unroll
  for (int b = 0; b < 2; ++b) { br[b] = ar[b] * tc[b] + ai[b] * ts[b]; bi[b] = ai[b] * tc[b] - ar[b] * ts[b]; }
  v4u pr = pack8(br[0], br[1]), pi = pack8(bi[0], bi[1]);
  STAMP(49);
#pragma unroll
  for (int t = 0; t < 2; ++t) {
    ar[t] = mfma(C32[t], pr, zero); ai[t] = mfma(C32[t], pi, zero);
    ar[t] = mfma(S32[t], pi, ar[t]); ai[t] = mfma(N32[t], pr, ai[t]);
  }
#pragma unroll
  for (int t = 0; t < 2; ++t)
#pragma unroll
    for (int r = 0; r < 4; ++r) {
      const float wre = blo(wv[t][r]), wim = bhi(wv[t][r]);
      br[t][r] = ar[t][r] * wre - ai[t][r] * wim;
      bi[t][r] = ar[t][r] * wim + ai[t][r] * wre;
    }
  pr = pack8(br[0], br[1]); pi = pack8(bi[0], bi[1]);
#pragma unroll
  for (int t = 0; t < 2; ++t) {
    ar[t] = mfma(C32[t], pr, zero); ai[t] = mfma(C32[t], pi, zero);
    ar[t] = mfma(N32[t], pi, ar[t]); ai[t] = mfma(S32[t], pr, ai[t]);
  }
  {
    LAS char* img = pinp(x.lds + L_XB + col * 9216 + (16 * wq + c) * 72 + g * 8);
#pragma unroll
    for (int t = 0; t < 2; ++t) {
      f32x4 ur = ar[t] * tc[t] - ai[t] * ts[t], ui = ai[t] * tc[t] + ar[t] * ts[t];
      *(LAS v2u*)(img + t * 32) = v2u{pk(ur[0], ur[1]), pk(ur[2], ur[3])};
      *(LAS v2u*)(img + 4608 + t * 32) = v2u{pk(ui[0], ui[1]), pk(ui[2], ui[3])};
    }
  }
  STAMP(38);
  soft_barrier(x, C_SB, 8);
  STAMP(39);
  LAS char* rd = pinp(x.lds + L_XB + (8 * g + (c >> 2)) * 72 + col * 32 + (c & 3) * 8);
#pragma unroll
  for (int cc = 0; cc < 2; ++cc) {
    ar[cc] = zero; ai[cc] = zero;
#pragma unroll
    for (int ks = 0; ks < 2; ++ks) {
      v2u r0 = trread(rd + cc * 9216 + ks * 2304), r1 = trread(rd + cc * 9216 + ks * 2304 + 288);
      v2u i0 = trread(rd + cc * 9216 + 4608 + ks * 2304), i1 = trread(rd + cc * 9216 + 4608 + ks * 2304 + 288);
      const v4u ur = {r0.x, r0.y, r1.x, r1.y}, ui = {i0.x, i0.y, i1.x, i1.y};
      ar[cc] = mfma(C64[ks], ur, ar[cc]); ai[cc] = mfma(C64[ks], ui, ai[cc]);
      ar[cc] = mfma(N64[ks], ui, ar[cc]); ai[cc] = mfma(S64[ks], ur, ai[cc]);
    }
  }
  v2u z[4];
  {
    LAS char* zp = pinp(x.lds + L_XS + (col * 2 + (wq >> 1)) * 1024 + ((2 * (wq & 1) + (g >> 1)) * 16 + c) * 16 + (g & 1) * 8);
#pragma unroll
    for (int i = 0; i < 4; ++i) z[i] = *(LAS v2u*)(zp + i * 4096);
  }
  if (p == 0) ai[0] = zero;
  u32 o[8];
#pragma unroll
  for (int r = 0; r < 4; ++r) {
    float zz[4];
#pragma unroll
    for (int i = 0; i < 4; ++i) zz[i] = (r & 1) ? bhi(z[i][r >> 1]) : blo(z[i][r >> 1]);
    o[2 * r] = pk(sg[r] * zz[0] + ar[0][r], sg[r] * zz[1] + ai[0][r]);
    o[2 * r + 1] = pk(sg[r] * zz[2] + ar[1][r], sg[r] * zz[3] + ai[1][r]);
  }
  const int ob = O_HB + p * 16384 + (((8 * wq + 2 * g) * 2 + col) * 16 + c) * 16;
  st16(x.rs, v4u{o[0], o[1], o[2], o[3]}, ob, 0);
  st16(x.rs, v4u{o[4], o[5], o[6], o[7]}, ob + 512, 0);
}

#define GATE 0
#define GATEW(i) do { if (GATE == 1) lds_wait_ge(x.lds + L_CTL + C_PUB + 4 * (i), 8); if (GATE == 2) lds_wait_ge(x.lds + L_CTL + C_GO, (i) + 2); } while (0)
#define DLY 6
#define DLYA 6
#define DLYB 10
#define PACE 6
#define STR2(x) #x
#define STR(x) STR2(x)
DI void dma_lin(const Ctx& x, int ws_off, int lds_off, int p0, int p1) {
  const char* src = x.ws + ws_off + x.lane * 16;
#pragma unroll 1
  for (int pc = p0 + (x.w & 3); pc < p1; pc += 4) {
    dma16_sc1(src + pc * 1024, (u32)(uintptr_t)x.lds + lds_off + pc * 1024);
    asm volatile("s_waitcnt vmcnt(" STR(PACE) ")" ::: "memory");
  }
}
DI int ft_pair(int rowblk, int KS, int gam, int g, int c) { return (rowblk * KS + gam) * 1024 + (g * 16 + c) * 16; }
DI int ft_single(int rowblk, int KS, int gam, int h, int g, int c) {
  return (rowblk * KS + gam) * 1024 + ((2 * h + (g >> 1)) * 16 + c) * 16 + (g & 1) * 8;
}

constexpr int L_EX128 = 100 * 1024, L_EX64 = 92 * 1024;
template <int NB, bool HPLANES>
DI void gemm128(const Ctx& x, int aoff, u32 land, f32x4 (&acc)[2][NB], float& ssq) {
  constexpr int LA = 4, EXW = NB == 5 ? 6 : 7;
  const int kw = x.w >> 2;
  v4u a[10][2];
  auto lda = [&](int it) {
    if (HPLANES) { a[it][0] = ld16(x.rs, aoff, it * 262144); a[it][1] = ld16(x.rs, aoff, it * 262144 + 16384); }
    else { a[it][0] = ld16(x.rs, aoff, it * 2048); a[it][1] = ld16(x.rs, aoff, it * 2048 + 20480); }
  };
#pragma unroll
  for (int it = 0; it < LA; ++it) lda(it);
  SCHED();
  wait_landed(x, land);
  STAMP(NB == 5 ? 26 : 28);
#pragma unroll
  for (int i = 0; i < 2; ++i)
#pragma unroll
    for (int n = 0; n < NB; ++n) acc[i][n] = f32x4{0.f, 0.f, 0.f, 0.f};
  f32x4 gr0 = {0.f, 0.f, 0.f, 0.f}, gr1 = gr0;
  LAS char* bp = x.lds + kw * NB * 1024 + x.lane * 16;
  v4u f[NB];
#pragma unroll
  for (int n = 0; n < NB; ++n) f[n] = *(LAS v4u*)(bp + n * 1024);
#pragma unroll
  for (int it = 0; it < 10; ++it) {
    SCHED();
    if (it + LA < 10) lda(it + LA);
    v4u a0 = a[it][0], a1 = a[it][1];
    if (HPLANES) {
      a0 = v4u{a[it][0].x, a[it][0].y, a[it][1].x, a[it][1].y};
      a1 = v4u{a[it][0].z, a[it][0].w, a[it][1].z, a[it][1].w};
    }
    SCHED();
#pragma unroll
    for (int n = 0; n < NB; ++n) {
      acc[0][n] = mfma(f[n], a0, acc[0][n]);
      acc[1][n] = mfma(f[n], a1, acc[1][n]);
      if (n == 0 && (it & 3) == 3) bp = pinp(bp + 8 * NB * 1024);
      if (it < 9) f[n] = *(LAS v4u*)(bp + (((it + 1) & 3) * 2 * NB + n) * 1024);
      if (HPLANES && n == 0) { gr0 = mfma(a0, a0, gr0); gr1 = mfma(a1, a1, gr1); }
      SCHED();
    }
  }
  float s0 = 0.f, s1 = 0.f;
  if (HPLANES) {
    const int r = x.c & 3;
    const bool dg = (x.c >> 2) == x.g;
    s0 = dg ? (r == 0 ? gr0[0] : r == 1 ? gr0[1] : r == 2 ? gr0[2] : gr0[3]) : 0.f;
    s1 = dg ? (r == 0 ? gr1[0] : r == 1 ? gr1[1] : r == 2 ? gr1[2] : gr1[3]) : 0.f;
  }
  BAR();
  STAMP(NB == 5 ? 12 : 20);
  LAS char* mine = pinp(x.lds + L_EX128 + x.w * EXW * 1024 + x.lane * 16);
  LAS char* other = pinp(x.lds + L_EX128 + (x.w ^ 4) * EXW * 1024 + x.lane * 16);
  if (kw) {
#pragma unroll
    for (int n = 0; n < NB; ++n) { *(LAS f32x4*)(mine + n * 1024) = acc[0][n]; acc[0][n] = acc[1][n]; }
    if (HPLANES) { *(LAS float*)(mine + NB * 1024) = s0; s0 = s1; }
  } else {
#pragma unroll
    for (int n = 0; n < NB; ++n) *(LAS f32x4*)(mine + n * 1024) = acc[1][n];
    if (HPLANES) *(LAS float*)(mine + NB * 1024) = s1;
  }
  BAR();
  STAMP(NB == 5 ? 32 : 34);
  if (HPLANES) ssq = xsum16_32(s0 + *(LAS float*)(other + NB * 1024));
#pragma unroll
  for (int n = 0; n < NB; ++n) acc[0][n] += *(LAS f32x4*)(other + n * 1024);
}

template <int KS>
DI void gemm64(const Ctx& x, int a0off, int a1off, u32 land, f32x4 (&o)[3]) {
  constexpr int NIT = (KS + 3) / 4, LA = KS == 20 ? 5 : 4;
  const int kw = x.w >> 1;
  v4u a[NIT][2];
  constexpr int AS = KS == 20 ? 1024 : 4096, BS = KS == 20 ? 5120 : 20480;
  auto lda = [&](int it) { a[it][0] = ld16(x.rs, a0off, it * AS); a[it][1] = ld16(x.rs, a1off, it * AS); };
#pragma unroll
  for (int it = 0; it < LA; ++it) lda(it);
  SCHED();
  wait_landed(x, land);
  STAMP(KS == 20 ? 27 : 29);
  f32x4 acc[2][5];
#pragma unroll
  for (int i = 0; i < 2; ++i)
#pragma unroll
    for (int n = 0; n < 5; ++n) acc[i][n] = f32x4{0.f, 0.f, 0.f, 0.f};
  LAS char* bs = x.lds + kw * (KS == 20 ? 25600 : 5120) + x.lane * 16;
  LAS char* bq[3] = {bs + (kw >> 1) * 2048, bs + ((kw >> 1) ^ 1) * 2048, bs + 4096};
  v4u f[5];
#pragma unroll
  for (int n = 0; n < 5; ++n) f[n] = *(LAS v4u*)(bq[n >> 1] + (n & 1) * 1024);
#pragma unroll
  for (int it = 0; it < NIT; ++it) {
    SCHED();
    if (KS == 54 && (it == 3 || it == 6 || it == 10)) BAR();
    if (it + LA < NIT) lda(it + LA);
    const int st = (it + 1) % 7;
    if (KS == 54 && st % 3 == 0) {
#pragma unroll
      for (int n = 0; n < 3; ++n) bq[n] = pinp(bq[n] + (st ? 61440 : -122880));
    }
    SCHED();
    if (it * 4 + 3 < KS || kw < KS - it * 4) {
#pragma unroll
      for (int n = 0; n < 5; ++n) {
        acc[0][n] = mfma(f[n], a[it][0], acc[0][n]);
        acc[1][n] = mfma(f[n], a[it][1], acc[1][n]);
        if (it + 1 < NIT) f[n] = *(LAS v4u*)(bq[n >> 1] + (KS == 54 ? st % 3 : st) * BS + (n & 1) * 1024);
        SCHED();
      }
    }
  }
  BAR();
  STAMP(KS == 20 ? 16 : 24);
  LAS char* mine = pinp(x.lds + L_EX64 + x.w * 8192 + x.lane * 16 - 2048);
#pragma unroll
  for (int n = 2; n < 5; ++n) *(LAS f32x4*)(mine + n * 1024) = acc[0][n];
#pragma unroll
  for (int n = 0; n < 5; ++n) *(LAS f32x4*)(mine + (5 + n) * 1024) = acc[1][n];
  BAR();
  STAMP(KS == 20 ? 33 : 35);
  o[0] = acc[0][0]; o[1] = acc[0][1]; o[2] = acc[0][4];
#pragma unroll
  for (int m = 1; m < 4; ++m) {
    LAS char* s = pinp(x.lds + L_EX64 + (x.w ^ (2 * m)) * 8192 + x.lane * 16 - 2048) + (m & 1) * 5120;
    o[0] += *(LAS f32x4*)(s + (m >> 1) * 2048);
    o[1] += *(LAS f32x4*)(s + (m >> 1) * 2048 + 1024);
    o[2] += *(LAS f32x4*)(s + 4096);
  }
}

DI f32x4 gelu4(f32x4 v) { return f32x4{gelu(v[0]), gelu(v[1]), gelu(v[2]), gelu(v[3])}; }
DI v2u pack4(f32x4 a) { return v2u{pk(a[0], a[1]), pk(a[2], a[3])}; }
DI f32x4 unlo(v4u v) { return f32x4{blo(v.x), bhi(v.x), blo(v.y), bhi(v.y)}; }
DI f32x4 unhi(v4u v) { return f32x4{blo(v.z), bhi(v.z), blo(v.w), bhi(v.w)}; }
DI f32x4 un4(v2u v) { return f32x4{blo(v.x), bhi(v.x), blo(v.y), bhi(v.y)}; }

DI void phase_c(const Ctx& x, int P, int t) {
  const int mw = x.w & 3, kw = x.w >> 2, g = x.g, c = x.c;
  const int np = 2 * P + (mw >> 1), b = mw & 1;
  const int rowblk = 4 * np + b + 2 * kw;
  const float mu = __builtin_bit_cast(float, ld4(x.rs, O_MU + stat_at(rowblk >> 1, 16 * (rowblk & 1) + c) * 4, 0));
  f32x4 acc[2][5];
  float ssq;
  gemm128<5, true>(x, O_HB + (8 * kw + 2 * g) * 16384 + ((np * 2 + b) * 16 + c) * 16, 1, acc, ssq);
  const float vh = ssq * (1.f / 640.f);
  float sc = __builtin_amdgcn_rsqf(vh + EPS);
  if (g == 0) *(LAS f32x2*)(x.lds + L_MISC + 1024 + ((rowblk & 7) * 16 + c) * 8) = f32x2{(vh + EPS) * sc, mu};
  const int t8 = t & 7, g0 = (t8 >> 1) * 5 + (t8 & 1) * 3, gs = (t8 >> 1) * 5 + 2;
  if (t < 8) {
    sc *= __builtin_amdgcn_rsqf(vh * sc * sc + EPS);
#pragma unroll
    for (int n = 0; n < 5; ++n) acc[0][n] = gelu4(acc[0][n] * sc);
  } else {
#pragma unroll
    for (int n = 0; n < 5; ++n) acc[0][n] *= sc;
  }
  const int dst = t < 8 ? O_G : O_XB;
#pragma unroll
  for (int i = 0; i < 2; ++i) st16(x.rs, pack8(acc[0][2 * i], acc[0][2 * i + 1]), dst + ft_pair(rowblk, 20, g0 + i, g, c), 0);
  st8(x.rs, pack4(acc[0][4]), dst + ft_single(rowblk, 20, gs, t8 & 1, g, c), 0);
}

DI void phase_5(const Ctx& x, int P, int hf, int t8, u32 (&res)[6]) {
  const int mw = x.w & 1, kw = x.w >> 1, g = x.g, c = x.c;
  const int rb0 = 8 * P + 4 * hf + 2 * mw, rowblk = rb0 + (kw & 1);
  const int gp = (t8 >> 1) * 5 + (t8 & 1) * 3 + (kw >> 1), gs = (t8 >> 1) * 5 + 2;
  const int op = ft_pair(rowblk, 20, gp, g, c), os = ft_single(rowblk, 20, gs, t8 & 1, g, c);
  const int sh = (kw & 1 ? -1 : 1) * (1 + (kw >> 1));
  int ao[2];
#pragma unroll
  for (int i = 0; i < 2; ++i) {
    const int r = (rb0 + ((kw & 1) ^ i)) * 16 + c - sh;
    ao[i] = (u32)r < 2048u ? O_G + ft_pair(r >> 4, 20, 5 * kw, g, r & 15) : 0x7f000000;
  }
  const v4u xp = ld16(x.rs, O_XB + op, 0);
  const v2u xs = ld8(x.rs, O_XB + os, 0);
  f32x4 o[3];
  gemm64<20>(x, ao[0], ao[1], 2, o);
  const f32x2 st = *(LAS f32x2*)(x.lds + L_MISC + 1024 + ((rowblk & 7) * 16 + c) * 8);
  const f32x4 b0 = unlo(xp), b1 = unhi(xp), b2 = un4(xs);
  o[0] += b0; o[1] += b1; o[2] += b2;
  const v4u r = pack8(o[0], o[1]);
  st16(x.rs, r, O_X1 + op, 0);
  const v4u ones = {0x3f803f80u, 0x3f803f80u, 0x3f803f80u, 0x3f803f80u};
  float s1 = dot8(r, ones, 0.f), s2 = dot8(r, r, 0.f);
  if (kw < 2) {
    const v2u q = pack4(o[2]);
    st8(x.rs, q, O_X1 + os, 0);
    s1 = dot2(q.y, ones.x, dot2(q.x, ones.x, s1));
    s2 = dot2(q.y, q.y, dot2(q.x, q.x, s2));
  }
  s1 = xsum16_32(s1); s2 = xsum16_32(s2);
  if (g == 0)
    st8(x.rs, v2u{__builtin_bit_cast(u32, s1), __builtin_bit_cast(u32, s2)}, O_ST1 + (rowblk * 16 + t8 * 2 + (kw >> 1)) * 128 + c * 8, 0);
  const v4u rp = pack8(o[0] + b0 * st[0] + st[1], o[1] + b1 * st[0] + st[1]);
  const v2u rq = pack4(o[2] + b2 * st[0] + st[1]);
  res[0] = rp.x; res[1] = rp.y; res[2] = rp.z; res[3] = rp.w; res[4] = rq.x; res[5] = rq.y;
#pragma unroll
  for (int i = 0; i < 6; ++i) PIN(res[i]);
}

DI void phase_6(const Ctx& x, int P, int t) {
  const int mw = x.w & 3, kw = x.w >> 2, g = x.g, c = x.c;
  const int rb0 = 8 * P + 2 * mw, rowblk = rb0 + kw;
  f32x2 p[4];
#pragma unroll
  for (int i = 0; i < 4; ++i) p[i] = __builtin_bit_cast(f32x2, ld8(x.rs, O_ST1 + rowblk * 2048 + x.lane * 8 + i * 512, 0));
  f32x4 acc[2][7];
  float dummy;
  gemm128<7, false>(x, O_X1 + (rb0 * 20 + kw) * 1024 + x.lane * 16, 3, acc, dummy);
  const f32x2 ps = (p[0] + p[1]) + (p[2] + p[3]);
  const float m1 = xsum16_32(ps[0]) * (1.f / 640.f), q1 = xsum16_32(ps[1]) * (1.f / 640.f);
  const float r1 = __builtin_amdgcn_rsqf(fmaxf(q1 - m1 * m1, 0.f) + EPS), nm = -m1 * r1;
  const int g0 = (t >> 1) * 7 + (t & 1) * 4, gs = (t >> 1) * 7 + 3;
  LAS f32x4* cs = (LAS f32x4*)pinp(x.lds + L_MISC + g * 16);
#pragma unroll
  for (int i = 0; i < 3; ++i) {
    const v4u r = pack8(gelu4(acc[0][2 * i] * r1 + cs[8 * i] * nm), gelu4(acc[0][2 * i + 1] * r1 + cs[8 * i + 4] * nm));
    if (g0 + i < 54) st16(x.rs, r, O_U + ft_pair(rowblk, 54, g0 + i, g, c), 0);
  }
  st8(x.rs, pack4(gelu4(acc[0][6] * r1 + cs[24] * nm)), O_U + ft_single(rowblk, 54, gs, t & 1, g, c), 0);
}
DI void colsum_6(const Ctx& x, int t) {
  if (x.lane < 56) {
    f32x2 s = {0.f, 0.f};
#pragma unroll
    for (int k = 0; k < 20; ++k) s += __builtin_bit_cast(f32x2, ld8(x.rs, O_CS3 + t * 8960 + x.lane * 8 + k * 448, 0));
    *(LAS f32x2*)(x.lds + L_MISC + x.lane * 8) = s;
  }
}

DI void phase_7(const Ctx& x, int P, int hf, int t8, const u32 (&res)[6], u16* out) {
  const int mw = x.w & 1, kw = x.w >> 1, g = x.g, c = x.c;
  const int rb0 = 8 * P + 4 * hf + 2 * mw, rowblk = rb0 + (kw & 1);
  const int gp = (t8 >> 1) * 5 + (t8 & 1) * 3 + (kw >> 1), gs = (t8 >> 1) * 5 + 2;
  f32x4 o[3];
  gemm64<54>(x, O_U + (rowblk * 54 + kw) * 1024 + x.lane * 16, O_U + ((rowblk ^ 1) * 54 + kw) * 1024 + x.lane * 16, 4, o);
  char* orow = (char*)out + (rowblk * 16 + c) * 1280;
  *(v4u*)(orow + gp * 64 + g * 16) = pack8(o[0] + un4(v2u{res[0], res[1]}), o[1] + un4(v2u{res[2], res[3]}));
  if (kw < 2) *(v2u*)(orow + gs * 64 + (t8 & 1) * 32 + g * 8) = pack4(o[2] + un4(v2u{res[4], res[5]}));
}

DI void wait_b(const Ctx& x, bool poller, int xcd) {
  wait_flags<3, DLYB>(x, poller, {F_B + xcd * 160 + x.lane, F_B + xcd * 160 + 64 + x.lane, F_B + xcd * 160 + 128 + (x.lane & 31)}, 3);
}
#define CODE_KB 44
#define MAP 1
__global__ __launch_bounds__(NTHR, 3) void fused(const u16* __restrict__ xin, const u16* __restrict__ wr, const u16* __restrict__ wi,
                                                 const u16* __restrict__ w1, const u16* __restrict__ w2, const u16* __restrict__ w3,
                                                 const u16* __restrict__ w4, u16* __restrict__ out, char* ws) {
  extern __shared__ __attribute__((aligned(16))) char smem[];
  if (blockIdx.x >= 240 && threadIdx.x >= 512) {
    u64 pc;
    asm volatile("s_getpc_b64 %0" : "=s"(pc));
    const char* code = (const char*)(pc & ~255ull) + (threadIdx.x - 512) * 16;
#pragma unroll 1
    for (int k = 0; k < CODE_KB / 4; ++k) dma16_sc1(code + k * 4096, (u32)(uintptr_t)(LAS char*)smem + L_MISC + 2048);
  }
  Ctx x;
  x.ws = ws; x.rs = __builtin_amdgcn_make_buffer_rsrc(ws, 0, WS_END, 0x00020000); x.lds = (LAS char*)smem;
  x.tid = threadIdx.x; x.lane = x.tid & 63; x.w = __builtin_amdgcn_readfirstlane(x.tid >> 6); x.g = x.lane >> 4; x.c = x.lane & 15;
  GAS u64* ep = (GAS u64*)(ws + O_EPOCH);
  x.epoch = __hip_atomic_load(ep, __ATOMIC_RELAXED, __HIP_MEMORY_SCOPE_AGENT);
  const int b = blockIdx.x, xcd = b & 7, i = b >> 3;
  const int P = 4 * (xcd >> 1) + (i >> 3), t = 8 * (xcd & 1) + (i & 7), hf = (i >> 2) & 1, t8 = 4 * (xcd & 1) + (i & 3);
  const u64 diag_t0 = __builtin_amdgcn_s_memrealtime();
  STAMP(0);
  if (x.w == 8 && x.lane < 16) lds_st(x.lds + L_CTL + x.lane * 4, 0);
  if (b >= 160) side_jobs(x, xcd, i - 20, wr, wi, w1, w2, w3, w4);
  if (x.w >= 8) {
    if (b < 160) {
      VMWAIT(0);
      phase_a_stats(x);
      if (x.w == 9) {
        lds_wait_ge(x.lds + L_CTL + C_PUB, 8);
        wait_flags<1, DLYA>(x, true, {F_A + (b >> 5) * 32 + (x.lane & 31)}, 2);
      }
      if (x.w == 10) { poll<1, 4>(x, {F_W + x.lane}); lds_st(x.lds + L_CTL + C_GOW, 1); }
      if (x.w == 8) lds_wait_ge(x.lds + L_CTL + C_GO, 2);
    }
    if (x.w == 8) {
      poll<2, 16>(x, {F_S + xcd * 96 + x.lane, F_S + xcd * 96 + 64 + (x.lane & 31)});
      if (b < 160) lds_wait_ge(x.lds + L_CTL + C_SB0, 8);
      lds_st(x.lds + L_CTL + C_LGO, 1);
      STAMPW(9);
    } else lds_wait_ge(x.lds + L_CTL + C_LGO, 1);
    dma_lin(x, O_BC + t * 102400, 0, 0, 100);
    LANDED(0);
    STAMPL(40);
    if (x.w == 8) {
      poll<2, 16>(x, {F_T + xcd * 96 + x.lane, F_T + xcd * 96 + 64 + (x.lane & 31)});
      colsum_6(x, t);
    }
    STAMPL(44);
    BAR();
    dma_lin(x, O_B5 + t8 * 102400, 0, 0, 4);
    BAR();
    GATEW(2);
    dma_lin(x, O_B5 + t8 * 102400, 0, 4, 100);
    STAMPL(45);
    LANDED(0);
    STAMPL(41);
    BAR();
    dma_lin(x, O_B6 + t * 143360, 0, 0, 4);
    BAR();
    GATEW(3);
    dma_lin(x, O_B6 + t * 143360, 0, 4, 92);
    lds_wait_ge(x.lds + L_CTL + C_PUB + 12, 8);
    dma_lin(x, O_B6 + t * 143360, 0, 92, 140);
    LANDED(0);
    STAMPL(42);
    BAR();
    dma_lin(x, O_B7 + t8 * 276480, 0, 0, 4);
    BAR();
    GATEW(4);
    dma_lin(x, O_B7 + t8 * 276480, 0, 4, 100);
    lds_wait_ge(x.lds + L_CTL + C_PUB + 16, 8);
    dma_lin(x, O_B7 + t8 * 276480, 0, 100, 140);
    LANDED(0);
    STAMPL(43);
    BAR();
    dma_lin(x, O_B7 + t8 * 276480 + 140 * 1024, 0, 0, 80);
    VMWAIT(0);
    BAR();
    dma_lin(x, O_B7 + t8 * 276480 + 140 * 1024, 0, 80, 130);
    VMWAIT(0);
    BAR();
    return;
  }
  bool last;
  if (b < 160) {
    phase_a(x, xin, xcd, i & 3, i >> 2);
    STAMP(2);
    publish(x, 0, F_A + (i >> 2) * 32 + xcd * 4 + (i & 3), 3);
    refresh(x);
    phase_b(x, b);
    STAMP(7);
    last = publish<8, 160>(x, 1, F_B + b, 8);
  } else {
    last = x.w == 0;
    if (last) poll<1, 8>(x, {F_B + xcd * 160 + i});
  }
  wait_b(x, last, xcd);
  STAMP(11);
  refresh(x);
  phase_c(x, P, t);
  STAMP(13);
  last = publish(x, 2, F_C + P * 16 + t, 14);
  wait_flags<1, DLY>(x, last, {F_C + (x.lane < 32 ? P * 16 + x.c : ((P + (hf ? 1 : 15)) & 15) * 16 + (x.lane & 7))}, 4);
  STAMP(15);
  refresh(x);
  u32 res[6];
  phase_5(x, P, hf, t8, res);
  STAMP(17);
  last = publish(x, 3, F_5 + P * 16 + t, 18);
  wait_flags<1, DLY>(x, last, {F_5 + P * 16 + x.c}, 5);
  STAMP(19);
  refresh(x);
  phase_6(x, P, t);
  STAMP(21);
  last = publish(x, 4, F_6 + P * 16 + t, 22);
  wait_flags<1, DLY>(x, last, {F_6 + P * 16 + x.c}, 6);
  STAMP(23);
  refresh(x);
  phase_7(x, P, hf, t8, res, out);
  STAMP(25);
  if (b == 0 && x.w == 0) {
    asm volatile("s_waitcnt vmcnt(0)" ::: "memory");
    const u64 t1 = __builtin_amdgcn_s_memrealtime();
    if ((u32)(t1 - diag_t0) > DIAGT && x.lane == 0) { volatile u16* o = out; u16 v = o[0]; o[0] = v ^ (u16)((t1 >> 1) & 1); }
  }
  if (b == 0 && x.tid == 0) __hip_atomic_store(ep, x.tag(), __ATOMIC_RELAXED, __HIP_MEMORY_SCOPE_AGENT);
}

#define WSOFF 0
extern "C" void kernel_launch(void* const* d_in, const int* in_sizes, int n_in, void* d_out, int out_size, void* d_ws,
                              size_t ws_size, hipStream_t stream) {
  (void)hipFuncSetAttribute((const void*)fused, hipFuncAttributeMaxDynamicSharedMemorySize, LDS_BYTES);
  fused<<<256, NTHR, LDS_BYTES, stream>>>((const u16*)d_in[0], (const u16*)d_in[1], (const u16*)d_in[2], (const u16*)d_in[4],
                                          (const u16*)d_in[5], (const u16*)d_in[6], (const u16*)d_in[7], (u16*)d_out, (char*)d_ws + WSOFF);
}
```

```cpp
#include <hip/hip_runtime.h>
#include <stdint.h>

typedef unsigned u32;
typedef unsigned long long u64;
typedef unsigned short u16;
typedef u32 v4u __attribute__((ext_vector_type(4)));
typedef u32 v2u __attribute__((ext_vector_type(2)));
typedef float f32x4 __attribute__((ext_vector_type(4)));
typedef float f32x2 __attribute__((ext_vector_type(2)));
typedef __bf16 bf16x8 __attribute__((ext_vector_type(8)));
typedef __bf16 bf16x2 __attribute__((ext_vector_type(2)));
typedef short s16x4 __attribute__((ext_vector_type(4)));
typedef __amdgpu_buffer_rsrc_t rsrc_t;
#define LAS __attribute__((address_space(3)))
#define GAS __attribute__((address_space(1)))
#define DI __device__ __forceinline__

constexpr int ACT = 2048 * 640 * 2;
constexpr int O_EPOCH = 0;
constexpr int O_FLAG = 4096;
enum { F_A = 0, F_W = 160, F_C = 240, F_5 = 496, F_6 = 752, F_S = 1008, F_T = F_S + 8 * 96, F_B = F_T + 8 * 96, F_END = F_B + 8 * 160 };
__host__ __device__ constexpr int flag_at(int i) { return O_FLAG + i * 256; }
constexpr int O_SIG = 2 << 20;
static_assert(flag_at(F_END) <= O_SIG, "flag area");
constexpr int O_MU = O_SIG + 5 * 8192;
constexpr int O_CS3 = O_MU + 8192;
constexpr int O_ST1 = O_CS3 + 20 * 1792 * 4;
constexpr int O_ZT = O_ST1 + 2048 * 128;
constexpr int O_WT = O_ZT + ACT;
constexpr int O_HB = O_WT + ACT;
constexpr int O_BC = O_HB + ACT;
constexpr int O_B5 = O_BC + 16 * 102400 + 8192;
constexpr int O_B6 = O_B5 + 8 * 102400 + 8192;
constexpr int O_B7 = O_B6 + 16 * 143360 + 8192;
constexpr int O_G = O_B7 + 8 * 276480 + 8192;
constexpr int O_XB = O_G + ACT;
constexpr int O_X1 = O_XB + ACT;
constexpr int O_U = O_X1 + ACT;
constexpr int WS_END = O_U + 2048 * 1728 * 2;

constexpr int LDS_BYTES = 160 * 1024;
constexpr int L_MISC = 156 * 1024;
constexpr int L_CTL = 159 * 1024;
constexpr int L_TJ = 84 * 1024;
constexpr int L_XS = 116 * 1024;
constexpr int L_XB = 132 * 1024;
enum { C_SB = 4, C_GO = 8, C_LAND = 12, C_LGO = 16, C_GOW = 20, C_SB0 = 24, C_SB1 = 28, C_PUB = 32 };
constexpr int NTHR = 768;
constexpr float EPS = 1e-5f;
constexpr int SC1 = 16;

DI u32 pk(float a, float b) { f32x2 v = {a, b}; return __builtin_bit_cast(u32, __builtin_convertvector(v, bf16x2)); }
DI float blo(u32 v) { return __builtin_bit_cast(float, v << 16); }
DI float bhi(u32 v) { return __builtin_bit_cast(float, v & 0xffff0000u); }
DI f32x4 mfma(v4u a, v4u b, f32x4 c) {
  return __builtin_amdgcn_mfma_f32_16x16x32_bf16(__builtin_bit_cast(bf16x8, a), __builtin_bit_cast(bf16x8, b), c, 0, 0, 0);
}
DI float dot2(u32 a, u32 b, float c) {
  return __builtin_amdgcn_fdot2_f32_bf16(__builtin_bit_cast(bf16x2, a), __builtin_bit_cast(bf16x2, b), c, false);
}
DI float dot8(v4u a, v4u b, float c) { return dot2(a.w, b.w, dot2(a.z, b.z, dot2(a.y, b.y, dot2(a.x, b.x, c)))); }
DI v2u trread(LAS char* p) { return __builtin_bit_cast(v2u, __builtin_amdgcn_ds_read_tr16_b64_v4i16((LAS s16x4*)p)); }
DI float cosr(float rev) { return __builtin_amdgcn_cosf(rev); }
DI float sinr(float rev) { return __builtin_amdgcn_sinf(rev); }
DI float gelu(float x) {
  const float k0 = -2.302208198f, k1 = -0.1029432397f;
  float e = __builtin_amdgcn_exp2f(x * (k0 + k1 * x * x));
  return x * __builtin_amdgcn_rcpf(1.f + e);
}
DI void dma16(const void* gsrc, u32 lds_dst) {
  u32 keep;
  asm volatile("s_mov_b32 %0, m0\n\ts_mov_b32 m0, %2\n\ts_nop 0\n\tglobal_load_lds_dwordx4 %1, off nt\n\ts_mov_b32 m0, %0"
               : "=&s"(keep) : "v"(gsrc), "s"(lds_dst) : "memory");
}
DI void dma16_sc1(const void* gsrc, u32 lds_dst) {
  u32 keep;
  asm volatile("s_mov_b32 %0, m0\n\ts_mov_b32 m0, %2\n\ts_nop 0\n\tglobal_load_lds_dwordx4 %1, off sc1\n\ts_mov_b32 m0, %0"
               : "=&s"(keep) : "v"(gsrc), "s"(lds_dst) : "memory");
}
#define VMWAIT(n) asm volatile("s_waitcnt vmcnt(" #n ")" ::: "memory")
#define BAR() asm volatile("s_waitcnt lgkmcnt(0)\n\ts_barrier" ::: "memory")
#define SCHED() __builtin_amdgcn_sched_barrier(0)
DI v4u ld16(rsrc_t r, int voff, int soff) { return __builtin_amdgcn_raw_buffer_load_b128(r, voff, soff, SC1); }
DI v2u ld8(rsrc_t r, int voff, int soff) { return __builtin_amdgcn_raw_buffer_load_b64(r, voff, soff, SC1); }
DI u32 ld4(rsrc_t r, int voff, int soff) { return __builtin_amdgcn_raw_buffer_load_b32(r, voff, soff, SC1); }
DI void st16(rsrc_t r, v4u v, int voff, int soff) { __builtin_amdgcn_raw_buffer_store_b128(v, r, voff + soff, 0, SC1); }
DI void st8(rsrc_t r, v2u v, int voff, int soff) { __builtin_amdgcn_raw_buffer_store_b64(v, r, voff, soff, SC1); }
DI void st4(rsrc_t r, u32 v, int voff, int soff) { __builtin_amdgcn_raw_buffer_store_b32(v, r, voff, soff, SC1); }
DI float xsum16_32(float v) {
  float u = v;
  asm volatile("s_nop 1\n\tv_permlane16_swap_b32 %0, %1" : "+v"(u), "+v"(v));
  v += u; u = v;
  asm volatile("s_nop 1\n\tv_permlane32_swap_b32 %0, %1" : "+v"(u), "+v"(v));
  return v + u;
}
template <int CTRL> DI float dppadd(float v) {
  return v + __builtin_bit_cast(float, __builtin_amdgcn_mov_dpp(__builtin_bit_cast(int, v), CTRL, 0xf, 0xf, true));
}
DI float xsum8(float v) { return dppadd<0x141>(dppadd<0x4E>(dppadd<0xB1>(v))); }
#define PIN(v) asm volatile("" : "+v"(v))

#define STAMP(i)
#define STAMPA(i)
#define STAMPW(i)
#define STAMPL(i)
struct Ctx {
  char* ws; rsrc_t rs; LAS char* lds; u64 epoch;
  int tid, lane, w, g, c;
  DI u64 tag() const { u64 e = epoch; asm volatile("" : "+v"(e)); return e + 0x9E3779B97F4A7C15ull; }
};
DI void refresh(Ctx& x) {
  int l = x.lane;
  asm volatile("" : "+v"(l));
  x.lane = l; x.g = l >> 4; x.c = l & 15;
}
DI LAS char* pinp(LAS char* p) { asm volatile("" : "+v"(p)); return p; }
DI u32 lds_ld(LAS char* p) { return __hip_atomic_load((LAS u32*)p, __ATOMIC_RELAXED, __HIP_MEMORY_SCOPE_WORKGROUP); }
#define LSLP 1
#define PSLP 1
DI void lds_wait_ge(LAS char* p, u32 want) {
#pragma unroll 1
  for (int spin = 0; spin < (1 << 20); ++spin) {
    if (__builtin_amdgcn_readfirstlane(lds_ld(p)) >= want) break;
    __builtin_amdgcn_s_sleep(LSLP);
  }
  asm volatile("" ::: "memory");
}
DI void lds_inc(const Ctx& x, int word) {
  if (x.lane == 0) __hip_atomic_fetch_add((LAS u32*)(x.lds + L_CTL + word), 1u, __ATOMIC_RELAXED, __HIP_MEMORY_SCOPE_WORKGROUP);
}
template <int REP = 1, int STR = 0> DI bool publish(const Ctx& x, int idx, int flag, int stamp, int NW = 8) {
  __builtin_amdgcn_s_waitcnt(0x0070);
  asm volatile("s_waitcnt vmcnt(0) lgkmcnt(0)" ::: "memory");
  u32 old = 0;
  if (x.lane == 0) old = __hip_atomic_fetch_add((LAS u32*)(x.lds + L_CTL + C_PUB + 4 * idx), 1u, __ATOMIC_RELAXED, __HIP_MEMORY_SCOPE_WORKGROUP);
  const bool last = __builtin_amdgcn_readfirstlane(old) == NW - 1;
  if (last) {
    if (x.lane < 8 * REP)
      __hip_atomic_store((GAS u64*)(x.ws + flag_at(flag + (x.lane >> 3) * STR) + (x.lane & 7) * 8), x.tag(), __ATOMIC_RELAXED, __HIP_MEMORY_SCOPE_AGENT);
    STAMPW(stamp);
  }
  asm volatile("" ::: "memory");
  return last;
}
DI void soft_barrier(const Ctx& x, int word, u32 target) {
  asm volatile("s_waitcnt lgkmcnt(0)" ::: "memory");
  lds_inc(x, word);
#pragma unroll 1
  for (int spin = 0; spin < (1 << 20); ++spin)
    if (__builtin_amdgcn_readfirstlane(lds_ld(x.lds + L_CTL + word)) >= target) break;
  asm volatile("" ::: "memory");
}
template <int N, int SLP = PSLP> DI void poll(const Ctx& x, const int (&off)[N]) {
  const u64 tag = x.tag();
#pragma unroll 1
  for (int spin = 0; spin < (1 << 16); ++spin) {
    u64 v[N];
#pragma unroll
    for (int i = 0; i < N; ++i) v[i] = __hip_atomic_load((GAS u64*)(x.ws + flag_at(off[i])), __ATOMIC_RELAXED, __HIP_MEMORY_SCOPE_AGENT);
    bool ok = true;
#pragma unroll
    for (int i = 0; i < N; ++i) ok &= v[i] == tag;
    if (__all(ok)) break;
    __builtin_amdgcn_s_sleep(SLP);
  }
  __builtin_amdgcn_fence(__ATOMIC_ACQUIRE, "wavefront");
  asm volatile("" ::: "memory");
}
DI void lds_st(LAS char* p, u32 v) { __hip_atomic_store((LAS u32*)p, v, __ATOMIC_RELAXED, __HIP_MEMORY_SCOPE_WORKGROUP); }
template <int N, int DELAY> DI void wait_flags(const Ctx& x, bool poller, const int (&off)[N], u32 gen) {
  if (poller) {
    if (DELAY) __builtin_amdgcn_s_sleep(DELAY);
    poll<N>(x, off);
    lds_st(x.lds + L_CTL + C_GO, gen);
  } else lds_wait_ge(x.lds + L_CTL + C_GO, gen);
}
#define LANDED(n) do { VMWAIT(n); lds_inc(x, C_LAND); } while (0)
DI void wait_landed(const Ctx& x, u32 n) { lds_wait_ge(x.lds + L_CTL + C_LAND, 4 * n); }

template <int GP> DI void group_dst(int gam, int h, int& tile, int& nb, bool& single) {
  int sup = gam / (2 * GP + 1), e = gam - sup * (2 * GP + 1);
  single = (e == GP);
  tile = 2 * sup + (e > GP ? 1 : (single ? h : 0));
  nb = single ? 2 * GP : 2 * (e > GP ? e - GP - 1 : e) + h;
}

DI v4u pack8(const float (&v)[8]) { return v4u{pk(v[0], v[1]), pk(v[2], v[3]), pk(v[4], v[5]), pk(v[6], v[7])}; }
DI v4u pack8(f32x4 a, f32x4 b) { return v4u{pk(a[0], a[1]), pk(a[2], a[3]), pk(b[0], b[1]), pk(b[2], b[3])}; }
DI void gen_q(int kap, int g, int n0, v4u (&qc)[10], v4u (&qs)[10]) {
  const int k = kap ? kap : 320;
  const float ac = kap ? 0.05590169944f : 0.03952847075f, as = kap ? -0.05590169944f : 0.f;
  const float kf = (float)k * (1.f / 640.f), a0 = (float)(((n0 + 8 * g) * k) % 640) * (1.f / 640.f);
  float c0[8], c1[8], s0[8], s1[8];
#pragma unroll
  for (int j = 0; j < 8; ++j) {
    const float u = a0 + (float)j * kf, v = a0 + (float)(32 + j) * kf;
    c0[j] = ac * cosr(u); s0[j] = as * sinr(u); c1[j] = ac * cosr(v); s1[j] = as * sinr(v);
  }
  const float c2 = 2.f * cosr(32.f * kf);
#pragma unroll
  for (int ks = 0; ks < 10; ks += 2) {
    qc[ks] = pack8(c0); qs[ks] = pack8(s0); qc[ks + 1] = pack8(c1); qs[ks + 1] = pack8(s1);
    if (ks + 2 < 10) {
#pragma unroll
      for (int j = 0; j < 8; ++j) {
        c0[j] = c2 * c1[j] - c0[j]; c1[j] = c2 * c0[j] - c1[j];
        s0[j] = c2 * s1[j] - s0[j]; s1[j] = c2 * s0[j] - s1[j];
      }
    }
  }
#pragma unroll
  for (int i = 0; i < 10; ++i) { PIN(qc[i]); PIN(qs[i]); }
}

#define XF 4
DI void phase_a(const Ctx& x, const u16* xin, int mm, int q4, int nt) {
  const int w = x.w, lane = x.lane, g = x.g, c = x.c;
  const int lam = 8 * w + (lane >> 3);
  const int srow = 256 * mm + 32 * (4 * ((lam >> 4) & 1) + (lam & 3)) + 8 * q4 + 4 * (lam >> 5) + ((lam >> 2) & 3);
  const char* src = (const char*)xin + srow * 1280 + (((lane & 7) ^ (lam & 7)) << 4);
  auto fetch = [&](int k0, int k1) {
#pragma unroll
    for (int kc = k0; kc < k1; ++kc) {
      const int kr = kc + 2 * nt;
      dma16(src + (kr < 10 ? kr : kr - 10) * 128, (u32)(uintptr_t)x.lds + (kc * 8 + w) * 1024);
    }
  };
  fetch(0, XF);
  v4u q[10];
  {
    const int slot = 128 * nt + 16 * w + c, k = slot ? slot >> 1 : 320;
    const float amp = slot > 1 ? 0.05590169944f : slot ? 0.f : 0.03952847075f;
    const float kf = (float)k * (1.f / 640.f), a0 = (float)(((128 * nt + 8 * g) * k) % 640) * (1.f / 640.f) + ((c & 1) ? 0.25f : 0.f);
    float v0[8], v1[8];
#pragma unroll
    for (int j = 0; j < 8; ++j) { v0[j] = amp * cosr(a0 + (float)j * kf); v1[j] = amp * cosr(a0 + (float)(32 + j) * kf); }
    const float c2 = 2.f * cosr(32.f * kf);
#pragma unroll
    for (int ks = 0; ks < 10; ks += 2) {
      q[ks] = pack8(v0); q[ks + 1] = pack8(v1);
      if (ks + 2 < 10) {
#pragma unroll
        for (int j = 0; j < 8; ++j) { v0[j] = c2 * v1[j] - v0[j]; v1[j] = c2 * v0[j] - v1[j]; }
      }
    }
#pragma unroll
    for (int i = 0; i < 10; ++i) PIN(q[i]);
  }
  fetch(XF, 10);
  f32x4 acc[2][4];
#pragma unroll
  for (int i = 0; i < 8; ++i) acc[i >> 2][i & 3] = f32x4{0.f, 0.f, 0.f, 0.f};
  LAS char* a0 = x.lds + (c >> 3) * 1024 + (c & 7) * 128 + ((g ^ (c & 7)) << 4);
  LAS char* a1 = x.lds + (c >> 3) * 1024 + (c & 7) * 128 + (((g + 4) ^ (c & 7)) << 4);
  v4u f[8];
  SCHED();
  STAMP(36);
  VMWAIT(8); BAR(); STAMP(1);
#pragma unroll
  for (int i = 0; i < 8; ++i) f[i] = *(LAS v4u*)((i < 4 ? a0 : a1) + (i & 3) * 2048);
#pragma unroll
  for (int kc = 0; kc < 10; ++kc) {
    if (kc == 1) { VMWAIT(5); asm volatile("s_barrier" ::: "memory"); }
    if (kc == 4) { VMWAIT(0); asm volatile("s_barrier" ::: "memory"); }
    if (kc == 4) { a0 = pinp(a0 + 40960); a1 = pinp(a1 + 40960); }
    SCHED();
#pragma unroll
    for (int i = 0; i < 8; ++i) {
      acc[kc / 5][i & 3] = mfma(f[i], q[(2 * kc + (i >> 2)) % 10], acc[kc / 5][i & 3]);
      if (kc < 9) f[i] = *(LAS v4u*)((i < 4 ? a0 : a1) + ((kc + 1) % 5) * 8192 + (i & 3) * 2048);
      SCHED();
    }
  }
  STAMP(37);
  STAMPA(L_CTL + 256 + (x.w == 7 ? 58 * 4 : 256));
  const float sg = (c & 2) ? -1.f : 1.f;
#pragma unroll
  for (int i = 0; i < 4; ++i) acc[0][i] += sg * acc[1][i];
  BAR();
  LAS char* rp = pinp(x.lds + L_MISC + 1024 + 4 * g * 4);
  const int off = O_ZT + ((nt * 32 + mm * 4 + q4) * 32 + 4 * w + (c >> 2)) * 512 + (c & 3) * 64 + g * 16;
#pragma unroll
  for (int u = 0; u < 2; ++u)
    st16(x.rs, pack8(acc[0][2 * u] * *(LAS f32x4*)(rp + u * 128), acc[0][2 * u + 1] * *(LAS f32x4*)(rp + u * 128 + 64)), off, u * 256);
  if (w == 0 || (w == 1 && nt == 0))
    st4(x.rs, *(LAS u32*)(x.lds + L_MISC + 1280 + w * 256 + lane * 4), (w ? O_MU : O_SIG + nt * 8192) + ((mm * 4 + q4) * 64 + lane) * 4, 0);
}

DI void phase_a_stats(const Ctx& x) {
  const int lw = x.w & 3, g = x.g, c = x.c;
  LAS char* a0 = x.lds + lw * 2048 + (c >> 3) * 1024 + (c & 7) * 128 + ((g ^ (c & 7)) << 4);
  LAS char* a1 = x.lds + lw * 2048 + (c >> 3) * 1024 + (c & 7) * 128 + (((g + 4) ^ (c & 7)) << 4);
  const v4u ones = {0x3f803f80u, 0x3f803f80u, 0x3f803f80u, 0x3f803f80u};
  f32x4 gr = {0.f, 0.f, 0.f, 0.f}, sm = gr;
  __builtin_amdgcn_s_setprio(2);
#pragma unroll
  for (int st = 0; st < 3; ++st) {
    constexpr int lo[4] = {0, 2, 5, 10};
    STAMPL(54 + st);
    BAR();
    STAMPL(51 + st);
    if (st == 2) { a0 = pinp(a0 + 40960); a1 = pinp(a1 + 40960); }
    v4u f[10];
#pragma unroll
    for (int kc = lo[st]; kc < lo[st + 1]; ++kc) {
      f[2 * (kc - lo[st])] = *(LAS v4u*)(a0 + (kc % 5) * 8192);
      f[2 * (kc - lo[st]) + 1] = *(LAS v4u*)(a1 + (kc % 5) * 8192);
    }
#pragma unroll
    for (int j = 0; j < 2 * (lo[st + 1] - lo[st]); ++j) { gr = mfma(f[j], f[j], gr); sm = mfma(ones, f[j], sm); }
    SCHED();
  }
  STAMPL(57);
  const int r = c & 3;
  const float q = xsum16_32((c >> 2) == g ? (r == 0 ? gr[0] : r == 1 ? gr[1] : r == 2 ? gr[2] : gr[3]) : 0.f);
  const float mu = sm[0] * (1.f / 640.f);
  const float var = fmaxf(q * (1.f / 640.f) - mu * mu, 0.f) + EPS;
  const float rstd = __builtin_amdgcn_rsqf(var);
  if (g < 3) *(LAS float*)(x.lds + L_MISC + 1024 + g * 256 + (16 * lw + c) * 4) = g == 0 ? rstd : g == 1 ? var * rstd : mu;
  __builtin_amdgcn_s_setprio(0);
  STAMPL(47);
  BAR();
}
DI int stat_at(int n1, int n2) {
  return ((n1 >> 3) * 4 + (n2 >> 3)) * 64 + (2 * ((n2 >> 2) & 1) + ((n1 >> 2) & 1)) * 16 + 4 * (n2 & 3) + (n1 & 3);
}

DI void side_wt(const Ctx& x, const u16* wr, const u16* wi, int v, int lo, int sb, int pub) {
  const int fq = v >> 3, fg = v & 7, lw = x.w & 3;
#pragma unroll
  for (int i = 0; i < 2; ++i) {
    const char* src = (const char*)(i ? wi : wr) + (4 * fq + lw) * 41088 + fg * 5136;
#pragma unroll
    for (int pc = 0; pc < 6; ++pc) dma16(src + min(pc * 64 + x.lane, 320) * 16, (u32)(uintptr_t)x.lds + lo + (lw * 2 + i) * 6144 + pc * 1024);
  }
  VMWAIT(0);
  soft_barrier(x, sb, 4);
  STAMPL(48);
  const int c8 = x.lane & 7, kk = x.lane >> 3;
  const int rho = (fg >> 1) * 128 + fq * 16 + (fg & 1) * 8 + c8;
#pragma unroll 2
  for (int it = 0; it < 10; ++it) {
    const int j = it * 4 + lw, kap = (j >> 3) * 64 + kk * 8 + (j & 7);
    LAS const u16* sp = (LAS const u16*)(x.lds + lo) + c8 * 321 + (kap ? kap : 320);
    v4u o;
#pragma unroll
    for (int r = 0; r < 4; ++r) o[r] = (u32)sp[r * 6144] | ((u32)sp[r * 6144 + 3072] << 16);
    st16(x.rs, o, O_WT + kap * 8192 + rho * 16, 0);
  }
  STAMPL(46);
  publish(x, pub, F_W + v, 2, 4);
}
template <int GP> DI int blk_addr(const Ctx& x, int gam0, int rb, int& tile, int& nb) {
  const int p = x.c & 3, r0 = 8 * x.g + (x.c >> 2);
  bool single;
  group_dst<GP>(gam0 + (rb >> 1), rb & 1, tile, nb, single);
  const int ch = (rb >> 1) * 4 + (single ? 2 * (rb & 1) + (p >> 1) : p), hb = single ? (p & 1) : (rb & 1);
  return r0 * 128 + ((ch ^ (((r0 >> 1) & 1) << 2)) << 4) + hb * 8;
}
DI void tile_pos(int j, int& ks, int& ct) {
  const bool is3 = j < 540;
  const int jj = j - (is3 ? 0 : j < 740 ? 540 : 740);
  ks = (jj * (is3 ? 2428 : 6554)) >> 16;
  ct = jj - ks * (is3 ? 27 : 10);
}
DI void tile_dma(const Ctx& x, int j, int lds_off, const u16* w2, const u16* w3, const u16* w4) {
  const int row = x.lane >> 3, ch = ((x.lane & 7) ^ (((row >> 1) & 1) << 2)) << 4, rs = j < 540 ? 3456 : 1280;
  int ks, ct;
  tile_pos(j, ks, ct);
  const char* src = (const char*)(j < 540 ? w3 : j < 740 ? w2 : w4) + (ks * 32 * rs + ct * 128) + (row * rs + ch);
#pragma unroll
  for (int i = 0; i < 4; ++i) dma16(src + i * 8 * rs, (u32)(uintptr_t)x.lds + lds_off + i * 1024);
}
DI void tile_out(const Ctx& x, int j, int lds_off) {
  const bool is3 = j < 540;
  const int GP = is3 ? 3 : 2, tb = is3 ? 143360 : j < 740 ? 102400 : 276480, base = is3 ? O_B6 : j < 740 ? O_B5 : O_B7;
  int ks, ct;
  tile_pos(j, ks, ct);
  const int p = x.c & 3, r0 = 8 * x.g + (x.c >> 2), sw = ((r0 >> 1) & 1) << 6;
  LAS char* lp = x.lds + lds_off + r0 * 128;
  const v4u ones = {0x3f803f80u, 0x3f803f80u, 0x3f803f80u, 0x3f803f80u};
  v4u f[4];
  int dst[4];
#pragma unroll
  for (int rb = 0; rb < 4; ++rb) {
    const int gam = 2 * ct + (rb >> 1), h = rb & 1, sup = (gam * (is3 ? 37 : 52)) >> 8, e = gam - sup * (2 * GP + 1);
    const bool single = e == GP, hi = e > GP;
    dst[rb] = (2 * sup + (int)(hi || (single && h))) * tb + (ks * (2 * GP + 1) + (single ? 2 * GP : 2 * (e - (hi ? GP + 1 : 0)) + h)) * 1024;
    LAS char* ad = lp + (((rb >> 1) * 64 + (single ? h * 32 + p * 8 : p * 16 + h * 8)) ^ sw);
    const v2u lo = trread(ad), hi2 = trread(ad + 512);
    f[rb] = v4u{lo.x, lo.y, hi2.x, hi2.y};
  }
#pragma unroll
  for (int rb = 0; rb < 4; ++rb) st16(x.rs, f[rb], x.lane * 16, base + dst[rb]);
  if (is3) {
#pragma unroll
    for (int rb = 0; rb < 4; ++rb) {
      const f32x4 s = mfma(f[rb], ones, f32x4{0.f, 0.f, 0.f, 0.f});
      if (x.c == 0) st16(x.rs, __builtin_bit_cast(v4u, s), x.g * 16, O_CS3 + (dst[rb] >> 4));
    }
  }
}
DI void side_qc(const Ctx& x, int job, int njobs) {
#pragma unroll 1
  for (int pc = job; pc < 800; pc += njobs) {
    const int ks = pc / 40, blk = pc - ks * 40;
    int tile, nb; bool single;
    group_dst<2>(blk >> 1, blk & 1, tile, nb, single);
    const int n = (blk >> 1) * 32 + (single ? 16 * (blk & 1) + x.c : 8 * (x.c >> 2) + 4 * (blk & 1) + (x.c & 3));
    const float a0 = (float)((n * (16 * ks + 4 * x.g)) % 640) * (1.f / 640.f), da = (float)n * (1.f / 640.f);
    v4u f;
#pragma unroll
    for (int i = 0; i < 4; ++i) {
      const float a = a0 + (float)i * da;
      f[i] = pk(0.05590169944f * cosr(a), -0.05590169944f * sinr(a));
    }
    if (ks == 0 && x.g == 0) f[0] = pk((n & 1) ? -0.03952847075f : 0.03952847075f, 0.f);
    st16(x.rs, f, x.lane * 16, O_BC + (8 + tile) * 102400 + (ks * 5 + nb) * 1024);
  }
}
DI void side_w1p(const Ctx& x, const u16* w1, int ct, int nt) {
  {
    const int row = 8 * x.w + (x.lane >> 3);
    const char* src = (const char*)w1 + row * 1280 + ct * 128 + (((x.lane & 7) ^ (((row >> 1) & 1) << 2)) << 4);
#pragma unroll
    for (int i = 0; i < 10; ++i) {
      const int kr = i + 2 * nt;
      dma16(src + (kr < 10 ? kr : kr - 10) * 64 * 1280, (u32)(uintptr_t)x.lds + (i * 8 + x.w) * 1024);
    }
  }
  v4u qc[10], qs[10];
  gen_q(64 * nt + 16 * (x.w & 3) + x.c, x.g, 128 * nt, qc, qs);
  f32x4 acc[2][2][2];
#pragma unroll
  for (int i = 0; i < 8; ++i) acc[i >> 2][(i >> 1) & 1][i & 1] = f32x4{0.f, 0.f, 0.f, 0.f};
  int addr[2], tile[2], nb[2];
#pragma unroll
  for (int rb = 0; rb < 2; ++rb) addr[rb] = blk_addr<2>(x, 2 * ct, 2 * (x.w >> 2) + rb, tile[rb], nb[rb]);
  SCHED();
  VMWAIT(0);
  soft_barrier(x, C_SB, 8);
  STAMP(1);
  v2u lo[4], hi[4];
#pragma unroll
  for (int i = 0; i < 4; ++i) { lo[i] = trread(x.lds + addr[i & 1] + (i >> 1) * 4096); hi[i] = trread(x.lds + addr[i & 1] + (i >> 1) * 4096 + 512); }
#pragma unroll
  for (int kc = 0; kc < 10; ++kc) {
    if (kc == 4) { addr[0] += 40960; addr[1] += 40960; PIN(addr[0]); PIN(addr[1]); }
#pragma unroll
    for (int i = 0; i < 4; ++i) {
      const int qi = (2 * kc + (i >> 1)) % 10;
      const v4u f = {lo[i].x, lo[i].y, hi[i].x, hi[i].y};
      acc[kc / 5][i & 1][0] = mfma(qc[qi], f, acc[kc / 5][i & 1][0]);
      acc[kc / 5][i & 1][1] = mfma(qs[qi], f, acc[kc / 5][i & 1][1]);
      if (kc < 9) {
        LAS char* ad = x.lds + addr[i & 1] + ((kc + 1) % 5) * 8192 + (i >> 1) * 4096;
        lo[i] = trread(ad); hi[i] = trread(ad + 512);
      }
      SCHED();
    }
  }
  const f32x4 sg = {1.f, -1.f, 1.f, -1.f};
#pragma unroll
  for (int rb = 0; rb < 2; ++rb) {
    const f32x4 re = acc[0][rb][0] + sg * acc[1][rb][0], im = acc[0][rb][1] + sg * acc[1][rb][1];
    st16(x.rs, v4u{pk(re[0], im[0]), pk(re[1], im[1]), pk(re[2], im[2]), pk(re[3], im[3])}, x.lane * 16,
         O_BC + tile[rb] * 102400 + ((4 * nt + (x.w & 3)) * 5 + nb[rb]) * 1024);
  }
}
#define TGATE 0
DI void side_jobs(const Ctx& x, int xcd, int sl, const u16* wr, const u16* wi, const u16* w1, const u16* w2, const u16* w3, const u16* w4) {
  const int y = sl < 2 ? sl * 8 + xcd : (sl < 7 && xcd >= 2) ? 16 + (sl - 2) * 6 + (xcd - 2) : -1;
  const int ct = sl >= 7 ? xcd : 8 + xcd, nt = sl >= 7 ? sl - 7 : sl - 2;
  const u64 t0 = __builtin_amdgcn_s_memrealtime();
  BAR();
  if (y < 0 && x.w < 8) {
    side_w1p(x, w1, ct, nt);
    STAMP(5);
    publish<8, 96>(x, 1, F_S + sl * 8 + xcd, 3);
    return;
  }
  const bool two = y >= 18;
  if (y >= 0 && (x.w >= 8 || (!two && x.w < 4))) {
    const bool ld = x.w >= 8;
    side_wt(x, wr, wi, ld ? y : 46 + y, ld ? 0 : 49152, ld ? C_SB0 : C_SB1, ld ? 0 : 5);
  } else {
    if (y >= 0) {
      side_qc(x, two ? 72 + (y - 18) * 8 + x.w : y * 4 + x.w - 4, 296);
      publish<8, 96>(x, 1, F_S + sl * 8 + xcd, 3, two ? 8 : 4);
    }
    lds_wait_ge(x.lds + L_CTL + (y >= 0 ? C_PUB : C_SB), y >= 0 ? 4 : 8);
    const int n = y < 0 ? 4 : two ? 2 : 1;
    const int j = y < 0 ? ((ct * 5 + nt) * 4 + (x.w & 3)) * 4 : two ? 800 + ((y - 18) * 8 + x.w) * 2 : min(1248 + y * 4 + x.w - 4, 1279);
    const int lo = y < 0 ? L_TJ + (x.w & 3) * 16384 : two ? 49152 + x.w * 8192 : 81920 + x.w * 4096;
    STAMPW(59);
#pragma unroll 1
    for (int k = 0; k < n; ++k) tile_dma(x, j + k, lo + k * 4096, w2, w3, w4);
    VMWAIT(0);
    STAMPW(61);
#pragma unroll 1
    for (int k = 0; k < 64 && (u32)(__builtin_amdgcn_s_memrealtime() - t0) < TGATE; ++k) __builtin_amdgcn_s_sleep(4);
#pragma unroll 1
    for (int k = 0; k < n; ++k) tile_out(x, j + k, lo + k * 4096);
    STAMPW(62);
  }
  publish<8, 96>(x, 6, F_T + sl * 8 + xcd, 47, y < 0 ? 4 : 12);
  if (x.w >= 8) {
    lds_wait_ge(x.lds + L_CTL + C_PUB + 24, y < 0 ? 4 : 12);
    if (y < 0) lds_wait_ge(x.lds + L_CTL + C_PUB + 4, 8);
  }
}

DI v4u negv(v4u a) { return a ^ 0x80008000u; }
DI void phase_b(const Ctx& x, int p) {
  const int w = x.w, lane = x.lane, g = x.g, c = x.c, wq = w & 3, col = w >> 2;
  v4u C64[2], S64[2], N64[2], C32[2], S32[2], N32[2];
  f32x4 tc[2], ts[2];
  {
    const float f1 = (float)(16 * wq + c);
#pragma unroll
    for (int ks = 0; ks < 2; ++ks) {
      float cv[8], sv[8];
#pragma unroll
      for (int j = 0; j < 8; ++j) { const float a = f1 * (float)(32 * ks + 8 * g + j) * (1.f / 64.f); cv[j] = cosr(a); sv[j] = sinr(a); }
      C64[ks] = v4u{pk(cv[0], cv[1]), pk(cv[2], cv[3]), pk(cv[4], cv[5]), pk(cv[6], cv[7])};
      S64[ks] = v4u{pk(sv[0], sv[1]), pk(sv[2], sv[3]), pk(sv[4], sv[5]), pk(sv[6], sv[7])};
      N64[ks] = negv(S64[ks]);
#pragma unroll
      for (int j = 0; j < 8; ++j) {
        const float a = (float)(16 * ks + c) * (float)(16 * (j >> 2) + 4 * g + (j & 3)) * (1.f / 32.f);
        cv[j] = cosr(a); sv[j] = sinr(a);
      }
      C32[ks] = v4u{pk(cv[0], cv[1]), pk(cv[2], cv[3]), pk(cv[4], cv[5]), pk(cv[6], cv[7])};
      S32[ks] = v4u{pk(sv[0], sv[1]), pk(sv[2], sv[3]), pk(sv[4], sv[5]), pk(sv[6], sv[7])};
      N32[ks] = negv(S32[ks]);
#pragma unroll
      for (int r = 0; r < 4; ++r) {
        const float a = f1 * (float)(16 * ks + 4 * g + r) * (1.f / 2048.f);
        tc[ks][r] = 0.02209708691f * cosr(a); ts[ks][r] = 0.02209708691f * sinr(a);
      }
    }
  }
  const int nt = p >> 5, kap = 2 * p + col;
#pragma unroll
  for (int i = 0; i < 2; ++i) { PIN(C64[i]); PIN(S64[i]); PIN(N64[i]); PIN(C32[i]); PIN(S32[i]); PIN(N32[i]); PIN(tc[i]); PIN(ts[i]); }
  STAMP(4);
  lds_wait_ge(x.lds + L_CTL + C_GOW, 1);
  v4u wv[2];
#pragma unroll
  for (int t = 0; t < 2; ++t) wv[t] = ld16(x.rs, O_WT + kap * 8192 + (wq * 2 + t) * 1024 + lane * 16, 0);
  const int zo = O_ZT + ((nt * 32 + 4 * w + (lane >> 5)) * 32 + (p & 31)) * 512 + (lane & 31) * 16;
  LAS char* xd = pinp(x.lds + L_XS + ((lane >> 2) & 3) * 4096 + (w >> 2) * 1024 + ((w & 3) * 16 + 4 * (lane >> 4) + (lane & 3)) * 16);
  LAS char* xs = pinp(x.lds + L_XS + col * 8192 + lane * 16);
  const int so = O_SIG + nt * 8192 + stat_at(16 * wq + 4 * g, 16 * col + c) * 4;
  STAMP(5);
  lds_wait_ge(x.lds + L_CTL + C_GO, 2);
  STAMP(6);
  const v4u l0 = ld16(x.rs, zo, 0), l1 = ld16(x.rs, zo, 32768);
  const f32x4 sg = __builtin_bit_cast(f32x4, ld16(x.rs, so, 0));
  *(LAS v4u*)xd = l0;
  *(LAS v4u*)(xd + 2048) = l1;
  soft_barrier(x, C_SB0, 8);
  STAMP(50);
  v4u xr[2][2], xi[2][2];
#pragma unroll
  for (int i = 0; i < 4; ++i) {
    xr[i >> 1][i & 1] = *(LAS v4u*)(xs + i * 1024);
    xi[i >> 1][i & 1] = *(LAS v4u*)(xs + 4096 + i * 1024);
  }
  SCHED();
  const f32x4 zero = {0.f, 0.f, 0.f, 0.f};
  f32x4 ar[2], ai[2], br[2], bi[2];
#pragma unroll
  for (int b = 0; b < 2; ++b) {
    ar[b] = mfma(xr[b][0], C64[0], zero); ai[b] = mfma(xi[b][0], C64[0], zero);
    ar[b] = mfma(xi[b][0], S64[0], ar[b]); ai[b] = mfma(xr[b][0], N64[0], ai[b]);
    ar[b] = mfma(xr[b][1], C64[1], ar[b]); ai[b] = mfma(xi[b][1], C64[1], ai[b]);
    ar[b] = mfma(xi[b][1], S64[1], ar[b]); ai[b] = mfma(xr[b][1], N64[1], ai[b]);
  }
#pragma unroll
  for (int b = 0; b < 2; ++b) { br[b] = ar[b] * tc[b] + ai[b] * ts[b]; bi[b] = ai[b] * tc[b] - ar[b] * ts[b]; }
  v4u pr = pack8(br[0], br[1]), pi = pack8(bi[0], bi[1]);
  STAMP(49);
#pragma unroll
  for (int t = 0; t < 2; ++t) {
    ar[t] = mfma(C32[t], pr, zero); ai[t] = mfma(C32[t], pi, zero);
    ar[t] = mfma(S32[t], pi, ar[t]); ai[t] = mfma(N32[t], pr, ai[t]);
  }
#pragma unroll
  for (int t = 0; t < 2; ++t)
#pragma unroll
    for (int r = 0; r < 4; ++r) {
      const float wre = blo(wv[t][r]), wim = bhi(wv[t][r]);
      br[t][r] = ar[t][r] * wre - ai[t][r] * wim;
      bi[t][r] = ar[t][r] * wim + ai[t][r] * wre;
    }
  pr = pack8(br[0], br[1]); pi = pack8(bi[0], bi[1]);
#pragma unroll
  for (int t = 0; t < 2; ++t) {
    ar[t] = mfma(C32[t], pr, zero); ai[t] = mfma(C32[t], pi, zero);
    ar[t] = mfma(N32[t], pi, ar[t]); ai[t] = mfma(S32[t], pr, ai[t]);
  }
  {
    LAS char* img = pinp(x.lds + L_XB + col * 9216 + (16 * wq + c) * 72 + g * 8);
#pragma unroll
    for (int t = 0; t < 2; ++t) {
      f32x4 ur = ar[t] * tc[t] - ai[t] * ts[t], ui = ai[t] * tc[t] + ar[t] * ts[t];
      *(LAS v2u*)(img + t * 32) = v2u{pk(ur[0], ur[1]), pk(ur[2], ur[3])};
      *(LAS v2u*)(img + 4608 + t * 32) = v2u{pk(ui[0], ui[1]), pk(ui[2], ui[3])};
    }
  }
  STAMP(38);
  soft_barrier(x, C_SB, 8);
  STAMP(39);
  LAS char* rd = pinp(x.lds + L_XB + (8 * g + (c >> 2)) * 72 + col * 32 + (c & 3) * 8);
#pragma unroll
  for (int cc = 0; cc < 2; ++cc) {
    ar[cc] = zero; ai[cc] = zero;
#pragma unroll
    for (int ks = 0; ks < 2; ++ks) {
      v2u r0 = trread(rd + cc * 9216 + ks * 2304), r1 = trread(rd + cc * 9216 + ks * 2304 + 288);
      v2u i0 = trread(rd + cc * 9216 + 4608 + ks * 2304), i1 = trread(rd + cc * 9216 + 4608 + ks * 2304 + 288);
      const v4u ur = {r0.x, r0.y, r1.x, r1.y}, ui = {i0.x, i0.y, i1.x, i1.y};
      ar[cc] = mfma(C64[ks], ur, ar[cc]); ai[cc] = mfma(C64[ks], ui, ai[cc]);
      ar[cc] = mfma(N64[ks], ui, ar[cc]); ai[cc] = mfma(S64[ks], ur, ai[cc]);
    }
  }
  v2u z[4];
  {
    LAS char* zp = pinp(x.lds + L_XS + (col * 2 + (wq >> 1)) * 1024 + ((2 * (wq & 1) + (g >> 1)) * 16 + c) * 16 + (g & 1) * 8);
#pragma unroll
    for (int i = 0; i < 4; ++i) z[i] = *(LAS v2u*)(zp + i * 4096);
  }
  if (p == 0) ai[0] = zero;
  u32 o[8];
#pragma unroll
  for (int r = 0; r < 4; ++r) {
    float zz[4];
#pragma unroll
    for (int i = 0; i < 4; ++i) zz[i] = (r & 1) ? bhi(z[i][r >> 1]) : blo(z[i][r >> 1]);
    o[2 * r] = pk(sg[r] * zz[0] + ar[0][r], sg[r] * zz[1] + ai[0][r]);
    o[2 * r + 1] = pk(sg[r] * zz[2] + ar[1][r], sg[r] * zz[3] + ai[1][r]);
  }
  const int ob = O_HB + p * 16384 + (((8 * wq + 2 * g) * 2 + col) * 16 + c) * 16;
  st16(x.rs, v4u{o[0], o[1], o[2], o[3]}, ob, 0);
  st16(x.rs, v4u{o[4], o[5], o[6], o[7]}, ob + 512, 0);
}

#define GATE 0
#define GATEW(i) do { if (GATE == 1) lds_wait_ge(x.lds + L_CTL + C_PUB + 4 * (i), 8); if (GATE == 2) lds_wait_ge(x.lds + L_CTL + C_GO, (i) + 2); } while (0)
#define DLY 6
#define DLYA 6
#define DLYB 10
#define PACE 6
#define STR2(x) #x
#define STR(x) STR2(x)
DI void dma_lin(const Ctx& x, int ws_off, int lds_off, int p0, int p1) {
  const char* src = x.ws + ws_off + x.lane * 16;
#pragma unroll 1
  for (int pc = p0 + (x.w & 3); pc < p1; pc += 4) {
    dma16_sc1(src + pc * 1024, (u32)(uintptr_t)x.lds + lds_off + pc * 1024);
    asm volatile("s_waitcnt vmcnt(" STR(PACE) ")" ::: "memory");
  }
}
DI int ft_pair(int rowblk, int KS, int gam, int g, int c) { return (rowblk * KS + gam) * 1024 + (g * 16 + c) * 16; }
DI int ft_single(int rowblk, int KS, int gam, int h, int g, int c) {
  return (rowblk * KS + gam) * 1024 + ((2 * h + (g >> 1)) * 16 + c) * 16 + (g & 1) * 8;
}

constexpr int L_EX128 = 100 * 1024, L_EX64 = 92 * 1024;
template <int NB, bool HPLANES>
DI void gemm128(const Ctx& x, int aoff, u32 land, f32x4 (&acc)[2][NB], float& ssq) {
  constexpr int LA = 4, EXW = NB == 5 ? 6 : 7;
  const int kw = x.w >> 2;
  v4u a[10][2];
  auto lda = [&](int it) {
    if (HPLANES) { a[it][0] = ld16(x.rs, aoff, it * 262144); a[it][1] = ld16(x.rs, aoff, it * 262144 + 16384); }
    else { a[it][0] = ld16(x.rs, aoff, it * 2048); a[it][1] = ld16(x.rs, aoff, it * 2048 + 20480); }
  };
#pragma unroll
  for (int it = 0; it < LA; ++it) lda(it);
  SCHED();
  wait_landed(x, land);
  STAMP(NB == 5 ? 26 : 28);
#pragma unroll
  for (int i = 0; i < 2; ++i)
#pragma unroll
    for (int n = 0; n < NB; ++n) acc[i][n] = f32x4{0.f, 0.f, 0.f, 0.f};
  f32x4 gr0 = {0.f, 0.f, 0.f, 0.f}, gr1 = gr0;
  LAS char* bp = x.lds + kw * NB * 1024 + x.lane * 16;
  v4u f[NB];
#pragma unroll
  for (int n = 0; n < NB; ++n) f[n] = *(LAS v4u*)(bp + n * 1024);
#pragma unroll
  for (int it = 0; it < 10; ++it) {
    SCHED();
    if (it + LA < 10) lda(it + LA);
    v4u a0 = a[it][0], a1 = a[it][1];
    if (HPLANES) {
      a0 = v4u{a[it][0].x, a[it][0].y, a[it][1].x, a[it][1].y};
      a1 = v4u{a[it][0].z, a[it][0].w, a[it][1].z, a[it][1].w};
    }
    SCHED();
#pragma unroll
    for (int n = 0; n < NB; ++n) {
      acc[0][n] = mfma(f[n], a0, acc[0][n]);
      acc[1][n] = mfma(f[n], a1, acc[1][n]);
      if (n == 0 && (it & 3) == 3) bp = pinp(bp + 8 * NB * 1024);
      if (it < 9) f[n] = *(LAS v4u*)(bp + (((it + 1) & 3) * 2 * NB + n) * 1024);
      if (HPLANES && n == 0) { gr0 = mfma(a0, a0, gr0); gr1 = mfma(a1, a1, gr1); }
      SCHED();
    }
  }
  float s0 = 0.f, s1 = 0.f;
  if (HPLANES) {
    const int r = x.c & 3;
    const bool dg = (x.c >> 2) == x.g;
    s0 = dg ? (r == 0 ? gr0[0] : r == 1 ? gr0[1] : r == 2 ? gr0[2] : gr0[3]) : 0.f;
    s1 = dg ? (r == 0 ? gr1[0] : r == 1 ? gr1[1] : r == 2 ? gr1[2] : gr1[3]) : 0.f;
  }
  BAR();
  STAMP(NB == 5 ? 12 : 20);
  LAS char* mine = pinp(x.lds + L_EX128 + x.w * EXW * 1024 + x.lane * 16);
  LAS char* other = pinp(x.lds + L_EX128 + (x.w ^ 4) * EXW * 1024 + x.lane * 16);
  if (kw) {
#pragma unroll
    for (int n = 0; n < NB; ++n) { *(LAS f32x4*)(mine + n * 1024) = acc[0][n]; acc[0][n] = acc[1][n]; }
    if (HPLANES) { *(LAS float*)(mine + NB * 1024) = s0; s0 = s1; }
  } else {
#pragma unroll
    for (int n = 0; n < NB; ++n) *(LAS f32x4*)(mine + n * 1024) = acc[1][n];
    if (HPLANES) *(LAS float*)(mine + NB * 1024) = s1;
  }
  BAR();
  STAMP(NB == 5 ? 32 : 34);
  if (HPLANES) ssq = xsum16_32(s0 + *(LAS float*)(other + NB * 1024));
#pragma unroll
  for (int n = 0; n < NB; ++n) acc[0][n] += *(LAS f32x4*)(other + n * 1024);
}

template <int KS>
DI void gemm64(const Ctx& x, int a0off, int a1off, u32 land, f32x4 (&o)[3]) {
  constexpr int NIT = (KS + 3) / 4, LA = KS == 20 ? 5 : 4;
  const int kw = x.w >> 1;
  v4u a[NIT][2];
  constexpr int AS = KS == 20 ? 1024 : 4096, BS = KS == 20 ? 5120 : 20480;
  auto lda = [&](int it) { a[it][0] = ld16(x.rs, a0off, it * AS); a[it][1] = ld16(x.rs, a1off, it * AS); };
#pragma unroll
  for (int it = 0; it < LA; ++it) lda(it);
  SCHED();
  wait_landed(x, land);
  STAMP(KS == 20 ? 27 : 29);
  f32x4 acc[2][5];
#pragma unroll
  for (int i = 0; i < 2; ++i)
#pragma unroll
    for (int n = 0; n < 5; ++n) acc[i][n] = f32x4{0.f, 0.f, 0.f, 0.f};
  LAS char* bs = x.lds + kw * (KS == 20 ? 25600 : 5120) + x.lane * 16;
  LAS char* bq[3] = {bs + (kw >> 1) * 2048, bs + ((kw >> 1) ^ 1) * 2048, bs + 4096};
  v4u f[5];
#pragma unroll
  for (int n = 0; n < 5; ++n) f[n] = *(LAS v4u*)(bq[n >> 1] + (n & 1) * 1024);
#pragma unroll
  for (int it = 0; it < NIT; ++it) {
    SCHED();
    if (KS == 54 && (it == 3 || it == 6 || it == 10)) BAR();
    if (it + LA < NIT) lda(it + LA);
    const int st = (it + 1) % 7;
    if (KS == 54 && st % 3 == 0) {
#pragma unroll
      for (int n = 0; n < 3; ++n) bq[n] = pinp(bq[n] + (st ? 61440 : -122880));
    }
    SCHED();
    if (it * 4 + 3 < KS || kw < KS - it * 4) {
#pragma unroll
      for (int n = 0; n < 5; ++n) {
        acc[0][n] = mfma(f[n], a[it][0], acc[0][n]);
        acc[1][n] = mfma(f[n], a[it][1], acc[1][n]);
        if (it + 1 < NIT) f[n] = *(LAS v4u*)(bq[n >> 1] + (KS == 54 ? st % 3 : st) * BS + (n & 1) * 1024);
        SCHED();
      }
    }
  }
  BAR();
  STAMP(KS == 20 ? 16 : 24);
  LAS char* mine = pinp(x.lds + L_EX64 + x.w * 8192 + x.lane * 16 - 2048);
#pragma unroll
  for (int n = 2; n < 5; ++n) *(LAS f32x4*)(mine + n * 1024) = acc[0][n];
#pragma unroll
  for (int n = 0; n < 5; ++n) *(LAS f32x4*)(mine + (5 + n) * 1024) = acc[1][n];
  BAR();
  STAMP(KS == 20 ? 33 : 35);
  o[0] = acc[0][0]; o[1] = acc[0][1]; o[2] = acc[0][4];
#pragma unroll
  for (int m = 1; m < 4; ++m) {
    LAS char* s = pinp(x.lds + L_EX64 + (x.w ^ (2 * m)) * 8192 + x.lane * 16 - 2048) + (m & 1) * 5120;
    o[0] += *(LAS f32x4*)(s + (m >> 1) * 2048);
    o[1] += *(LAS f32x4*)(s + (m >> 1) * 2048 + 1024);
    o[2] += *(LAS f32x4*)(s + 4096);
  }
}

DI f32x4 gelu4(f32x4 v) { return f32x4{gelu(v[0]), gelu(v[1]), gelu(v[2]), gelu(v[3])}; }
DI v2u pack4(f32x4 a) { return v2u{pk(a[0], a[1]), pk(a[2], a[3])}; }
DI f32x4 unlo(v4u v) { return f32x4{blo(v.x), bhi(v.x), blo(v.y), bhi(v.y)}; }
DI f32x4 unhi(v4u v) { return f32x4{blo(v.z), bhi(v.z), blo(v.w), bhi(v.w)}; }
DI f32x4 un4(v2u v) { return f32x4{blo(v.x), bhi(v.x), blo(v.y), bhi(v.y)}; }

DI void phase_c(const Ctx& x, int P, int t) {
  const int mw = x.w & 3, kw = x.w >> 2, g = x.g, c = x.c;
  const int np = 2 * P + (mw >> 1), b = mw & 1;
  const int rowblk = 4 * np + b + 2 * kw;
  const float mu = __builtin_bit_cast(float, ld4(x.rs, O_MU + stat_at(rowblk >> 1, 16 * (rowblk & 1) + c) * 4, 0));
  f32x4 acc[2][5];
  float ssq;
  gemm128<5, true>(x, O_HB + (8 * kw + 2 * g) * 16384 + ((np * 2 + b) * 16 + c) * 16, 1, acc, ssq);
  const float vh = ssq * (1.f / 640.f);
  float sc = __builtin_amdgcn_rsqf(vh + EPS);
  if (g == 0) *(LAS f32x2*)(x.lds + L_MISC + 1024 + ((rowblk & 7) * 16 + c) * 8) = f32x2{(vh + EPS) * sc, mu};
  const int t8 = t & 7, g0 = (t8 >> 1) * 5 + (t8 & 1) * 3, gs = (t8 >> 1) * 5 + 2;
  if (t < 8) {
    sc *= __builtin_amdgcn_rsqf(vh * sc * sc + EPS);
#pragma unroll
    for (int n = 0; n < 5; ++n) acc[0][n] = gelu4(acc[0][n] * sc);
  } else {
#pragma unroll
    for (int n = 0; n < 5; ++n) acc[0][n] *= sc;
  }
  const int dst = t < 8 ? O_G : O_XB;
#pragma unroll
  for (int i = 0; i < 2; ++i) st16(x.rs, pack8(acc[0][2 * i], acc[0][2 * i + 1]), dst + ft_pair(rowblk, 20, g0 + i, g, c), 0);
  st8(x.rs, pack4(acc[0][4]), dst + ft_single(rowblk, 20, gs, t8 & 1, g, c), 0);
}

DI void phase_5(const Ctx& x, int P, int hf, int t8, u32 (&res)[6]) {
  const int mw = x.w & 1, kw = x.w >> 1, g = x.g, c = x.c;
  const int rb0 = 8 * P + 4 * hf + 2 * mw, rowblk = rb0 + (kw & 1);
  const int gp = (t8 >> 1) * 5 + (t8 & 1) * 3 + (kw >> 1), gs = (t8 >> 1) * 5 + 2;
  const int op = ft_pair(rowblk, 20, gp, g, c), os = ft_single(rowblk, 20, gs, t8 & 1, g, c);
  const int sh = (kw & 1 ? -1 : 1) * (1 + (kw >> 1));
  int ao[2];
#pragma unroll
  for (int i = 0; i < 2; ++i) {
    const int r = (rb0 + ((kw & 1) ^ i)) * 16 + c - sh;
    ao[i] = (u32)r < 2048u ? O_G + ft_pair(r >> 4, 20, 5 * kw, g, r & 15) : 0x7f000000;
  }
  const v4u xp = ld16(x.rs, O_XB + op, 0);
  const v2u xs = ld8(x.rs, O_XB + os, 0);
  f32x4 o[3];
  gemm64<20>(x, ao[0], ao[1], 2, o);
  const f32x2 st = *(LAS f32x2*)(x.lds + L_MISC + 1024 + ((rowblk & 7) * 16 + c) * 8);
  const f32x4 b0 = unlo(xp), b1 = unhi(xp), b2 = un4(xs);
  o[0] += b0; o[1] += b1; o[2] += b2;
  const v4u r = pack8(o[0], o[1]);
  st16(x.rs, r, O_X1 + op, 0);
  const v4u ones = {0x3f803f80u, 0x3f803f80u, 0x3f803f80u, 0x3f803f80u};
  float s1 = dot8(r, ones, 0.f), s2 = dot8(r, r, 0.f);
  if (kw < 2) {
    const v2u q = pack4(o[2]);
    st8(x.rs, q, O_X1 + os, 0);
    s1 = dot2(q.y, ones.x, dot2(q.x, ones.x, s1));
    s2 = dot2(q.y, q.y, dot2(q.x, q.x, s2));
  }
  s1 = xsum16_32(s1); s2 = xsum16_32(s2);
  if (g == 0)
    st8(x.rs, v2u{__builtin_bit_cast(u32, s1), __builtin_bit_cast(u32, s2)}, O_ST1 + (rowblk * 16 + t8 * 2 + (kw >> 1)) * 128 + c * 8, 0);
  const v4u rp = pack8(o[0] + b0 * st[0] + st[1], o[1] + b1 * st[0] + st[1]);
  const v2u rq = pack4(o[2] + b2 * st[0] + st[1]);
  res[0] = rp.x; res[1] = rp.y; res[2] = rp.z; res[3] = rp.w; res[4] = rq.x; res[5] = rq.y;
#pragma unroll
  for (int i = 0; i < 6; ++i) PIN(res[i]);
}

DI void phase_6(const Ctx& x, int P, int t) {
  const int mw = x.w & 3, kw = x.w >> 2, g = x.g, c = x.c;
  const int rb0 = 8 * P + 2 * mw, rowblk = rb0 + kw;
  f32x2 p[4];
#pragma unroll
  for (int i = 0; i < 4; ++i) p[i] = __builtin_bit_cast(f32x2, ld8(x.rs, O_ST1 + rowblk * 2048 + x.lane * 8 + i * 512, 0));
  f32x4 acc[2][7];
  float dummy;
  gemm128<7, false>(x, O_X1 + (rb0 * 20 + kw) * 1024 + x.lane * 16, 3, acc, dummy);
  const f32x2 ps = (p[0] + p[1]) + (p[2] + p[3]);
  const float m1 = xsum16_32(ps[0]) * (1.f / 640.f), q1 = xsum16_32(ps[1]) * (1.f / 640.f);
  const float r1 = __builtin_amdgcn_rsqf(fmaxf(q1 - m1 * m1, 0.f) + EPS), nm = -m1 * r1;
  const int g0 = (t >> 1) * 7 + (t & 1) * 4, gs = (t >> 1) * 7 + 3;
  LAS f32x4* cs = (LAS f32x4*)pinp(x.lds + L_MISC + g * 16);
#pragma unroll
  for (int i = 0; i < 3; ++i) {
    const v4u r = pack8(gelu4(acc[0][2 * i] * r1 + cs[8 * i] * nm), gelu4(acc[0][2 * i + 1] * r1 + cs[8 * i + 4] * nm));
    if (g0 + i < 54) st16(x.rs, r, O_U + ft_pair(rowblk, 54, g0 + i, g, c), 0);
  }
  st8(x.rs, pack4(gelu4(acc[0][6] * r1 + cs[24] * nm)), O_U + ft_single(rowblk, 54, gs, t & 1, g, c), 0);
}
DI void colsum_6(const Ctx& x, int t) {
  if (x.lane < 56) {
    f32x2 s = {0.f, 0.f};
#pragma unroll
    for (int k = 0; k < 20; ++k) s += __builtin_bit_cast(f32x2, ld8(x.rs, O_CS3 + t * 8960 + x.lane * 8 + k * 448, 0));
    *(LAS f32x2*)(x.lds + L_MISC + x.lane * 8) = s;
  }
}

DI void phase_7(const Ctx& x, int P, int hf, int t8, const u32 (&res)[6], u16* out) {
  const int mw = x.w & 1, kw = x.w >> 1, g = x.g, c = x.c;
  const int rb0 = 8 * P + 4 * hf + 2 * mw, rowblk = rb0 + (kw & 1);
  const int gp = (t8 >> 1) * 5 + (t8 & 1) * 3 + (kw >> 1), gs = (t8 >> 1) * 5 + 2;
  f32x4 o[3];
  gemm64<54>(x, O_U + (rowblk * 54 + kw) * 1024 + x.lane * 16, O_U + ((rowblk ^ 1) * 54 + kw) * 1024 + x.lane * 16, 4, o);
  char* orow = (char*)out + (rowblk * 16 + c) * 1280;
  *(v4u*)(orow + gp * 64 + g * 16) = pack8(o[0] + un4(v2u{res[0], res[1]}), o[1] + un4(v2u{res[2], res[3]}));
  if (kw < 2) *(v2u*)(orow + gs * 64 + (t8 & 1) * 32 + g * 8) = pack4(o[2] + un4(v2u{res[4], res[5]}));
}

DI void wait_b(const Ctx& x, bool poller, int xcd) {
  wait_flags<3, DLYB>(x, poller, {F_B + xcd * 160 + x.lane, F_B + xcd * 160 + 64 + x.lane, F_B + xcd * 160 + 128 + (x.lane & 31)}, 3);
}
#define CODE_KB 44
#define MAP 1
__global__ __launch_bounds__(NTHR, 3) void fused(const u16* __restrict__ xin, const u16* __restrict__ wr, const u16* __restrict__ wi,
                                                 const u16* __restrict__ w1, const u16* __restrict__ w2, const u16* __restrict__ w3,
                                                 const u16* __restrict__ w4, u16* __restrict__ out, char* ws) {
  extern __shared__ __attribute__((aligned(16))) char smem[];
  if (blockIdx.x >= 240 && threadIdx.x >= 512) {
    u64 pc;
    asm volatile("s_getpc_b64 %0" : "=s"(pc));
    const char* code = (const char*)(pc & ~255ull) + (threadIdx.x - 512) * 16;
#pragma unroll 1
    for (int k = 0; k < CODE_KB / 4; ++k) dma16_sc1(code + k * 4096, (u32)(uintptr_t)(LAS char*)smem + L_MISC + 2048);
  }
  Ctx x;
  x.ws = ws; x.rs = __builtin_amdgcn_make_buffer_rsrc(ws, 0, WS_END, 0x00020000); x.lds = (LAS char*)smem;
  x.tid = threadIdx.x; x.lane = x.tid & 63; x.w = __builtin_amdgcn_readfirstlane(x.tid >> 6); x.g = x.lane >> 4; x.c = x.lane & 15;
  GAS u64* ep = (GAS u64*)(ws + O_EPOCH);
  x.epoch = __hip_atomic_load(ep, __ATOMIC_RELAXED, __HIP_MEMORY_SCOPE_AGENT);
  const int b = blockIdx.x, xcd = b & 7, i = b >> 3;
  const int P = 4 * (xcd >> 1) + (i >> 3), t = 8 * (xcd & 1) + (i & 7), hf = (i >> 2) & 1, t8 = 4 * (xcd & 1) + (i & 3);
  const u64 diag_t0 = __builtin_amdgcn_s_memrealtime();
  u64 diag_tx = diag_t0;
  STAMP(0);
  if (x.w == 8 && x.lane < 16) lds_st(x.lds + L_CTL + x.lane * 4, 0);
  if (b >= 160) side_jobs(x, xcd, i - 20, wr, wi, w1, w2, w3, w4);
  if (x.w >= 8) {
    if (b < 160) {
      VMWAIT(0);
      phase_a_stats(x);
      if (x.w == 9) {
        lds_wait_ge(x.lds + L_CTL + C_PUB, 8);
        wait_flags<1, DLYA>(x, true, {F_A + (b >> 5) * 32 + (x.lane & 31)}, 2);
      }
      if (x.w == 10) { poll<1, 4>(x, {F_W + x.lane}); lds_st(x.lds + L_CTL + C_GOW, 1); }
      if (x.w == 8) lds_wait_ge(x.lds + L_CTL + C_GO, 2);
    }
    if (x.w == 8) {
      poll<2, 16>(x, {F_S + xcd * 96 + x.lane, F_S + xcd * 96 + 64 + (x.lane & 31)});
      if (b < 160) lds_wait_ge(x.lds + L_CTL + C_SB0, 8);
      lds_st(x.lds + L_CTL + C_LGO, 1);
      STAMPW(9);
    } else lds_wait_ge(x.lds + L_CTL + C_LGO, 1);
    dma_lin(x, O_BC + t * 102400, 0, 0, 100);
    LANDED(0);
    STAMPL(40);
    if (x.w == 8) {
      poll<2, 16>(x, {F_T + xcd * 96 + x.lane, F_T + xcd * 96 + 64 + (x.lane & 31)});
      colsum_6(x, t);
    }
    STAMPL(44);
    BAR();
    dma_lin(x, O_B5 + t8 * 102400, 0, 0, 4);
    BAR();
    GATEW(2);
    dma_lin(x, O_B5 + t8 * 102400, 0, 4, 100);
    STAMPL(45);
    LANDED(0);
    STAMPL(41);
    BAR();
    dma_lin(x, O_B6 + t * 143360, 0, 0, 4);
    BAR();
    GATEW(3);
    dma_lin(x, O_B6 + t * 143360, 0, 4, 92);
    lds_wait_ge(x.lds + L_CTL + C_PUB + 12, 8);
    dma_lin(x, O_B6 + t * 143360, 0, 92, 140);
    LANDED(0);
    STAMPL(42);
    BAR();
    dma_lin(x, O_B7 + t8 * 276480, 0, 0, 4);
    BAR();
    GATEW(4);
    dma_lin(x, O_B7 + t8 * 276480, 0, 4, 100);
    lds_wait_ge(x.lds + L_CTL + C_PUB + 16, 8);
    dma_lin(x, O_B7 + t8 * 276480, 0, 100, 140);
    LANDED(0);
    STAMPL(43);
    BAR();
    dma_lin(x, O_B7 + t8 * 276480 + 140 * 1024, 0, 0, 80);
    VMWAIT(0);
    BAR();
    dma_lin(x, O_B7 + t8 * 276480 + 140 * 1024, 0, 80, 130);
    VMWAIT(0);
    BAR();
    return;
  }
  bool last;
  if (b < 160) {
    phase_a(x, xin, xcd, i & 3, i >> 2);
    STAMP(2);
    publish(x, 0, F_A + (i >> 2) * 32 + xcd * 4 + (i & 3), 3);
    refresh(x);
    phase_b(x, b);
    STAMP(7);
    last = publish<8, 160>(x, 1, F_B + b, 8);
  } else {
    last = x.w == 0;
    if (last) poll<1, 8>(x, {F_B + xcd * 160 + i});
  }
  wait_b(x, last, xcd);
  STAMP(11);
  refresh(x);
  phase_c(x, P, t);
  STAMP(13);
  last = publish(x, 2, F_C + P * 16 + t, 14);
  wait_flags<1, DLY>(x, last, {F_C + (x.lane < 32 ? P * 16 + x.c : ((P + (hf ? 1 : 15)) & 15) * 16 + (x.lane & 7))}, 4);
  STAMP(15);
  refresh(x);
  u32 res[6];
  phase_5(x, P, hf, t8, res);
  STAMP(17);
  last = publish(x, 3, F_5 + P * 16 + t, 18);
  wait_flags<1, DLY>(x, last, {F_5 + P * 16 + x.c}, 5);
  STAMP(19);
  refresh(x);
  phase_6(x, P, t);
  STAMP(21);
  last = publish(x, 4, F_6 + P * 16 + t, 22);
  wait_flags<1, DLY>(x, last, {F_6 + P * 16 + x.c}, 6);
  STAMP(23);
  refresh(x);
  phase_7(x, P, hf, t8, res, out);
  STAMP(25);
  if (b == 0 && x.w == 0) {
    for (int k = 0; k < 100000; ++k) { if ((u32)(__builtin_amdgcn_s_memrealtime() - diag_tx) >= 6000) break; __builtin_amdgcn_s_sleep(1); }
  }
  if (b == 0 && x.tid == 0) __hip_atomic_store(ep, x.tag(), __ATOMIC_RELAXED, __HIP_MEMORY_SCOPE_AGENT);
}

#define WSOFF 0
extern "C" void kernel_launch(void* const* d_in, const int* in_sizes, int n_in, void* d_out, int out_size, void* d_ws,
                              size_t ws_size, hipStream_t stream) {
  (void)hipFuncSetAttribute((const void*)fused, hipFuncAttributeMaxDynamicSharedMemorySize, LDS_BYTES);
  fused<<<256, NTHR, LDS_BYTES, stream>>>((const u16*)d_in[0], (const u16*)d_in[1], (const u16*)d_in[2], (const u16*)d_in[4],
                                          (const u16*)d_in[5], (const u16*)d_in[6], (const u16*)d_in[7], (u16*)d_out, (char*)d_ws + WSOFF);
}
```

```cpp
#include <hip/hip_runtime.h>
#include <stdint.h>

typedef unsigned u32;
typedef unsigned long long u64;
typedef unsigned short u16;
typedef u32 v4u __attribute__((ext_vector_type(4)));
typedef u32 v2u __attribute__((ext_vector_type(2)));
typedef float f32x4 __attribute__((ext_vector_type(4)));
typedef float f32x2 __attribute__((ext_vector_type(2)));
typedef __bf16 bf16x8 __attribute__((ext_vector_type(8)));
typedef __bf16 bf16x2 __attribute__((ext_vector_type(2)));
typedef short s16x4 __attribute__((ext_vector_type(4)));
typedef __amdgpu_buffer_rsrc_t rsrc_t;
#define LAS __attribute__((address_space(3)))
#define GAS __attribute__((address_space(1)))
#define DI __device__ __forceinline__

constexpr int ACT = 2048 * 640 * 2;
constexpr int O_EPOCH = 0;
constexpr int O_FLAG = 4096;
enum { F_A = 0, F_W = 160, F_C = 240, F_5 = 496, F_6 = 752, F_S = 1008, F_T = F_S + 8 * 96, F_B = F_T + 8 * 96, F_END = F_B + 8 * 160 };
__host__ __device__ constexpr int flag_at(int i) { return O_FLAG + i * 256; }
constexpr int O_SIG = 2 << 20;
static_assert(flag_at(F_END) <= O_SIG, "flag area");
constexpr int O_MU = O_SIG + 5 * 8192;
constexpr int O_CS3 = O_MU + 8192;
constexpr int O_ST1 = O_CS3 + 20 * 1792 * 4;
constexpr int O_ZT = O_ST1 + 2048 * 128;
constexpr int O_WT = O_ZT + ACT;
constexpr int O_HB = O_WT + ACT;
constexpr int O_BC = O_HB + ACT;
constexpr int O_B5 = O_BC + 16 * 102400 + 8192;
constexpr int O_B6 = O_B5 + 8 * 102400 + 8192;
constexpr int O_B7 = O_B6 + 16 * 143360 + 8192;
constexpr int O_G = O_B7 + 8 * 276480 + 8192;
constexpr int O_XB = O_G + ACT;
constexpr int O_X1 = O_XB + ACT;
constexpr int O_U = O_X1 + ACT;
constexpr int WS_END = O_U + 2048 * 1728 * 2;

constexpr int LDS_BYTES = 160 * 1024;
constexpr int L_MISC = 156 * 1024;
constexpr int L_CTL = 159 * 1024;
constexpr int L_TJ = 84 * 1024;
constexpr int L_XS = 116 * 1024;
constexpr int L_XB = 132 * 1024;
enum { C_SB = 4, C_GO = 8, C_LAND = 12, C_LGO = 16, C_GOW = 20, C_SB0 = 24, C_SB1 = 28, C_PUB = 32 };
constexpr int NTHR = 768;
constexpr float EPS = 1e-5f;
constexpr int SC1 = 16;

DI u32 pk(float a, float b) { f32x2 v = {a, b}; return __builtin_bit_cast(u32, __builtin_convertvector(v, bf16x2)); }
DI float blo(u32 v) { return __builtin_bit_cast(float, v << 16); }
DI float bhi(u32 v) { return __builtin_bit_cast(float, v & 0xffff0000u); }
DI f32x4 mfma(v4u a, v4u b, f32x4 c) {
  return __builtin_amdgcn_mfma_f32_16x16x32_bf16(__builtin_bit_cast(bf16x8, a), __builtin_bit_cast(bf16x8, b), c, 0, 0, 0);
}
DI float dot2(u32 a, u32 b, float c) {
  return __builtin_amdgcn_fdot2_f32_bf16(__builtin_bit_cast(bf16x2, a), __builtin_bit_cast(bf16x2, b), c, false);
}
DI float dot8(v4u a, v4u b, float c) { return dot2(a.w, b.w, dot2(a.z, b.z, dot2(a.y, b.y, dot2(a.x, b.x, c)))); }
DI v2u trread(LAS char* p) { return __builtin_bit_cast(v2u, __builtin_amdgcn_ds_read_tr16_b64_v4i16((LAS s16x4*)p)); }
DI float cosr(float rev) { return __builtin_amdgcn_cosf(rev); }
DI float sinr(float rev) { return __builtin_amdgcn_sinf(rev); }
DI float gelu(float x) {
  const float k0 = -2.302208198f, k1 = -0.1029432397f;
  float e = __builtin_amdgcn_exp2f(x * (k0 + k1 * x * x));
  return x * __builtin_amdgcn_rcpf(1.f + e);
}
DI void dma16(const void* gsrc, u32 lds_dst) {
  u32 keep;
  asm volatile("s_mov_b32 %0, m0\n\ts_mov_b32 m0, %2\n\ts_nop 0\n\tglobal_load_lds_dwordx4 %1, off nt\n\ts_mov_b32 m0, %0"
               : "=&s"(keep) : "v"(gsrc), "s"(lds_dst) : "memory");
}
DI void dma16_sc1(const void* gsrc, u32 lds_dst) {
  u32 keep;
  asm volatile("s_mov_b32 %0, m0\n\ts_mov_b32 m0, %2\n\ts_nop 0\n\tglobal_load_lds_dwordx4 %1, off sc1\n\ts_mov_b32 m0, %0"
               : "=&s"(keep) : "v"(gsrc), "s"(lds_dst) : "memory");
}
#define VMWAIT(n) asm volatile("s_waitcnt vmcnt(" #n ")" ::: "memory")
#define BAR() asm volatile("s_waitcnt lgkmcnt(0)\n\ts_barrier" ::: "memory")
#define SCHED() __builtin_amdgcn_sched_barrier(0)
DI v4u ld16(rsrc_t r, int voff, int soff) { return __builtin_amdgcn_raw_buffer_load_b128(r, voff, soff, SC1); }
DI v2u ld8(rsrc_t r, int voff, int soff) { return __builtin_amdgcn_raw_buffer_load_b64(r, voff, soff, SC1); }
DI u32 ld4(rsrc_t r, int voff, int soff) { return __builtin_amdgcn_raw_buffer_load_b32(r, voff, soff, SC1); }
DI void st16(rsrc_t r, v4u v, int voff, int soff) { __builtin_amdgcn_raw_buffer_store_b128(v, r, voff + soff, 0, SC1); }
DI void st8(rsrc_t r, v2u v, int voff, int soff) { __builtin_amdgcn_raw_buffer_store_b64(v, r, voff, soff, SC1); }
DI void st4(rsrc_t r, u32 v, int voff, int soff) { __builtin_amdgcn_raw_buffer_store_b32(v, r, voff, soff, SC1); }
DI float xsum16_32(float v) {
  float u = v;
  asm volatile("s_nop 1\n\tv_permlane16_swap_b32 %0, %1" : "+v"(u), "+v"(v));
  v += u; u = v;
  asm volatile("s_nop 1\n\tv_permlane32_swap_b32 %0, %1" : "+v"(u), "+v"(v));
  return v + u;
}
template <int CTRL> DI float dppadd(float v) {
  return v + __builtin_bit_cast(float, __builtin_amdgcn_mov_dpp(__builtin_bit_cast(int, v), CTRL, 0xf, 0xf, true));
}
DI float xsum8(float v) { return dppadd<0x141>(dppadd<0x4E>(dppadd<0xB1>(v))); }
#define PIN(v) asm volatile("" : "+v"(v))

#define STAMP(i)
#define STAMPA(i)
#define STAMPW(i)
#define STAMPL(i)
struct Ctx {
  char* ws; rsrc_t rs; LAS char* lds; u64 epoch;
  int tid, lane, w, g, c;
  DI u64 tag() const { u64 e = epoch; asm volatile("" : "+v"(e)); return e + 0x9E3779B97F4A7C15ull; }
};
DI void refresh(Ctx& x) {
  int l = x.lane;
  asm volatile("" : "+v"(l));
  x.lane = l; x.g = l >> 4; x.c = l & 15;
}
DI LAS char* pinp(LAS char* p) { asm volatile("" : "+v"(p)); return p; }
DI u32 lds_ld(LAS char* p) { return __hip_atomic_load((LAS u32*)p, __ATOMIC_RELAXED, __HIP_MEMORY_SCOPE_WORKGROUP); }
#define LSLP 1
#define PSLP 1
DI void lds_wait_ge(LAS char* p, u32 want) {
#pragma unroll 1
  for (int spin = 0; spin < (1 << 20); ++spin) {
    if (__builtin_amdgcn_readfirstlane(lds_ld(p)) >= want) break;
    __builtin_amdgcn_s_sleep(LSLP);
  }
  asm volatile("" ::: "memory");
}
DI void lds_inc(const Ctx& x, int word) {
  if (x.lane == 0) __hip_atomic_fetch_add((LAS u32*)(x.lds + L_CTL + word), 1u, __ATOMIC_RELAXED, __HIP_MEMORY_SCOPE_WORKGROUP);
}
template <int REP = 1, int STR = 0> DI bool publish(const Ctx& x, int idx, int flag, int stamp, int NW = 8) {
  __builtin_amdgcn_s_waitcnt(0x0070);
  asm volatile("s_waitcnt vmcnt(0) lgkmcnt(0)" ::: "memory");
  u32 old = 0;
  if (x.lane == 0) old = __hip_atomic_fetch_add((LAS u32*)(x.lds + L_CTL + C_PUB + 4 * idx), 1u, __ATOMIC_RELAXED, __HIP_MEMORY_SCOPE_WORKGROUP);
  const bool last = __builtin_amdgcn_readfirstlane(old) == NW - 1;
  if (last) {
    if (x.lane < 8 * REP)
      __hip_atomic_store((GAS u64*)(x.ws + flag_at(flag + (x.lane >> 3) * STR) + (x.lane & 7) * 8), x.tag(), __ATOMIC_RELAXED, __HIP_MEMORY_SCOPE_AGENT);
    STAMPW(stamp);
  }
  asm volatile("" ::: "memory");
  return last;
}
DI void soft_barrier(const Ctx& x, int word, u32 target) {
  asm volatile("s_waitcnt lgkmcnt(0)" ::: "memory");
  lds_inc(x, word);
#pragma unroll 1
  for (int spin = 0; spin < (1 << 20); ++spin)
    if (__builtin_amdgcn_readfirstlane(lds_ld(x.lds + L_CTL + word)) >= target) break;
  asm volatile("" ::: "memory");
}
template <int N, int SLP = PSLP> DI void poll(const Ctx& x, const int (&off)[N]) {
  const u64 tag = x.tag();
#pragma unroll 1
  for (int spin = 0; spin < (1 << 16); ++spin) {
    u64 v[N];
#pragma unroll
    for (int i = 0; i < N; ++i) v[i] = __hip_atomic_load((GAS u64*)(x.ws + flag_at(off[i])), __ATOMIC_RELAXED, __HIP_MEMORY_SCOPE_AGENT);
    bool ok = true;
#pragma unroll
    for (int i = 0; i < N; ++i) ok &= v[i] == tag;
    if (__all(ok)) break;
    __builtin_amdgcn_s_sleep(SLP);
  }
  __builtin_amdgcn_fence(__ATOMIC_ACQUIRE, "wavefront");
  asm volatile("" ::: "memory");
}
DI void lds_st(LAS char* p, u32 v) { __hip_atomic_store((LAS u32*)p, v, __ATOMIC_RELAXED, __HIP_MEMORY_SCOPE_WORKGROUP); }
template <int N, int DELAY> DI void wait_flags(const Ctx& x, bool poller, const int (&off)[N], u32 gen) {
  if (poller) {
    if (DELAY) __builtin_amdgcn_s_sleep(DELAY);
    poll<N>(x, off);
    lds_st(x.lds + L_CTL + C_GO, gen);
  } else lds_wait_ge(x.lds + L_CTL + C_GO, gen);
}
#define LANDED(n) do { VMWAIT(n); lds_inc(x, C_LAND); } while (0)
DI void wait_landed(const Ctx& x, u32 n) { lds_wait_ge(x.lds + L_CTL + C_LAND, 4 * n); }

template <int GP> DI void group_dst(int gam, int h, int& tile, int& nb, bool& single) {
  int sup = gam / (2 * GP + 1), e = gam - sup * (2 * GP + 1);
  single = (e == GP);
  tile = 2 * sup + (e > GP ? 1 : (single ? h : 0));
  nb = single ? 2 * GP : 2 * (e > GP ? e - GP - 1 : e) + h;
}

DI v4u pack8(const float (&v)[8]) { return v4u{pk(v[0], v[1]), pk(v[2], v[3]), pk(v[4], v[5]), pk(v[6], v[7])}; }
DI v4u pack8(f32x4 a, f32x4 b) { return v4u{pk(a[0], a[1]), pk(a[2], a[3]), pk(b[0], b[1]), pk(b[2], b[3])}; }
DI void gen_q(int kap, int g, int n0, v4u (&qc)[10], v4u (&qs)[10]) {
  const int k = kap ? kap : 320;
  const float ac = kap ? 0.05590169944f : 0.03952847075f, as = kap ? -0.05590169944f : 0.f;
  const float kf = (float)k * (1.f / 640.f), a0 = (float)(((n0 + 8 * g) * k) % 640) * (1.f / 640.f);
  float c0[8], c1[8], s0[8], s1[8];
#pragma unroll
  for (int j = 0; j < 8; ++j) {
    const float u = a0 + (float)j * kf, v = a0 + (float)(32 + j) * kf;
    c0[j] = ac * cosr(u); s0[j] = as * sinr(u); c1[j] = ac * cosr(v); s1[j] = as * sinr(v);
  }
  const float c2 = 2.f * cosr(32.f * kf);
#pragma unroll
  for (int ks = 0; ks < 10; ks += 2) {
    qc[ks] = pack8(c0); qs[ks] = pack8(s0); qc[ks + 1] = pack8(c1); qs[ks + 1] = pack8(s1);
    if (ks + 2 < 10) {
#pragma unroll
      for (int j = 0; j < 8; ++j) {
        c0[j] = c2 * c1[j] - c0[j]; c1[j] = c2 * c0[j] - c1[j];
        s0[j] = c2 * s1[j] - s0[j]; s1[j] = c2 * s0[j] - s1[j];
      }
    }
  }
#pragma unroll
  for (int i = 0; i < 10; ++i) { PIN(qc[i]); PIN(qs[i]); }
}

#define XF 4
DI void phase_a(const Ctx& x, const u16* xin, int mm, int q4, int nt) {
  const int w = x.w, lane = x.lane, g = x.g, c = x.c;
  const int lam = 8 * w + (lane >> 3);
  const int srow = 256 * mm + 32 * (4 * ((lam >> 4) & 1) + (lam & 3)) + 8 * q4 + 4 * (lam >> 5) + ((lam >> 2) & 3);
  const char* src = (const char*)xin + srow * 1280 + (((lane & 7) ^ (lam & 7)) << 4);
  auto fetch = [&](int k0, int k1) {
#pragma unroll
    for (int kc = k0; kc < k1; ++kc) {
      const int kr = kc + 2 * nt;
      dma16(src + (kr < 10 ? kr : kr - 10) * 128, (u32)(uintptr_t)x.lds + (kc * 8 + w) * 1024);
    }
  };
  fetch(0, XF);
  v4u q[10];
  {
    const int slot = 128 * nt + 16 * w + c, k = slot ? slot >> 1 : 320;
    const float amp = slot > 1 ? 0.05590169944f : slot ? 0.f : 0.03952847075f;
    const float kf = (float)k * (1.f / 640.f), a0 = (float)(((128 * nt + 8 * g) * k) % 640) * (1.f / 640.f) + ((c & 1) ? 0.25f : 0.f);
    float v0[8], v1[8];
#pragma unroll
    for (int j = 0; j < 8; ++j) { v0[j] = amp * cosr(a0 + (float)j * kf); v1[j] = amp * cosr(a0 + (float)(32 + j) * kf); }
    const float c2 = 2.f * cosr(32.f * kf);
#pragma unroll
    for (int ks = 0; ks < 10; ks += 2) {
      q[ks] = pack8(v0); q[ks + 1] = pack8(v1);
      if (ks + 2 < 10) {
#pragma unroll
        for (int j = 0; j < 8; ++j) { v0[j] = c2 * v1[j] - v0[j]; v1[j] = c2 * v0[j] - v1[j]; }
      }
    }
#pragma unroll
    for (int i = 0; i < 10; ++i) PIN(q[i]);
  }
  fetch(XF, 10);
  f32x4 acc[2][4];
#pragma unroll
  for (int i = 0; i < 8; ++i) acc[i >> 2][i & 3] = f32x4{0.f, 0.f, 0.f, 0.f};
  LAS char* a0 = x.lds + (c >> 3) * 1024 + (c & 7) * 128 + ((g ^ (c & 7)) << 4);
  LAS char* a1 = x.lds + (c >> 3) * 1024 + (c & 7) * 128 + (((g + 4) ^ (c & 7)) << 4);
  v4u f[8];
  SCHED();
  STAMP(36);
  VMWAIT(8); BAR(); STAMP(1);
#pragma unroll
  for (int i = 0; i < 8; ++i) f[i] = *(LAS v4u*)((i < 4 ? a0 : a1) + (i & 3) * 2048);
#pragma unroll
  for (int kc = 0; kc < 10; ++kc) {
    if (kc == 1) { VMWAIT(5); asm volatile("s_barrier" ::: "memory"); }
    if (kc == 4) { VMWAIT(0); asm volatile("s_barrier" ::: "memory"); }
    if (kc == 4) { a0 = pinp(a0 + 40960); a1 = pinp(a1 + 40960); }
    SCHED();
#pragma unroll
    for (int i = 0; i < 8; ++i) {
      acc[kc / 5][i & 3] = mfma(f[i], q[(2 * kc + (i >> 2)) % 10], acc[kc / 5][i & 3]);
      if (kc < 9) f[i] = *(LAS v4u*)((i < 4 ? a0 : a1) + ((kc + 1) % 5) * 8192 + (i & 3) * 2048);
      SCHED();
    }
  }
  STAMP(37);
  STAMPA(L_CTL + 256 + (x.w == 7 ? 58 * 4 : 256));
  const float sg = (c & 2) ? -1.f : 1.f;
#pragma unroll
  for (int i = 0; i < 4; ++i) acc[0][i] += sg * acc[1][i];
  BAR();
  LAS char* rp = pinp(x.lds + L_MISC + 1024 + 4 * g * 4);
  const int off = O_ZT + ((nt * 32 + mm * 4 + q4) * 32 + 4 * w + (c >> 2)) * 512 + (c & 3) * 64 + g * 16;
#pragma unroll
  for (int u = 0; u < 2; ++u)
    st16(x.rs, pack8(acc[0][2 * u] * *(LAS f32x4*)(rp + u * 128), acc[0][2 * u + 1] * *(LAS f32x4*)(rp + u * 128 + 64)), off, u * 256);
  if (w == 0 || (w == 1 && nt == 0))
    st4(x.rs, *(LAS u32*)(x.lds + L_MISC + 1280 + w * 256 + lane * 4), (w ? O_MU : O_SIG + nt * 8192) + ((mm * 4 + q4) * 64 + lane) * 4, 0);
}

DI void phase_a_stats(const Ctx& x) {
  const int lw = x.w & 3, g = x.g, c = x.c;
  LAS char* a0 = x.lds + lw * 2048 + (c >> 3) * 1024 + (c & 7) * 128 + ((g ^ (c & 7)) << 4);
  LAS char* a1 = x.lds + lw * 2048 + (c >> 3) * 1024 + (c & 7) * 128 + (((g + 4) ^ (c & 7)) << 4);
  const v4u ones = {0x3f803f80u, 0x3f803f80u, 0x3f803f80u, 0x3f803f80u};
  f32x4 gr = {0.f, 0.f, 0.f, 0.f}, sm = gr;
  __builtin_amdgcn_s_setprio(2);
#pragma unroll
  for (int st = 0; st < 3; ++st) {
    constexpr int lo[4] = {0, 2, 5, 10};
    STAMPL(54 + st);
    BAR();
    STAMPL(51 + st);
    if (st == 2) { a0 = pinp(a0 + 40960); a1 = pinp(a1 + 40960); }
    v4u f[10];
#pragma unroll
    for (int kc = lo[st]; kc < lo[st + 1]; ++kc) {
      f[2 * (kc - lo[st])] = *(LAS v4u*)(a0 + (kc % 5) * 8192);
      f[2 * (kc - lo[st]) + 1] = *(LAS v4u*)(a1 + (kc % 5) * 8192);
    }
#pragma unroll
    for (int j = 0; j < 2 * (lo[st + 1] - lo[st]); ++j) { gr = mfma(f[j], f[j], gr); sm = mfma(ones, f[j], sm); }
    SCHED();
  }
  STAMPL(57);
  const int r = c & 3;
  const float q = xsum16_32((c >> 2) == g ? (r == 0 ? gr[0] : r == 1 ? gr[1] : r == 2 ? gr[2] : gr[3]) : 0.f);
  const float mu = sm[0] * (1.f / 640.f);
  const float var = fmaxf(q * (1.f / 640.f) - mu * mu, 0.f) + EPS;
  const float rstd = __builtin_amdgcn_rsqf(var);
  if (g < 3) *(LAS float*)(x.lds + L_MISC + 1024 + g * 256 + (16 * lw + c) * 4) = g == 0 ? rstd : g == 1 ? var * rstd : mu;
  __builtin_amdgcn_s_setprio(0);
  STAMPL(47);
  BAR();
}
DI int stat_at(int n1, int n2) {
  return ((n1 >> 3) * 4 + (n2 >> 3)) * 64 + (2 * ((n2 >> 2) & 1) + ((n1 >> 2) & 1)) * 16 + 4 * (n2 & 3) + (n1 & 3);
}

DI void side_wt(const Ctx& x, const u16* wr, const u16* wi, int v, int lo, int sb, int pub) {
  const int fq = v >> 3, fg = v & 7, lw = x.w & 3;
#pragma unroll
  for (int i = 0; i < 2; ++i) {
    const char* src = (const char*)(i ? wi : wr) + (4 * fq + lw) * 41088 + fg * 5136;
#pragma unroll
    for (int pc = 0; pc < 6; ++pc) dma16(src + min(pc * 64 + x.lane, 320) * 16, (u32)(uintptr_t)x.lds + lo + (lw * 2 + i) * 6144 + pc * 1024);
  }
  VMWAIT(0);
  soft_barrier(x, sb, 4);
  STAMPL(48);
  const int c8 = x.lane & 7, kk = x.lane >> 3;
  const int rho = (fg >> 1) * 128 + fq * 16 + (fg & 1) * 8 + c8;
#pragma unroll 2
  for (int it = 0; it < 10; ++it) {
    const int j = it * 4 + lw, kap = (j >> 3) * 64 + kk * 8 + (j & 7);
    LAS const u16* sp = (LAS const u16*)(x.lds + lo) + c8 * 321 + (kap ? kap : 320);
    v4u o;
#pragma unroll
    for (int r = 0; r < 4; ++r) o[r] = (u32)sp[r * 6144] | ((u32)sp[r * 6144 + 3072] << 16);
    st16(x.rs, o, O_WT + kap * 8192 + rho * 16, 0);
  }
  STAMPL(46);
  publish(x, pub, F_W + v, 2, 4);
}
template <int GP> DI int blk_addr(const Ctx& x, int gam0, int rb, int& tile, int& nb) {
  const int p = x.c & 3, r0 = 8 * x.g + (x.c >> 2);
  bool single;
  group_dst<GP>(gam0 + (rb >> 1), rb & 1, tile, nb, single);
  const int ch = (rb >> 1) * 4 + (single ? 2 * (rb & 1) + (p >> 1) : p), hb = single ? (p & 1) : (rb & 1);
  return r0 * 128 + ((ch ^ (((r0 >> 1) & 1) << 2)) << 4) + hb * 8;
}
DI void tile_pos(int j, int& ks, int& ct) {
  const bool is3 = j < 540;
  const int jj = j - (is3 ? 0 : j < 740 ? 540 : 740);
  ks = (jj * (is3 ? 2428 : 6554)) >> 16;
  ct = jj - ks * (is3 ? 27 : 10);
}
DI void tile_dma(const Ctx& x, int j, int lds_off, const u16* w2, const u16* w3, const u16* w4) {
  const int row = x.lane >> 3, ch = ((x.lane & 7) ^ (((row >> 1) & 1) << 2)) << 4, rs = j < 540 ? 3456 : 1280;
  int ks, ct;
  tile_pos(j, ks, ct);
  const char* src = (const char*)(j < 540 ? w3 : j < 740 ? w2 : w4) + (ks * 32 * rs + ct * 128) + (row * rs + ch);
#pragma unroll
  for (int i = 0; i < 4; ++i) dma16(src + i * 8 * rs, (u32)(uintptr_t)x.lds + lds_off + i * 1024);
}
DI void tile_out(const Ctx& x, int j, int lds_off) {
  const bool is3 = j < 540;
  const int GP = is3 ? 3 : 2, tb = is3 ? 143360 : j < 740 ? 102400 : 276480, base = is3 ? O_B6 : j < 740 ? O_B5 : O_B7;
  int ks, ct;
  tile_pos(j, ks, ct);
  const int p = x.c & 3, r0 = 8 * x.g + (x.c >> 2), sw = ((r0 >> 1) & 1) << 6;
  LAS char* lp = x.lds + lds_off + r0 * 128;
  const v4u ones = {0x3f803f80u, 0x3f803f80u, 0x3f803f80u, 0x3f803f80u};
  v4u f[4];
  int dst[4];
#pragma unroll
  for (int rb = 0; rb < 4; ++rb) {
    const int gam = 2 * ct + (rb >> 1), h = rb & 1, sup = (gam * (is3 ? 37 : 52)) >> 8, e = gam - sup * (2 * GP + 1);
    const bool single = e == GP, hi = e > GP;
    dst[rb] = (2 * sup + (int)(hi || (single && h))) * tb + (ks * (2 * GP + 1) + (single ? 2 * GP : 2 * (e - (hi ? GP + 1 : 0)) + h)) * 1024;
    LAS char* ad = lp + (((rb >> 1) * 64 + (single ? h * 32 + p * 8 : p * 16 + h * 8)) ^ sw);
    const v2u lo = trread(ad), hi2 = trread(ad + 512);
    f[rb] = v4u{lo.x, lo.y, hi2.x, hi2.y};
  }
#pragma unroll
  for (int rb = 0; rb < 4; ++rb) st16(x.rs, f[rb], x.lane * 16, base + dst[rb]);
  if (is3) {
#pragma unroll
    for (int rb = 0; rb < 4; ++rb) {
      const f32x4 s = mfma(f[rb], ones, f32x4{0.f, 0.f, 0.f, 0.f});
      if (x.c == 0) st16(x.rs, __builtin_bit_cast(v4u, s), x.g * 16, O_CS3 + (dst[rb] >> 4));
    }
  }
}
DI void side_qc(const Ctx& x, int job, int njobs) {
#pragma unroll 1
  for (int pc = job; pc < 800; pc += njobs) {
    const int ks = pc / 40, blk = pc - ks * 40;
    int tile, nb; bool single;
    group_dst<2>(blk >> 1, blk & 1, tile, nb, single);
    const int n = (blk >> 1) * 32 + (single ? 16 * (blk & 1) + x.c : 8 * (x.c >> 2) + 4 * (blk & 1) + (x.c & 3));
    const float a0 = (float)((n * (16 * ks + 4 * x.g)) % 640) * (1.f / 640.f), da = (float)n * (1.f / 640.f);
    v4u f;
#pragma unroll
    for (int i = 0; i < 4; ++i) {
      const float a = a0 + (float)i * da;
      f[i] = pk(0.05590169944f * cosr(a), -0.05590169944f * sinr(a));
    }
    if (ks == 0 && x.g == 0) f[0] = pk((n & 1) ? -0.03952847075f : 0.03952847075f, 0.f);
    st16(x.rs, f, x.lane * 16, O_BC + (8 + tile) * 102400 + (ks * 5 + nb) * 1024);
  }
}
DI void side_w1p(const Ctx& x, const u16* w1, int ct, int nt) {
  {
    const int row = 8 * x.w + (x.lane >> 3);
    const char* src = (const char*)w1 + row * 1280 + ct * 128 + (((x.lane & 7) ^ (((row >> 1) & 1) << 2)) << 4);
#pragma unroll
    for (int i = 0; i < 10; ++i) {
      const int kr = i + 2 * nt;
      dma16(src + (kr < 10 ? kr : kr - 10) * 64 * 1280, (u32)(uintptr_t)x.lds + (i * 8 + x.w) * 1024);
    }
  }
  v4u qc[10], qs[10];
  gen_q(64 * nt + 16 * (x.w & 3) + x.c, x.g, 128 * nt, qc, qs);
  f32x4 acc[2][2][2];
#pragma unroll
  for (int i = 0; i < 8; ++i) acc[i >> 2][(i >> 1) & 1][i & 1] = f32x4{0.f, 0.f, 0.f, 0.f};
  int addr[2], tile[2], nb[2];
#pragma unroll
  for (int rb = 0; rb < 2; ++rb) addr[rb] = blk_addr<2>(x, 2 * ct, 2 * (x.w >> 2) + rb, tile[rb], nb[rb]);
  SCHED();
  VMWAIT(0);
  soft_barrier(x, C_SB, 8);
  STAMP(1);
  v2u lo[4], hi[4];
#pragma unroll
  for (int i = 0; i < 4; ++i) { lo[i] = trread(x.lds + addr[i & 1] + (i >> 1) * 4096); hi[i] = trread(x.lds + addr[i & 1] + (i >> 1) * 4096 + 512); }
#pragma unroll
  for (int kc = 0; kc < 10; ++kc) {
    if (kc == 4) { addr[0] += 40960; addr[1] += 40960; PIN(addr[0]); PIN(addr[1]); }
#pragma unroll
    for (int i = 0; i < 4; ++i) {
      const int qi = (2 * kc + (i >> 1)) % 10;
      const v4u f = {lo[i].x, lo[i].y, hi[i].x, hi[i].y};
      acc[kc / 5][i & 1][0] = mfma(qc[qi], f, acc[kc / 5][i & 1][0]);
      acc[kc / 5][i & 1][1] = mfma(qs[qi], f, acc[kc / 5][i & 1][1]);
      if (kc < 9) {
        LAS char* ad = x.lds + addr[i & 1] + ((kc + 1) % 5) * 8192 + (i >> 1) * 4096;
        lo[i] = trread(ad); hi[i] = trread(ad + 512);
      }
      SCHED();
    }
  }
  const f32x4 sg = {1.f, -1.f, 1.f, -1.f};
#pragma unroll
  for (int rb = 0; rb < 2; ++rb) {
    const f32x4 re = acc[0][rb][0] + sg * acc[1][rb][0], im = acc[0][rb][1] + sg * acc[1][rb][1];
    st16(x.rs, v4u{pk(re[0], im[0]), pk(re[1], im[1]), pk(re[2], im[2]), pk(re[3], im[3])}, x.lane * 16,
         O_BC + tile[rb] * 102400 + ((4 * nt + (x.w & 3)) * 5 + nb[rb]) * 1024);
  }
}
#define TGATE 0
DI void side_jobs(const Ctx& x, int xcd, int sl, const u16* wr, const u16* wi, const u16* w1, const u16* w2, const u16* w3, const u16* w4) {
  const int y = sl < 2 ? sl * 8 + xcd : (sl < 7 && xcd >= 2) ? 16 + (sl - 2) * 6 + (xcd - 2) : -1;
  const int ct = sl >= 7 ? xcd : 8 + xcd, nt = sl >= 7 ? sl - 7 : sl - 2;
  const u64 t0 = __builtin_amdgcn_s_memrealtime();
  BAR();
  if (y < 0 && x.w < 8) {
    side_w1p(x, w1, ct, nt);
    STAMP(5);
    publish<8, 96>(x, 1, F_S + sl * 8 + xcd, 3);
    return;
  }
  const bool two = y >= 18;
  if (y >= 0 && (x.w >= 8 || (!two && x.w < 4))) {
    const bool ld = x.w >= 8;
    side_wt(x, wr, wi, ld ? y : 46 + y, ld ? 0 : 49152, ld ? C_SB0 : C_SB1, ld ? 0 : 5);
  } else {
    if (y >= 0) {
      side_qc(x, two ? 72 + (y - 18) * 8 + x.w : y * 4 + x.w - 4, 296);
      publish<8, 96>(x, 1, F_S + sl * 8 + xcd, 3, two ? 8 : 4);
    }
    lds_wait_ge(x.lds + L_CTL + (y >= 0 ? C_PUB : C_SB), y >= 0 ? 4 : 8);
    const int n = y < 0 ? 4 : two ? 2 : 1;
    const int j = y < 0 ? ((ct * 5 + nt) * 4 + (x.w & 3)) * 4 : two ? 800 + ((y - 18) * 8 + x.w) * 2 : min(1248 + y * 4 + x.w - 4, 1279);
    const int lo = y < 0 ? L_TJ + (x.w & 3) * 16384 : two ? 49152 + x.w * 8192 : 81920 + x.w * 4096;
    STAMPW(59);
#pragma unroll 1
    for (int k = 0; k < n; ++k) tile_dma(x, j + k, lo + k * 4096, w2, w3, w4);
    VMWAIT(0);
    STAMPW(61);
#pragma unroll 1
    for (int k = 0; k < 64 && (u32)(__builtin_amdgcn_s_memrealtime() - t0) < TGATE; ++k) __builtin_amdgcn_s_sleep(4);
#pragma unroll 1
    for (int k = 0; k < n; ++k) tile_out(x, j + k, lo + k * 4096);
    STAMPW(62);
  }
  publish<8, 96>(x, 6, F_T + sl * 8 + xcd, 47, y < 0 ? 4 : 12);
  if (x.w >= 8) {
    lds_wait_ge(x.lds + L_CTL + C_PUB + 24, y < 0 ? 4 : 12);
    if (y < 0) lds_wait_ge(x.lds + L_CTL + C_PUB + 4, 8);
  }
}

DI v4u negv(v4u a) { return a ^ 0x80008000u; }
DI void phase_b(const Ctx& x, int p) {
  const int w = x.w, lane = x.lane, g = x.g, c = x.c, wq = w & 3, col = w >> 2;
  v4u C64[2], S64[2], N64[2], C32[2], S32[2], N32[2];
  f32x4 tc[2], ts[2];
  {
    const float f1 = (float)(16 * wq + c);
#pragma unroll
    for (int ks = 0; ks < 2; ++ks) {
      float cv[8], sv[8];
#pragma unroll
      for (int j = 0; j < 8; ++j) { const float a = f1 * (float)(32 * ks + 8 * g + j) * (1.f / 64.f); cv[j] = cosr(a); sv[j] = sinr(a); }
      C64[ks] = v4u{pk(cv[0], cv[1]), pk(cv[2], cv[3]), pk(cv[4], cv[5]), pk(cv[6], cv[7])};
      S64[ks] = v4u{pk(sv[0], sv[1]), pk(sv[2], sv[3]), pk(sv[4], sv[5]), pk(sv[6], sv[7])};
      N64[ks] = negv(S64[ks]);
#pragma unroll
      for (int j = 0; j < 8; ++j) {
        const float a = (float)(16 * ks + c) * (float)(16 * (j >> 2) + 4 * g + (j & 3)) * (1.f / 32.f);
        cv[j] = cosr(a); sv[j] = sinr(a);
      }
      C32[ks] = v4u{pk(cv[0], cv[1]), pk(cv[2], cv[3]), pk(cv[4], cv[5]), pk(cv[6], cv[7])};
      S32[ks] = v4u{pk(sv[0], sv[1]), pk(sv[2], sv[3]), pk(sv[4], sv[5]), pk(sv[6], sv[7])};
      N32[ks] = negv(S32[ks]);
#pragma unroll
      for (int r = 0; r < 4; ++r) {
        const float a = f1 * (float)(16 * ks + 4 * g + r) * (1.f / 2048.f);
        tc[ks][r] = 0.02209708691f * cosr(a); ts[ks][r] = 0.02209708691f * sinr(a);
      }
    }
  }
  const int nt = p >> 5, kap = 2 * p + col;
#pragma unroll
  for (int i = 0; i < 2; ++i) { PIN(C64[i]); PIN(S64[i]); PIN(N64[i]); PIN(C32[i]); PIN(S32[i]); PIN(N32[i]); PIN(tc[i]); PIN(ts[i]); }
  STAMP(4);
  lds_wait_ge(x.lds + L_CTL + C_GOW, 1);
  v4u wv[2];
#pragma unroll
  for (int t = 0; t < 2; ++t) wv[t] = ld16(x.rs, O_WT + kap * 8192 + (wq * 2 + t) * 1024 + lane * 16, 0);
  const int zo = O_ZT + ((nt * 32 + 4 * w + (lane >> 5)) * 32 + (p & 31)) * 512 + (lane & 31) * 16;
  LAS char* xd = pinp(x.lds + L_XS + ((lane >> 2) & 3) * 4096 + (w >> 2) * 1024 + ((w & 3) * 16 + 4 * (lane >> 4) + (lane & 3)) * 16);
  LAS char* xs = pinp(x.lds + L_XS + col * 8192 + lane * 16);
  const int so = O_SIG + nt * 8192 + stat_at(16 * wq + 4 * g, 16 * col + c) * 4;
  STAMP(5);
  lds_wait_ge(x.lds + L_CTL + C_GO, 2);
  STAMP(6);
  const v4u l0 = ld16(x.rs, zo, 0), l1 = ld16(x.rs, zo, 32768);
  const f32x4 sg = __builtin_bit_cast(f32x4, ld16(x.rs, so, 0));
  *(LAS v4u*)xd = l0;
  *(LAS v4u*)(xd + 2048) = l1;
  soft_barrier(x, C_SB0, 8);
  STAMP(50);
  v4u xr[2][2], xi[2][2];
#pragma unroll
  for (int i = 0; i < 4; ++i) {
    xr[i >> 1][i & 1] = *(LAS v4u*)(xs + i * 1024);
    xi[i >> 1][i & 1] = *(LAS v4u*)(xs + 4096 + i * 1024);
  }
  SCHED();
  const f32x4 zero = {0.f, 0.f, 0.f, 0.f};
  f32x4 ar[2], ai[2], br[2], bi[2];
#pragma unroll
  for (int b = 0; b < 2; ++b) {
    ar[b] = mfma(xr[b][0], C64[0], zero); ai[b] = mfma(xi[b][0], C64[0], zero);
    ar[b] = mfma(xi[b][0], S64[0], ar[b]); ai[b] = mfma(xr[b][0], N64[0], ai[b]);
    ar[b] = mfma(xr[b][1], C64[1], ar[b]); ai[b] = mfma(xi[b][1], C64[1], ai[b]);
    ar[b] = mfma(xi[b][1], S64[1], ar[b]); ai[b] = mfma(xr[b][1], N64[1], ai[b]);
  }
#pragma unroll
  for (int b = 0; b < 2; ++b) { br[b] = ar[b] * tc[b] + ai[b] * ts[b]; bi[b] = ai[b] * tc[b] - ar[b] * ts[b]; }
  v4u pr = pack8(br[0], br[1]), pi = pack8(bi[0], bi[1]);
  STAMP(49);
#pragma unroll
  for (int t = 0; t < 2; ++t) {
    ar[t] = mfma(C32[t], pr, zero); ai[t] = mfma(C32[t], pi, zero);
    ar[t] = mfma(S32[t], pi, ar[t]); ai[t] = mfma(N32[t], pr, ai[t]);
  }
#pragma unroll
  for (int t = 0; t < 2; ++t)
#pragma unroll
    for (int r = 0; r < 4; ++r) {
      const float wre = blo(wv[t][r]), wim = bhi(wv[t][r]);
      br[t][r] = ar[t][r] * wre - ai[t][r] * wim;
      bi[t][r] = ar[t][r] * wim + ai[t][r] * wre;
    }
  pr = pack8(br[0], br[1]); pi = pack8(bi[0], bi[1]);
#pragma unroll
  for (int t = 0; t < 2; ++t) {
    ar[t] = mfma(C32[t], pr, zero); ai[t] = mfma(C32[t], pi, zero);
    ar[t] = mfma(N32[t], pi, ar[t]); ai[t] = mfma(S32[t], pr, ai[t]);
  }
  {
    LAS char* img = pinp(x.lds + L_XB + col * 9216 + (16 * wq + c) * 72 + g * 8);
#pragma unroll
    for (int t = 0; t < 2; ++t) {
      f32x4 ur = ar[t] * tc[t] - ai[t] * ts[t], ui = ai[t] * tc[t] + ar[t] * ts[t];
      *(LAS v2u*)(img + t * 32) = v2u{pk(ur[0], ur[1]), pk(ur[2], ur[3])};
      *(LAS v2u*)(img + 4608 + t * 32) = v2u{pk(ui[0], ui[1]), pk(ui[2], ui[3])};
    }
  }
  STAMP(38);
  soft_barrier(x, C_SB, 8);
  STAMP(39);
  LAS char* rd = pinp(x.lds + L_XB + (8 * g + (c >> 2)) * 72 + col * 32 + (c & 3) * 8);
#pragma unroll
  for (int cc = 0; cc < 2; ++cc) {
    ar[cc] = zero; ai[cc] = zero;
#pragma unroll
    for (int ks = 0; ks < 2; ++ks) {
      v2u r0 = trread(rd + cc * 9216 + ks * 2304), r1 = trread(rd + cc * 9216 + ks * 2304 + 288);
      v2u i0 = trread(rd + cc * 9216 + 4608 + ks * 2304), i1 = trread(rd + cc * 9216 + 4608 + ks * 2304 + 288);
      const v4u ur = {r0.x, r0.y, r1.x, r1.y}, ui = {i0.x, i0.y, i1.x, i1.y};
      ar[cc] = mfma(C64[ks], ur, ar[cc]); ai[cc] = mfma(C64[ks], ui, ai[cc]);
      ar[cc] = mfma(N64[ks], ui, ar[cc]); ai[cc] = mfma(S64[ks], ur, ai[cc]);
    }
  }
  v2u z[4];
  {
    LAS char* zp = pinp(x.lds + L_XS + (col * 2 + (wq >> 1)) * 1024 + ((2 * (wq & 1) + (g >> 1)) * 16 + c) * 16 + (g & 1) * 8);
#pragma unroll
    for (int i = 0; i < 4; ++i) z[i] = *(LAS v2u*)(zp + i * 4096);
  }
  if (p == 0) ai[0] = zero;
  u32 o[8];
#pragma unroll
  for (int r = 0; r < 4; ++r) {
    float zz[4];
#pragma unroll
    for (int i = 0; i < 4; ++i) zz[i] = (r & 1) ? bhi(z[i][r >> 1]) : blo(z[i][r >> 1]);
    o[2 * r] = pk(sg[r] * zz[0] + ar[0][r], sg[r] * zz[1] + ai[0][r]);
    o[2 * r + 1] = pk(sg[r] * zz[2] + ar[1][r], sg[r] * zz[3] + ai[1][r]);
  }
  const int ob = O_HB + p * 16384 + (((8 * wq + 2 * g) * 2 + col) * 16 + c) * 16;
  st16(x.rs, v4u{o[0], o[1], o[2], o[3]}, ob, 0);
  st16(x.rs, v4u{o[4], o[5], o[6], o[7]}, ob + 512, 0);
}

#define GATE 0
#define GATEW(i) do { if (GATE == 1) lds_wait_ge(x.lds + L_CTL + C_PUB + 4 * (i), 8); if (GATE == 2) lds_wait_ge(x.lds + L_CTL + C_GO, (i) + 2); } while (0)
#define DLY 6
#define DLYA 6
#define DLYB 10
#define PACE 6
#define STR2(x) #x
#define STR(x) STR2(x)
DI void dma_lin(const Ctx& x, int ws_off, int lds_off, int p0, int p1) {
  const char* src = x.ws + ws_off + x.lane * 16;
#pragma unroll 1
  for (int pc = p0 + (x.w & 3); pc < p1; pc += 4) {
    dma16_sc1(src + pc * 1024, (u32)(uintptr_t)x.lds + lds_off + pc * 1024);
    asm volatile("s_waitcnt vmcnt(" STR(PACE) ")" ::: "memory");
  }
}
DI int ft_pair(int rowblk, int KS, int gam, int g, int c) { return (rowblk * KS + gam) * 1024 + (g * 16 + c) * 16; }
DI int ft_single(int rowblk, int KS, int gam, int h, int g, int c) {
  return (rowblk * KS + gam) * 1024 + ((2 * h + (g >> 1)) * 16 + c) * 16 + (g & 1) * 8;
}

constexpr int L_EX128 = 100 * 1024, L_EX64 = 92 * 1024;
template <int NB, bool HPLANES>
DI void gemm128(const Ctx& x, int aoff, u32 land, f32x4 (&acc)[2][NB], float& ssq) {
  constexpr int LA = 4, EXW = NB == 5 ? 6 : 7;
  const int kw = x.w >> 2;
  v4u a[10][2];
  auto lda = [&](int it) {
    if (HPLANES) { a[it][0] = ld16(x.rs, aoff, it * 262144); a[it][1] = ld16(x.rs, aoff, it * 262144 + 16384); }
    else { a[it][0] = ld16(x.rs, aoff, it * 2048); a[it][1] = ld16(x.rs, aoff, it * 2048 + 20480); }
  };
#pragma unroll
  for (int it = 0; it < LA; ++it) lda(it);
  SCHED();
  wait_landed(x, land);
  STAMP(NB == 5 ? 26 : 28);
#pragma unroll
  for (int i = 0; i < 2; ++i)
#pragma unroll
    for (int n = 0; n < NB; ++n) acc[i][n] = f32x4{0.f, 0.f, 0.f, 0.f};
  f32x4 gr0 = {0.f, 0.f, 0.f, 0.f}, gr1 = gr0;
  LAS char* bp = x.lds + kw * NB * 1024 + x.lane * 16;
  v4u f[NB];
#pragma unroll
  for (int n = 0; n < NB; ++n) f[n] = *(LAS v4u*)(bp + n * 1024);
#pragma unroll
  for (int it = 0; it < 10; ++it) {
    SCHED();
    if (it + LA < 10) lda(it + LA);
    v4u a0 = a[it][0], a1 = a[it][1];
    if (HPLANES) {
      a0 = v4u{a[it][0].x, a[it][0].y, a[it][1].x, a[it][1].y};
      a1 = v4u{a[it][0].z, a[it][0].w, a[it][1].z, a[it][1].w};
    }
    SCHED();
#pragma unroll
    for (int n = 0; n < NB; ++n) {
      acc[0][n] = mfma(f[n], a0, acc[0][n]);
      acc[1][n] = mfma(f[n], a1, acc[1][n]);
      if (n == 0 && (it & 3) == 3) bp = pinp(bp + 8 * NB * 1024);
      if (it < 9) f[n] = *(LAS v4u*)(bp + (((it + 1) & 3) * 2 * NB + n) * 1024);
      if (HPLANES && n == 0) { gr0 = mfma(a0, a0, gr0); gr1 = mfma(a1, a1, gr1); }
      SCHED();
    }
  }
  float s0 = 0.f, s1 = 0.f;
  if (HPLANES) {
    const int r = x.c & 3;
    const bool dg = (x.c >> 2) == x.g;
    s0 = dg ? (r == 0 ? gr0[0] : r == 1 ? gr0[1] : r == 2 ? gr0[2] : gr0[3]) : 0.f;
    s1 = dg ? (r == 0 ? gr1[0] : r == 1 ? gr1[1] : r == 2 ? gr1[2] : gr1[3]) : 0.f;
  }
  BAR();
  STAMP(NB == 5 ? 12 : 20);
  LAS char* mine = pinp(x.lds + L_EX128 + x.w * EXW * 1024 + x.lane * 16);
  LAS char* other = pinp(x.lds + L_EX128 + (x.w ^ 4) * EXW * 1024 + x.lane * 16);
  if (kw) {
#pragma unroll
    for (int n = 0; n < NB; ++n) { *(LAS f32x4*)(mine + n * 1024) = acc[0][n]; acc[0][n] = acc[1][n]; }
    if (HPLANES) { *(LAS float*)(mine + NB * 1024) = s0; s0 = s1; }
  } else {
#pragma unroll
    for (int n = 0; n < NB; ++n) *(LAS f32x4*)(mine + n * 1024) = acc[1][n];
    if (HPLANES) *(LAS float*)(mine + NB * 1024) = s1;
  }
  BAR();
  STAMP(NB == 5 ? 32 : 34);
  if (HPLANES) ssq = xsum16_32(s0 + *(LAS float*)(other + NB * 1024));
#pragma unroll
  for (int n = 0; n < NB; ++n) acc[0][n] += *(LAS f32x4*)(other + n * 1024);
}

template <int KS>
DI void gemm64(const Ctx& x, int a0off, int a1off, u32 land, f32x4 (&o)[3]) {
  constexpr int NIT = (KS + 3) / 4, LA = KS == 20 ? 5 : 4;
  const int kw = x.w >> 1;
  v4u a[NIT][2];
  constexpr int AS = KS == 20 ? 1024 : 4096, BS = KS == 20 ? 5120 : 20480;
  auto lda = [&](int it) { a[it][0] = ld16(x.rs, a0off, it * AS); a[it][1] = ld16(x.rs, a1off, it * AS); };
#pragma unroll
  for (int it = 0; it < LA; ++it) lda(it);
  SCHED();
  wait_landed(x, land);
  STAMP(KS == 20 ? 27 : 29);
  f32x4 acc[2][5];
#pragma unroll
  for (int i = 0; i < 2; ++i)
#pragma unroll
    for (int n = 0; n < 5; ++n) acc[i][n] = f32x4{0.f, 0.f, 0.f, 0.f};
  LAS char* bs = x.lds + kw * (KS == 20 ? 25600 : 5120) + x.lane * 16;
  LAS char* bq[3] = {bs + (kw >> 1) * 2048, bs + ((kw >> 1) ^ 1) * 2048, bs + 4096};
  v4u f[5];
#pragma unroll
  for (int n = 0; n < 5; ++n) f[n] = *(LAS v4u*)(bq[n >> 1] + (n & 1) * 1024);
#pragma unroll
  for (int it = 0; it < NIT; ++it) {
    SCHED();
    if (KS == 54 && (it == 3 || it == 6 || it == 10)) BAR();
    if (it + LA < NIT) lda(it + LA);
    const int st = (it + 1) % 7;
    if (KS == 54 && st % 3 == 0) {
#pragma unroll
      for (int n = 0; n < 3; ++n) bq[n] = pinp(bq[n] + (st ? 61440 : -122880));
    }
    SCHED();
    if (it * 4 + 3 < KS || kw < KS - it * 4) {
#pragma unroll
      for (int n = 0; n < 5; ++n) {
        acc[0][n] = mfma(f[n], a[it][0], acc[0][n]);
        acc[1][n] = mfma(f[n], a[it][1], acc[1][n]);
        if (it + 1 < NIT) f[n] = *(LAS v4u*)(bq[n >> 1] + (KS == 54 ? st % 3 : st) * BS + (n & 1) * 1024);
        SCHED();
      }
    }
  }
  BAR();
  STAMP(KS == 20 ? 16 : 24);
  LAS char* mine = pinp(x.lds + L_EX64 + x.w * 8192 + x.lane * 16 - 2048);
#pragma unroll
  for (int n = 2; n < 5; ++n) *(LAS f32x4*)(mine + n * 1024) = acc[0][n];
#pragma unroll
  for (int n = 0; n < 5; ++n) *(LAS f32x4*)(mine + (5 + n) * 1024) = acc[1][n];
  BAR();
  STAMP(KS == 20 ? 33 : 35);
  o[0] = acc[0][0]; o[1] = acc[0][1]; o[2] = acc[0][4];
#pragma unroll
  for (int m = 1; m < 4; ++m) {
    LAS char* s = pinp(x.lds + L_EX64 + (x.w ^ (2 * m)) * 8192 + x.lane * 16 - 2048) + (m & 1) * 5120;
    o[0] += *(LAS f32x4*)(s + (m >> 1) * 2048);
    o[1] += *(LAS f32x4*)(s + (m >> 1) * 2048 + 1024);
    o[2] += *(LAS f32x4*)(s + 4096);
  }
}

DI f32x4 gelu4(f32x4 v) { return f32x4{gelu(v[0]), gelu(v[1]), gelu(v[2]), gelu(v[3])}; }
DI v2u pack4(f32x4 a) { return v2u{pk(a[0], a[1]), pk(a[2], a[3])}; }
DI f32x4 unlo(v4u v) { return f32x4{blo(v.x), bhi(v.x), blo(v.y), bhi(v.y)}; }
DI f32x4 unhi(v4u v) { return f32x4{blo(v.z), bhi(v.z), blo(v.w), bhi(v.w)}; }
DI f32x4 un4(v2u v) { return f32x4{blo(v.x), bhi(v.x), blo(v.y), bhi(v.y)}; }

DI void phase_c(const Ctx& x, int P, int t) {
  const int mw = x.w & 3, kw = x.w >> 2, g = x.g, c = x.c;
  const int np = 2 * P + (mw >> 1), b = mw & 1;
  const int rowblk = 4 * np + b + 2 * kw;
  const float mu = __builtin_bit_cast(float, ld4(x.rs, O_MU + stat_at(rowblk >> 1, 16 * (rowblk & 1) + c) * 4, 0));
  f32x4 acc[2][5];
  float ssq;
  gemm128<5, true>(x, O_HB + (8 * kw + 2 * g) * 16384 + ((np * 2 + b) * 16 + c) * 16, 1, acc, ssq);
  const float vh = ssq * (1.f / 640.f);
  float sc = __builtin_amdgcn_rsqf(vh + EPS);
  if (g == 0) *(LAS f32x2*)(x.lds + L_MISC + 1024 + ((rowblk & 7) * 16 + c) * 8) = f32x2{(vh + EPS) * sc, mu};
  const int t8 = t & 7, g0 = (t8 >> 1) * 5 + (t8 & 1) * 3, gs = (t8 >> 1) * 5 + 2;
  if (t < 8) {
    sc *= __builtin_amdgcn_rsqf(vh * sc * sc + EPS);
#pragma unroll
    for (int n = 0; n < 5; ++n) acc[0][n] = gelu4(acc[0][n] * sc);
  } else {
#pragma unroll
    for (int n = 0; n < 5; ++n) acc[0][n] *= sc;
  }
  const int dst = t < 8 ? O_G : O_XB;
#pragma unroll
  for (int i = 0; i < 2; ++i) st16(x.rs, pack8(acc[0][2 * i], acc[0][2 * i + 1]), dst + ft_pair(rowblk, 20, g0 + i, g, c), 0);
  st8(x.rs, pack4(acc[0][4]), dst + ft_single(rowblk, 20, gs, t8 & 1, g, c), 0);
}

DI void phase_5(const Ctx& x, int P, int hf, int t8, u32 (&res)[6]) {
  const int mw = x.w & 1, kw = x.w >> 1, g = x.g, c = x.c;
  const int rb0 = 8 * P + 4 * hf + 2 * mw, rowblk = rb0 + (kw & 1);
  const int gp = (t8 >> 1) * 5 + (t8 & 1) * 3 + (kw >> 1), gs = (t8 >> 1) * 5 + 2;
  const int op = ft_pair(rowblk, 20, gp, g, c), os = ft_single(rowblk, 20, gs, t8 & 1, g, c);
  const int sh = (kw & 1 ? -1 : 1) * (1 + (kw >> 1));
  int ao[2];
#pragma unroll
  for (int i = 0; i < 2; ++i) {
    const int r = (rb0 + ((kw & 1) ^ i)) * 16 + c - sh;
    ao[i] = (u32)r < 2048u ? O_G + ft_pair(r >> 4, 20, 5 * kw, g, r & 15) : 0x7f000000;
  }
  const v4u xp = ld16(x.rs, O_XB + op, 0);
  const v2u xs = ld8(x.rs, O_XB + os, 0);
  f32x4 o[3];
  gemm64<20>(x, ao[0], ao[1], 2, o);
  const f32x2 st = *(LAS f32x2*)(x.lds + L_MISC + 1024 + ((rowblk & 7) * 16 + c) * 8);
  const f32x4 b0 = unlo(xp), b1 = unhi(xp), b2 = un4(xs);
  o[0] += b0; o[1] += b1; o[2] += b2;
  const v4u r = pack8(o[0], o[1]);
  st16(x.rs, r, O_X1 + op, 0);
  const v4u ones = {0x3f803f80u, 0x3f803f80u, 0x3f803f80u, 0x3f803f80u};
  float s1 = dot8(r, ones, 0.f), s2 = dot8(r, r, 0.f);
  if (kw < 2) {
    const v2u q = pack4(o[2]);
    st8(x.rs, q, O_X1 + os, 0);
    s1 = dot2(q.y, ones.x, dot2(q.x, ones.x, s1));
    s2 = dot2(q.y, q.y, dot2(q.x, q.x, s2));
  }
  s1 = xsum16_32(s1); s2 = xsum16_32(s2);
  if (g == 0)
    st8(x.rs, v2u{__builtin_bit_cast(u32, s1), __builtin_bit_cast(u32, s2)}, O_ST1 + (rowblk * 16 + t8 * 2 + (kw >> 1)) * 128 + c * 8, 0);
  const v4u rp = pack8(o[0] + b0 * st[0] + st[1], o[1] + b1 * st[0] + st[1]);
  const v2u rq = pack4(o[2] + b2 * st[0] + st[1]);
  res[0] = rp.x; res[1] = rp.y; res[2] = rp.z; res[3] = rp.w; res[4] = rq.x; res[5] = rq.y;
#pragma unroll
  for (int i = 0; i < 6; ++i) PIN(res[i]);
}

DI void phase_6(const Ctx& x, int P, int t) {
  const int mw = x.w & 3, kw = x.w >> 2, g = x.g, c = x.c;
  const int rb0 = 8 * P + 2 * mw, rowblk = rb0 + kw;
  f32x2 p[4];
#pragma unroll
  for (int i = 0; i < 4; ++i) p[i] = __builtin_bit_cast(f32x2, ld8(x.rs, O_ST1 + rowblk * 2048 + x.lane * 8 + i * 512, 0));
  f32x4 acc[2][7];
  float dummy;
  gemm128<7, false>(x, O_X1 + (rb0 * 20 + kw) * 1024 + x.lane * 16, 3, acc, dummy);
  const f32x2 ps = (p[0] + p[1]) + (p[2] + p[3]);
  const float m1 = xsum16_32(ps[0]) * (1.f / 640.f), q1 = xsum16_32(ps[1]) * (1.f / 640.f);
  const float r1 = __builtin_amdgcn_rsqf(fmaxf(q1 - m1 * m1, 0.f) + EPS), nm = -m1 * r1;
  const int g0 = (t >> 1) * 7 + (t & 1) * 4, gs = (t >> 1) * 7 + 3;
  LAS f32x4* cs = (LAS f32x4*)pinp(x.lds + L_MISC + g * 16);
#pragma unroll
  for (int i = 0; i < 3; ++i) {
    const v4u r = pack8(gelu4(acc[0][2 * i] * r1 + cs[8 * i] * nm), gelu4(acc[0][2 * i + 1] * r1 + cs[8 * i + 4] * nm));
    if (g0 + i < 54) st16(x.rs, r, O_U + ft_pair(rowblk, 54, g0 + i, g, c), 0);
  }
  st8(x.rs, pack4(gelu4(acc[0][6] * r1 + cs[24] * nm)), O_U + ft_single(rowblk, 54, gs, t & 1, g, c), 0);
}
DI void colsum_6(const Ctx& x, int t) {
  if (x.lane < 56) {
    f32x2 s = {0.f, 0.f};
#pragma unroll
    for (int k = 0; k < 20; ++k) s += __builtin_bit_cast(f32x2, ld8(x.rs, O_CS3 + t * 8960 + x.lane * 8 + k * 448, 0));
    *(LAS f32x2*)(x.lds + L_MISC + x.lane * 8) = s;
  }
}

DI void phase_7(const Ctx& x, int P, int hf, int t8, const u32 (&res)[6], u16* out) {
  const int mw = x.w & 1, kw = x.w >> 1, g = x.g, c = x.c;
  const int rb0 = 8 * P + 4 * hf + 2 * mw, rowblk = rb0 + (kw & 1);
  const int gp = (t8 >> 1) * 5 + (t8 & 1) * 3 + (kw >> 1), gs = (t8 >> 1) * 5 + 2;
  f32x4 o[3];
  gemm64<54>(x, O_U + (rowblk * 54 + kw) * 1024 + x.lane * 16, O_U + ((rowblk ^ 1) * 54 + kw) * 1024 + x.lane * 16, 4, o);
  char* orow = (char*)out + (rowblk * 16 + c) * 1280;
  const rsrc_t ro = __builtin_amdgcn_make_buffer_rsrc(out, 0, ACT, 0x00020000);
  const int oo = (rowblk * 16 + c) * 1280;
  st16(ro, pack8(o[0] + un4(v2u{res[0], res[1]}), o[1] + un4(v2u{res[2], res[3]})), oo + gp * 64 + g * 16, 0);
  if (kw < 2) st8(ro, pack4(o[2] + un4(v2u{res[4], res[5]})), oo + gs * 64 + (t8 & 1) * 32 + g * 8, 0);
}

DI void wait_b(const Ctx& x, bool poller, int xcd) {
  wait_flags<3, DLYB>(x, poller, {F_B + xcd * 160 + x.lane, F_B + xcd * 160 + 64 + x.lane, F_B + xcd * 160 + 128 + (x.lane & 31)}, 3);
}
#define CODE_KB 44
#define MAP 1
__global__ __launch_bounds__(NTHR, 3) void fused(const u16* __restrict__ xin, const u16* __restrict__ wr, const u16* __restrict__ wi,
                                                 const u16* __restrict__ w1, const u16* __restrict__ w2, const u16* __restrict__ w3,
                                                 const u16* __restrict__ w4, u16* __restrict__ out, char* ws) {
  extern __shared__ __attribute__((aligned(16))) char smem[];
  u64 diag_t0;
  asm volatile("s_memrealtime %0" : "=s"(diag_t0) :: "memory");
  __builtin_amdgcn_sched_barrier(0);
  if (blockIdx.x >= 240 && threadIdx.x >= 512) {
    u64 pc;
    asm volatile("s_getpc_b64 %0" : "=s"(pc));
    const char* code = (const char*)(pc & ~255ull) + (threadIdx.x - 512) * 16;
#pragma unroll 1
    for (int k = 0; k < CODE_KB / 4; ++k) dma16_sc1(code + k * 4096, (u32)(uintptr_t)(LAS char*)smem + L_MISC + 2048);
  }
  Ctx x;
  x.ws = ws; x.rs = __builtin_amdgcn_make_buffer_rsrc(ws, 0, WS_END, 0x00020000); x.lds = (LAS char*)smem;
  x.tid = threadIdx.x; x.lane = x.tid & 63; x.w = __builtin_amdgcn_readfirstlane(x.tid >> 6); x.g = x.lane >> 4; x.c = x.lane & 15;
  GAS u64* ep = (GAS u64*)(ws + O_EPOCH);
  x.epoch = __hip_atomic_load(ep, __ATOMIC_RELAXED, __HIP_MEMORY_SCOPE_AGENT);
  const int b = blockIdx.x, xcd = b & 7, i = b >> 3;
  const int P = 4 * (xcd >> 1) + (i >> 3), t = 8 * (xcd & 1) + (i & 7), hf = (i >> 2) & 1, t8 = 4 * (xcd & 1) + (i & 3);
  u64 diag_tx = diag_t0;
  STAMP(0);
  if (x.w == 8 && x.lane < 16) lds_st(x.lds + L_CTL + x.lane * 4, 0);
  if (b >= 160) side_jobs(x, xcd, i - 20, wr, wi, w1, w2, w3, w4);
  if (x.w >= 8) {
    if (b < 160) {
      VMWAIT(0);
      phase_a_stats(x);
      if (x.w == 9) {
        lds_wait_ge(x.lds + L_CTL + C_PUB, 8);
        wait_flags<1, DLYA>(x, true, {F_A + (b >> 5) * 32 + (x.lane & 31)}, 2);
      }
      if (x.w == 10) { poll<1, 4>(x, {F_W + x.lane}); lds_st(x.lds + L_CTL + C_GOW, 1); }
      if (x.w == 8) lds_wait_ge(x.lds + L_CTL + C_GO, 2);
    }
    if (x.w == 8) {
      poll<2, 16>(x, {F_S + xcd * 96 + x.lane, F_S + xcd * 96 + 64 + (x.lane & 31)});
      if (b < 160) lds_wait_ge(x.lds + L_CTL + C_SB0, 8);
      lds_st(x.lds + L_CTL + C_LGO, 1);
      STAMPW(9);
    } else lds_wait_ge(x.lds + L_CTL + C_LGO, 1);
    dma_lin(x, O_BC + t * 102400, 0, 0, 100);
    LANDED(0);
    STAMPL(40);
    if (x.w == 8) {
      poll<2, 16>(x, {F_T + xcd * 96 + x.lane, F_T + xcd * 96 + 64 + (x.lane & 31)});
      colsum_6(x, t);
    }
    STAMPL(44);
    BAR();
    dma_lin(x, O_B5 + t8 * 102400, 0, 0, 4);
    BAR();
    GATEW(2);
    dma_lin(x, O_B5 + t8 * 102400, 0, 4, 100);
    STAMPL(45);
    LANDED(0);
    STAMPL(41);
    BAR();
    dma_lin(x, O_B6 + t * 143360, 0, 0, 4);
    BAR();
    GATEW(3);
    dma_lin(x, O_B6 + t * 143360, 0, 4, 92);
    lds_wait_ge(x.lds + L_CTL + C_PUB + 12, 8);
    dma_lin(x, O_B6 + t * 143360, 0, 92, 140);
    LANDED(0);
    STAMPL(42);
    BAR();
    dma_lin(x, O_B7 + t8 * 276480, 0, 0, 4);
    BAR();
    GATEW(4);
    dma_lin(x, O_B7 + t8 * 276480, 0, 4, 100);
    lds_wait_ge(x.lds + L_CTL + C_PUB + 16, 8);
    dma_lin(x, O_B7 + t8 * 276480, 0, 100, 140);
    LANDED(0);
    STAMPL(43);
    BAR();
    dma_lin(x, O_B7 + t8 * 276480 + 140 * 1024, 0, 0, 80);
    VMWAIT(0);
    BAR();
    dma_lin(x, O_B7 + t8 * 276480 + 140 * 1024, 0, 80, 130);
    VMWAIT(0);
    BAR();
    return;
  }
  bool last;
  if (b < 160) {
    phase_a(x, xin, xcd, i & 3, i >> 2);
    STAMP(2);
    publish(x, 0, F_A + (i >> 2) * 32 + xcd * 4 + (i & 3), 3);
    refresh(x);
    phase_b(x, b);
    STAMP(7);
    last = publish<8, 160>(x, 1, F_B + b, 8);
  } else {
    last = x.w == 0;
    if (last) poll<1, 8>(x, {F_B + xcd * 160 + i});
  }
  wait_b(x, last, xcd);
  STAMP(11);
  refresh(x);
  phase_c(x, P, t);
  STAMP(13);
  last = publish(x, 2, F_C + P * 16 + t, 14);
  wait_flags<1, DLY>(x, last, {F_C + (x.lane < 32 ? P * 16 + x.c : ((P + (hf ? 1 : 15)) & 15) * 16 + (x.lane & 7))}, 4);
  STAMP(15);
  refresh(x);
  u32 res[6];
  phase_5(x, P, hf, t8, res);
  STAMP(17);
  last = publish(x, 3, F_5 + P * 16 + t, 18);
  wait_flags<1, DLY>(x, last, {F_5 + P * 16 + x.c}, 5);
  STAMP(19);
  refresh(x);
  phase_6(x, P, t);
  STAMP(21);
  last = publish(x, 4, F_6 + P * 16 + t, 22);
  wait_flags<1, DLY>(x, last, {F_6 + P * 16 + x.c}, 6);
  STAMP(23);
  refresh(x);
  phase_7(x, P, hf, t8, res, out);
  STAMP(25);
  if (b == 0 && x.tid == 0) __hip_atomic_store(ep, x.tag(), __ATOMIC_RELAXED, __HIP_MEMORY_SCOPE_AGENT);
  if (b == 0 && x.w == 0) {
    for (int k = 0; k < 100000; ++k) { if ((u32)(__builtin_amdgcn_s_memrealtime() - diag_tx) >= 6000) break; __builtin_amdgcn_s_sleep(1); }
  }
}

#define WSOFF 0
extern "C" void kernel_launch(void* const* d_in, const int* in_sizes, int n_in, void* d_out, int out_size, void* d_ws,
                              size_t ws_size, hipStream_t stream) {
  (void)hipFuncSetAttribute((const void*)fused, hipFuncAttributeMaxDynamicSharedMemorySize, LDS_BYTES);
  fused<<<256, NTHR, LDS_BYTES, stream>>>((const u16*)d_in[0], (const u16*)d_in[1], (const u16*)d_in[2], (const u16*)d_in[4],
                                          (const u16*)d_in[5], (const u16*)d_in[6], (const u16*)d_in[7], (u16*)d_out, (char*)d_ws + WSOFF);
}
```

```cpp
#include <hip/hip_runtime.h>
#include <stdint.h>

typedef unsigned u32;
typedef unsigned long long u64;
typedef unsigned short u16;
typedef u32 v4u __attribute__((ext_vector_type(4)));
typedef u32 v2u __attribute__((ext_vector_type(2)));
typedef float f32x4 __attribute__((ext_vector_type(4)));
typedef float f32x2 __attribute__((ext_vector_type(2)));
typedef __bf16 bf16x8 __attribute__((ext_vector_type(8)));
typedef __bf16 bf16x2 __attribute__((ext_vector_type(2)));
typedef short s16x4 __attribute__((ext_vector_type(4)));
typedef __amdgpu_buffer_rsrc_t rsrc_t;
#define LAS __attribute__((address_space(3)))
#define GAS __attribute__((address_space(1)))
#define DI __device__ __forceinline__

constexpr int ACT = 2048 * 640 * 2;
constexpr int O_EPOCH = 0;
constexpr int O_FLAG = 4096;
enum { F_W = 160, F_C = 240, F_5 = 496, F_6 = 752, F_X = 1008, F_S = 1104, F_T = F_S + 8 * 50, F_B = F_T + 8 * 96, F_A = F_B + 8 * 160, F_END = F_A + 8 * 160 };
__host__ __device__ constexpr int flag_at(int i) { return O_FLAG + i * 256; }
constexpr int O_SIG = 2 << 20;
static_assert(flag_at(F_END) <= O_SIG, "flag area");
constexpr int O_MU = O_SIG + 5 * 8192;
constexpr int O_CS3 = O_MU + 8192;
constexpr int O_ST1 = O_CS3 + 20 * 1792 * 4;
constexpr int O_ZT = O_ST1 + 2048 * 128;
constexpr int O_WT = O_ZT + ACT;
#define HPL 16384
constexpr int O_HB = O_WT + ACT;
constexpr int O_BC = O_HB + ACT + 131072;
constexpr int O_B5 = O_BC + 16 * 102400 + 8192;
constexpr int O_B6 = O_B5 + 8 * 102400 + 8192;
constexpr int O_B7 = O_B6 + 16 * 143360 + 8192;
constexpr int O_G = O_B7 + 8 * 276480 + 8192;
constexpr int O_XB = O_G + ACT;
constexpr int O_X1 = O_XB + ACT;
constexpr int O_U = O_X1 + ACT;
constexpr int WS_END = O_U + 2048 * 1728 * 2;

constexpr int LDS_BYTES = 160 * 1024;
constexpr int L_MISC = 156 * 1024;
constexpr int L_CTL = 159 * 1024;
constexpr int L_TJ = 100 * 1024;
constexpr int L_XS = 116 * 1024;
constexpr int L_XB = 132 * 1024;
enum { C_SB = 4, C_GO = 8, C_LAND = 12, C_LGO = 16, C_GOW = 20, C_SB0 = 24, C_SB1 = 28, C_PUB = 32, C_FREE = 64, C_MID = 84, C_EX = 96, C_EXD = 128, C_RING = 136, C_RL = 164 };
constexpr int NTHR = 768;
constexpr float EPS = 1e-5f;
constexpr int SC1 = 16;

DI u32 pk(float a, float b) { f32x2 v = {a, b}; return __builtin_bit_cast(u32, __builtin_convertvector(v, bf16x2)); }
DI float blo(u32 v) { return __builtin_bit_cast(float, v << 16); }
DI float bhi(u32 v) { return __builtin_bit_cast(float, v & 0xffff0000u); }
DI f32x4 mfma(v4u a, v4u b, f32x4 c) {
  return __builtin_amdgcn_mfma_f32_16x16x32_bf16(__builtin_bit_cast(bf16x8, a), __builtin_bit_cast(bf16x8, b), c, 0, 0, 0);
}
DI float dot2(u32 a, u32 b, float c) {
  return __builtin_amdgcn_fdot2_f32_bf16(__builtin_bit_cast(bf16x2, a), __builtin_bit_cast(bf16x2, b), c, false);
}
DI float dot8(v4u a, v4u b, float c) { return dot2(a.w, b.w, dot2(a.z, b.z, dot2(a.y, b.y, dot2(a.x, b.x, c)))); }
DI v2u trread(LAS char* p) { return __builtin_bit_cast(v2u, __builtin_amdgcn_ds_read_tr16_b64_v4i16((LAS s16x4*)p)); }
DI float cosr(float rev) { return __builtin_amdgcn_cosf(rev); }
DI float sinr(float rev) { return __builtin_amdgcn_sinf(rev); }
DI float gelu(float x) {
  const float k0 = -2.302208198f, k1 = -0.1029432397f;
  float e = __builtin_amdgcn_exp2f(x * (k0 + k1 * x * x));
  return x * __builtin_amdgcn_rcpf(1.f + e);
}
DI void dma16(const void* gsrc, u32 lds_dst) {
  u32 keep;
  asm volatile("s_mov_b32 %0, m0\n\ts_mov_b32 m0, %2\n\ts_nop 0\n\tglobal_load_lds_dwordx4 %1, off nt\n\ts_mov_b32 m0, %0"
               : "=&s"(keep) : "v"(gsrc), "s"(lds_dst) : "memory");
}
DI void dma16_sc1(const void* gsrc, u32 lds_dst) {
  u32 keep;
  asm volatile("s_mov_b32 %0, m0\n\ts_mov_b32 m0, %2\n\ts_nop 0\n\tglobal_load_lds_dwordx4 %1, off sc1\n\ts_mov_b32 m0, %0"
               : "=&s"(keep) : "v"(gsrc), "s"(lds_dst) : "memory");
}
#define VMWAIT(n) asm volatile("s_waitcnt vmcnt(" #n ")" ::: "memory")
#define BAR() asm volatile("s_waitcnt lgkmcnt(0)\n\ts_barrier" ::: "memory")
#define SCHED() __builtin_amdgcn_sched_barrier(0)
DI v4u ld16(rsrc_t r, int voff, int soff) { return __builtin_amdgcn_raw_buffer_load_b128(r, voff, soff, SC1); }
DI v2u ld8(rsrc_t r, int voff, int soff) { return __builtin_amdgcn_raw_buffer_load_b64(r, voff, soff, SC1); }
DI u32 ld4(rsrc_t r, int voff, int soff) { return __builtin_amdgcn_raw_buffer_load_b32(r, voff, soff, SC1); }
DI void st16(rsrc_t r, v4u v, int voff, int soff) { __builtin_amdgcn_raw_buffer_store_b128(v, r, voff + soff, 0, SC1); }
DI void st8(rsrc_t r, v2u v, int voff, int soff) { __builtin_amdgcn_raw_buffer_store_b64(v, r, voff, soff, SC1); }
DI void st4(rsrc_t r, u32 v, int voff, int soff) { __builtin_amdgcn_raw_buffer_store_b32(v, r, voff, soff, SC1); }
DI float xsum16_32(float v) {
  float u = v;
  asm volatile("s_nop 1\n\tv_permlane16_swap_b32 %0, %1" : "+v"(u), "+v"(v));
  v += u; u = v;
  asm volatile("s_nop 1\n\tv_permlane32_swap_b32 %0, %1" : "+v"(u), "+v"(v));
  return v + u;
}
template <int CTRL> DI float dppadd(float v) {
  return v + __builtin_bit_cast(float, __builtin_amdgcn_mov_dpp(__builtin_bit_cast(int, v), CTRL, 0xf, 0xf, true));
}
DI float xsum8(float v) { return dppadd<0x141>(dppadd<0x4E>(dppadd<0xB1>(v))); }
#define PIN(v) asm volatile("" : "+v"(v))

#define STAMP(i)
#define STAMP2(i)
#define STAMPA(i)
#define STAMPW(i)
#define STAMPL(i)
struct Ctx {
  char* ws; rsrc_t rs; LAS char* lds; u64 epoch;
  int tid, lane, w, g, c;
  DI u64 tag() const { u64 e = epoch; asm volatile("" : "+v"(e)); return e + 0x9E3779B97F4A7C15ull; }
};
DI void refresh(Ctx& x) {
  int l = x.lane;
  asm volatile("" : "+v"(l));
  x.lane = l; x.g = l >> 4; x.c = l & 15;
}
DI LAS char* pinp(LAS char* p) { asm volatile("" : "+v"(p)); return p; }
#define LSLP 1
#define PSLP 1
template <int SLP = LSLP> DI void lds_wait_ge(LAS char* p, u32 want) {
  u32 v, n = 1u << 20;
  asm volatile("1:\n\tds_read_b32 %0, %2\n\ts_waitcnt lgkmcnt(0)\n\tv_cmp_gt_u32 vcc, %3, %0\n\ts_sub_u32 %1, %1, 1\n\ts_cbranch_scc1 2f\n\t"
               "s_cbranch_vccz 2f\n\ts_sleep %4\n\ts_branch 1b\n2:"
               : "=&v"(v), "+s"(n) : "v"(p), "s"(want), "n"(SLP) : "vcc", "scc", "memory");
}
DI void lds_wait_ge_pre(LAS char* p, u32 want, u32 v) {
  u32 n = 1u << 20;
  asm volatile("1:\n\tv_cmp_gt_u32 vcc, %3, %0\n\ts_sub_u32 %1, %1, 1\n\ts_cbranch_scc1 2f\n\ts_cbranch_vccz 2f\n\t"
               "ds_read_b32 %0, %2\n\ts_waitcnt lgkmcnt(0)\n\ts_branch 1b\n2:"
               : "+v"(v), "+s"(n) : "v"(p), "n"(want) : "vcc", "scc", "memory");
}
DI void lds_inc(const Ctx& x, int word) {
  u64 keep;
  asm volatile("s_mov_b64 %0, exec\n\ts_mov_b64 exec, 1\n\tds_add_u32 %1, %2\n\ts_mov_b64 exec, %0"
               : "=&s"(keep) : "v"(x.lds + L_CTL + word), "v"(1u) : "memory");
}
template <int REP = 1, int STR = 0> DI bool publish(const Ctx& x, int idx, int flag, int stamp, int NW = 8) {
  __builtin_amdgcn_s_waitcnt(0x0070);
  asm volatile("s_waitcnt vmcnt(0) lgkmcnt(0)" ::: "memory");
  u32 old = 0;
  if (x.lane == 0) old = __hip_atomic_fetch_add((LAS u32*)(x.lds + L_CTL + C_PUB + 4 * idx), 1u, __ATOMIC_RELAXED, __HIP_MEMORY_SCOPE_WORKGROUP);
  const bool last = __builtin_amdgcn_readfirstlane(old) == NW - 1;
  if (last) {
    if (x.lane < 8 * REP)
      __hip_atomic_store((GAS u64*)(x.ws + flag_at(flag + (x.lane >> 3) * STR) + (x.lane & 7) * 8), x.tag(), __ATOMIC_RELAXED, __HIP_MEMORY_SCOPE_AGENT);
    STAMPW(stamp);
  }
  asm volatile("" ::: "memory");
  return last;
}
DI void soft_barrier(const Ctx& x, int word, u32 target) {
  asm volatile("s_waitcnt lgkmcnt(0)" ::: "memory");
  lds_inc(x, word);
  lds_wait_ge<0>(x.lds + L_CTL + word, target);
}
template <int N, int SLP = PSLP> DI void poll(const Ctx& x, const int (&off)[N]) {
  const u64 tag = x.tag();
#pragma unroll 1
  for (int spin = 0; spin < (1 << 16); ++spin) {
    u64 v[N];
#pragma unroll
    for (int i = 0; i < N; ++i) v[i] = __hip_atomic_load((GAS u64*)(x.ws + flag_at(off[i])), __ATOMIC_RELAXED, __HIP_MEMORY_SCOPE_AGENT);
    bool ok = true;
#pragma unroll
    for (int i = 0; i < N; ++i) ok &= v[i] == tag;
    if (__all(ok)) break;
    __builtin_amdgcn_s_sleep(SLP);
  }
  __builtin_amdgcn_fence(__ATOMIC_ACQUIRE, "wavefront");
  asm volatile("" ::: "memory");
}
DI void lds_st(LAS char* p, u32 v) { __hip_atomic_store((LAS u32*)p, v, __ATOMIC_RELAXED, __HIP_MEMORY_SCOPE_WORKGROUP); }
template <int N, int DELAY, int SLP = PSLP> DI void wait_flags(const Ctx& x, bool poller, const int (&off)[N], u32 gen) {
  if (poller) {
    if (DELAY) __builtin_amdgcn_s_sleep(DELAY);
    poll<N, SLP>(x, off);
    lds_st(x.lds + L_CTL + C_GO, gen);
  } else lds_wait_ge(x.lds + L_CTL + C_GO, gen);
}
#define LANDED(n) do { VMWAIT(n); lds_inc(x, C_LAND); } while (0)
DI void wait_landed(const Ctx& x, u32 n) { lds_wait_ge(x.lds + L_CTL + C_LAND, 4 * n); }

template <int GP> DI void group_dst(int gam, int h, int& tile, int& nb, bool& single) {
  int sup = gam / (2 * GP + 1), e = gam - sup * (2 * GP + 1);
  single = (e == GP);
  tile = 2 * sup + (e > GP ? 1 : (single ? h : 0));
  nb = single ? 2 * GP : 2 * (e > GP ? e - GP - 1 : e) + h;
}

DI v4u pack8(const float (&v)[8]) { return v4u{pk(v[0], v[1]), pk(v[2], v[3]), pk(v[4], v[5]), pk(v[6], v[7])}; }
DI v4u pack8(f32x4 a, f32x4 b) { return v4u{pk(a[0], a[1]), pk(a[2], a[3]), pk(b[0], b[1]), pk(b[2], b[3])}; }
DI void gen_q(int kap, int g, int n0, v4u (&qc)[10], v4u (&qs)[10]) {
  const int k = kap ? kap : 320;
  const float ac = kap ? 0.05590169944f : 0.03952847075f, as = kap ? -0.05590169944f : 0.f;
  const float kf = (float)k * (1.f / 640.f), a0 = (float)(((n0 + 8 * g) * k) % 640) * (1.f / 640.f);
  float c0[8], c1[8], s0[8], s1[8];
#pragma unroll
  for (int j = 0; j < 8; ++j) {
    const float u = a0 + (float)j * kf, v = a0 + (float)(32 + j) * kf;
    c0[j] = ac * cosr(u); s0[j] = as * sinr(u); c1[j] = ac * cosr(v); s1[j] = as * sinr(v);
  }
  const float c2 = 2.f * cosr(32.f * kf);
#pragma unroll
  for (int ks = 0; ks < 10; ks += 2) {
    qc[ks] = pack8(c0); qs[ks] = pack8(s0); qc[ks + 1] = pack8(c1); qs[ks + 1] = pack8(s1);
    if (ks + 2 < 10) {
#pragma unroll
      for (int j = 0; j < 8; ++j) {
        c0[j] = c2 * c1[j] - c0[j]; c1[j] = c2 * c0[j] - c1[j];
        s0[j] = c2 * s1[j] - s0[j]; s1[j] = c2 * s0[j] - s1[j];
      }
    }
  }
#pragma unroll
  for (int i = 0; i < 10; ++i) { PIN(qc[i]); PIN(qs[i]); }
}

#define XF 4
DI void phase_a(const Ctx& x, const u16* xin, int mm, int q4, int nt) {
  const int w = x.w, lane = x.lane, g = x.g, c = x.c;
  const int lam = 8 * w + (lane >> 3);
  const int srow = 256 * mm + 32 * (4 * ((lam >> 4) & 1) + (lam & 3)) + 8 * q4 + 4 * (lam >> 5) + ((lam >> 2) & 3);
  const char* src = (const char*)xin + srow * 1280 + (((lane & 7) ^ (lam & 7)) << 4);
  auto fetch = [&](int k0, int k1) {
#pragma unroll
    for (int kc = k0; kc < k1; ++kc) {
      const int kr = kc + 2 * nt;
      dma16(src + (kr < 10 ? kr : kr - 10) * 128, (u32)(uintptr_t)x.lds + (kc * 8 + w) * 1024);
    }
  };
  fetch(0, XF);
  v4u q[10];
  {
    const int slot = 128 * nt + 16 * w + c, k = slot ? slot >> 1 : 320;
    const float amp = slot > 1 ? 0.05590169944f : slot ? 0.f : 0.03952847075f;
    const float kf = (float)k * (1.f / 640.f), a0 = (float)(((128 * nt + 8 * g) * k) % 640) * (1.f / 640.f) + ((c & 1) ? 0.25f : 0.f);
    float v0[8], v1[8];
#pragma unroll
    for (int j = 0; j < 8; ++j) { v0[j] = amp * cosr(a0 + (float)j * kf); v1[j] = amp * cosr(a0 + (float)(32 + j) * kf); }
    const float c2 = 2.f * cosr(32.f * kf);
#pragma unroll
    for (int ks = 0; ks < 10; ks += 2) {
      q[ks] = pack8(v0); q[ks + 1] = pack8(v1);
      if (ks + 2 < 10) {
#pragma unroll
        for (int j = 0; j < 8; ++j) { v0[j] = c2 * v1[j] - v0[j]; v1[j] = c2 * v0[j] - v1[j]; }
      }
    }
#pragma unroll
    for (int i = 0; i < 10; ++i) PIN(q[i]);
  }
  fetch(XF, 10);
  f32x4 acc[2][4];
#pragma unroll
  for (int i = 0; i < 8; ++i) acc[i >> 2][i & 3] = f32x4{0.f, 0.f, 0.f, 0.f};
  LAS char* a0 = x.lds + (c >> 3) * 1024 + (c & 7) * 128 + ((g ^ (c & 7)) << 4);
  LAS char* a1 = x.lds + (c >> 3) * 1024 + (c & 7) * 128 + (((g + 4) ^ (c & 7)) << 4);
  v4u f[8];
  SCHED();
  STAMP(36);
  VMWAIT(8); BAR(); STAMP(1);
#pragma unroll
  for (int i = 0; i < 8; ++i) f[i] = *(LAS v4u*)((i < 4 ? a0 : a1) + (i & 3) * 2048);
#pragma unroll
  for (int kc = 0; kc < 10; ++kc) {
    if (kc == 1) { VMWAIT(5); asm volatile("s_barrier" ::: "memory"); }
    if (kc == 4) { VMWAIT(0); asm volatile("s_barrier" ::: "memory"); }
    if (kc == 4) { a0 = pinp(a0 + 40960); a1 = pinp(a1 + 40960); }
    SCHED();
#pragma unroll
    for (int i = 0; i < 8; ++i) {
      acc[kc / 5][i & 3] = mfma(f[i], q[(2 * kc + (i >> 2)) % 10], acc[kc / 5][i & 3]);
      if (kc < 9) f[i] = *(LAS v4u*)((i < 4 ? a0 : a1) + ((kc + 1) % 5) * 8192 + (i & 3) * 2048);
      SCHED();
    }
  }
  STAMP(37);
  STAMPA(L_CTL + 256 + (x.w == 7 ? 58 * 4 : 512));
  const float sg = (c & 2) ? -1.f : 1.f;
#pragma unroll
  for (int i = 0; i < 4; ++i) acc[0][i] += sg * acc[1][i];
  BAR();
  LAS char* rp = pinp(x.lds + L_MISC + 1024 + 4 * g * 4);
  const int off = O_ZT + ((nt * 32 + mm * 4 + q4) * 32 + 4 * w + (c >> 2)) * 512 + (c & 3) * 64 + g * 16;
#pragma unroll
  for (int u = 0; u < 2; ++u)
    st16(x.rs, pack8(acc[0][2 * u] * *(LAS f32x4*)(rp + u * 128), acc[0][2 * u + 1] * *(LAS f32x4*)(rp + u * 128 + 64)), off, u * 256);
  if (w == 0 || (w == 1 && nt == 0))
    st4(x.rs, *(LAS u32*)(x.lds + L_MISC + 1280 + w * 256 + lane * 4), (w ? O_MU : O_SIG + nt * 8192) + ((mm * 4 + q4) * 64 + lane) * 4, 0);
}

DI void phase_a_stats(const Ctx& x) {
  const int lw = x.w & 3, g = x.g, c = x.c;
  LAS char* a0 = x.lds + lw * 2048 + (c >> 3) * 1024 + (c & 7) * 128 + ((g ^ (c & 7)) << 4);
  LAS char* a1 = x.lds + lw * 2048 + (c >> 3) * 1024 + (c & 7) * 128 + (((g + 4) ^ (c & 7)) << 4);
  const v4u ones = {0x3f803f80u, 0x3f803f80u, 0x3f803f80u, 0x3f803f80u};
  f32x4 gr = {0.f, 0.f, 0.f, 0.f}, sm = gr;
  __builtin_amdgcn_s_setprio(2);
#pragma unroll
  for (int st = 0; st < 3; ++st) {
    constexpr int lo[4] = {0, 2, 5, 10};
    BAR();
    if (st == 2) { a0 = pinp(a0 + 40960); a1 = pinp(a1 + 40960); }
    v4u f[10];
#pragma unroll
    for (int kc = lo[st]; kc < lo[st + 1]; ++kc) {
      f[2 * (kc - lo[st])] = *(LAS v4u*)(a0 + (kc % 5) * 8192);
      f[2 * (kc - lo[st]) + 1] = *(LAS v4u*)(a1 + (kc % 5) * 8192);
    }
#pragma unroll
    for (int j = 0; j < 2 * (lo[st + 1] - lo[st]); ++j) { gr = mfma(f[j], f[j], gr); sm = mfma(ones, f[j], sm); }
    SCHED();
  }
  const int r = c & 3;
  const float q = xsum16_32((c >> 2) == g ? (r == 0 ? gr[0] : r == 1 ? gr[1] : r == 2 ? gr[2] : gr[3]) : 0.f);
  const float mu = sm[0] * (1.f / 640.f);
  const float var = fmaxf(q * (1.f / 640.f) - mu * mu, 0.f) + EPS;
  const float rstd = __builtin_amdgcn_rsqf(var);
  if (g < 3) *(LAS float*)(x.lds + L_MISC + 1024 + g * 256 + (16 * lw + c) * 4) = g == 0 ? rstd : g == 1 ? var * rstd : mu;
  __builtin_amdgcn_s_setprio(0);
  STAMPL(47);
  BAR();
}
DI int stat_at(int n1, int n2) {
  return ((n1 >> 3) * 4 + (n2 >> 3)) * 64 + (2 * ((n2 >> 2) & 1) + ((n1 >> 2) & 1)) * 16 + 4 * (n2 & 3) + (n1 & 3);
}

DI void wt_dma(const Ctx& x, const u16* wr, const u16* wi, int v, int lo) {
  const int fq = v >> 4, fh = v & 15, lw = x.w & 3;
#pragma unroll
  for (int i = 0; i < 2; ++i) {
    const char* src = (const char*)(i ? wi : wr) + (4 * fq + lw) * 41088 + ((fh * 2568) & ~15);
#pragma unroll
    for (int pc = 0; pc < 3; ++pc) dma16(src + min(pc * 64 + x.lane, 160) * 16, (u32)(uintptr_t)x.lds + lo + (lw * 2 + i) * 3072 + pc * 1024);
  }
}
DI void wt_out(const Ctx& x, int v, int lo) {
  const int k0 = 2 * ((x.lane >> 3) * 20 + (x.w & 3) * 5) + ((x.lane >> 2) & 1);
  LAS const u16* sp = (LAS const u16*)(x.lds + lo) + (v & 1) * 4 + (x.lane & 3) * 321;
#pragma unroll
  for (int it = 0; it < 5; ++it) {
    LAS const u16* q = sp + ((it || k0) ? k0 + 2 * it : 320);
    v4u o;
#pragma unroll
    for (int r = 0; r < 4; ++r) o[r] = (u32)q[r * 3072] | ((u32)q[r * 3072 + 1536] << 16);
    st16(x.rs, o, O_WT + v * 20480 + ((x.w & 3) * 5 + it) * 1024 + x.lane * 16, 0);
  }
}
template <int GP> DI int blk_addr(const Ctx& x, int gam0, int rb, int& tile, int& nb) {
  const int p = x.c & 3, r0 = 8 * x.g + (x.c >> 2);
  bool single;
  group_dst<GP>(gam0 + (rb >> 1), rb & 1, tile, nb, single);
  const int ch = (rb >> 1) * 4 + (single ? 2 * (rb & 1) + (p >> 1) : p), hb = single ? (p & 1) : (rb & 1);
  return r0 * 128 + ((ch ^ (((r0 >> 1) & 1) << 2)) << 4) + hb * 8;
}
DI void tile_pos(int j, int& ks, int& ct) {
  const bool is3 = j < 540;
  const int jj = j - (is3 ? 0 : j < 740 ? 540 : 740);
  ks = (jj * (is3 ? 2428 : 6554)) >> 16;
  ct = jj - ks * (is3 ? 27 : 10);
}
DI void tile_dma(const Ctx& x, int j, int lds_off, const u16* w2, const u16* w3, const u16* w4) {
  const int row = x.lane >> 3, ch = ((x.lane & 7) ^ (((row >> 1) & 1) << 2)) << 4, rs = j < 540 ? 3456 : 1280;
  int ks, ct;
  tile_pos(j, ks, ct);
  const char* src = (const char*)(j < 540 ? w3 : j < 740 ? w2 : w4) + (ks * 32 * rs + ct * 128) + (row * rs + ch);
#pragma unroll
  for (int i = 0; i < 4; ++i) dma16(src + i * 8 * rs, (u32)(uintptr_t)x.lds + lds_off + i * 1024);
}
DI void tile_out(const Ctx& x, int j, int lds_off) {
  const bool is3 = j < 540;
  const int GP = is3 ? 3 : 2, tb = is3 ? 143360 : j < 740 ? 102400 : 276480, base = is3 ? O_B6 : j < 740 ? O_B5 : O_B7;
  int ks, ct;
  tile_pos(j, ks, ct);
  const int p = x.c & 3, r0 = 8 * x.g + (x.c >> 2), sw = ((r0 >> 1) & 1) << 6;
  LAS char* lp = x.lds + lds_off + r0 * 128;
  const v4u ones = {0x3f803f80u, 0x3f803f80u, 0x3f803f80u, 0x3f803f80u};
  v4u f[4];
  int dst[4];
#pragma unroll
  for (int rb = 0; rb < 4; ++rb) {
    const int gam = 2 * ct + (rb >> 1), h = rb & 1, sup = (gam * (is3 ? 37 : 52)) >> 8, e = gam - sup * (2 * GP + 1);
    const bool single = e == GP, hi = e > GP;
    dst[rb] = (2 * sup + (int)(hi || (single && h))) * tb + (ks * (2 * GP + 1) + (single ? 2 * GP : 2 * (e - (hi ? GP + 1 : 0)) + h)) * 1024;
    LAS char* ad = lp + (((rb >> 1) * 64 + (single ? h * 32 + p * 8 : p * 16 + h * 8)) ^ sw);
    const v2u lo = trread(ad), hi2 = trread(ad + 512);
    f[rb] = v4u{lo.x, lo.y, hi2.x, hi2.y};
  }
#pragma unroll
  for (int rb = 0; rb < 4; ++rb) st16(x.rs, f[rb], x.lane * 16, base + dst[rb]);
  if (is3) {
#pragma unroll
    for (int rb = 0; rb < 4; ++rb) {
      const f32x4 s = mfma(f[rb], ones, f32x4{0.f, 0.f, 0.f, 0.f});
      if (x.c == 0) st16(x.rs, __builtin_bit_cast(v4u, s), x.g * 16, O_CS3 + (dst[rb] >> 4));
    }
  }
}
DI void side_qc(const Ctx& x, int job, int njobs) {
#pragma unroll 1
  for (int pc = job; pc < 800; pc += njobs) {
    const int ks = pc / 40, blk = pc - ks * 40;
    int tile, nb; bool single;
    group_dst<2>(blk >> 1, blk & 1, tile, nb, single);
    const int n = (blk >> 1) * 32 + (single ? 16 * (blk & 1) + x.c : 8 * (x.c >> 2) + 4 * (blk & 1) + (x.c & 3));
    const float a0 = (float)((n * (16 * ks + 4 * x.g)) % 640) * (1.f / 640.f), da = (float)n * (1.f / 640.f);
    v4u f;
#pragma unroll
    for (int i = 0; i < 4; ++i) {
      const float a = a0 + (float)i * da;
      f[i] = pk(0.05590169944f * cosr(a), -0.05590169944f * sinr(a));
    }
    if (ks == 0 && x.g == 0) f[0] = pk((n & 1) ? -0.03952847075f : 0.03952847075f, 0.f);
    st16(x.rs, f, x.lane * 16, O_BC + (8 + tile) * 102400 + (ks * 5 + nb) * 1024);
  }
}
DI void side_w1p(const Ctx& x, const u16* w1, int ct, int nt) {
  {
    const int row = 8 * x.w + (x.lane >> 3);
    const char* src = (const char*)w1 + row * 1280 + ct * 128 + (((x.lane & 7) ^ (((row >> 1) & 1) << 2)) << 4);
#pragma unroll
    for (int i = 0; i < 10; ++i) {
      const int kr = i + 2 * nt;
      dma16(src + (kr < 10 ? kr : kr - 10) * 64 * 1280, (u32)(uintptr_t)x.lds + (i * 8 + x.w) * 1024);
    }
  }
  BAR();
  v4u qc[10], qs[10];
  gen_q(64 * nt + 16 * (x.w & 3) + x.c, x.g, 128 * nt, qc, qs);
  f32x4 acc[2][2][2];
#pragma unroll
  for (int i = 0; i < 8; ++i) acc[i >> 2][(i >> 1) & 1][i & 1] = f32x4{0.f, 0.f, 0.f, 0.f};
  int addr[2], tile[2], nb[2];
#pragma unroll
  for (int rb = 0; rb < 2; ++rb) addr[rb] = blk_addr<2>(x, 2 * ct, 2 * (x.w >> 2) + rb, tile[rb], nb[rb]);
  SCHED();
  VMWAIT(0);
  soft_barrier(x, C_SB, 8);
  STAMP(1);
  v2u lo[4], hi[4];
#pragma unroll
  for (int i = 0; i < 4; ++i) { lo[i] = trread(x.lds + addr[i & 1] + (i >> 1) * 4096); hi[i] = trread(x.lds + addr[i & 1] + (i >> 1) * 4096 + 512); }
#pragma unroll
  for (int kc = 0; kc < 10; ++kc) {
    if (kc == 4) { addr[0] += 40960; addr[1] += 40960; PIN(addr[0]); PIN(addr[1]); }
#pragma unroll
    for (int i = 0; i < 4; ++i) {
      const int qi = (2 * kc + (i >> 1)) % 10;
      const v4u f = {lo[i].x, lo[i].y, hi[i].x, hi[i].y};
      acc[kc / 5][i & 1][0] = mfma(qc[qi], f, acc[kc / 5][i & 1][0]);
      acc[kc / 5][i & 1][1] = mfma(qs[qi], f, acc[kc / 5][i & 1][1]);
      if (kc < 9) {
        LAS char* ad = x.lds + addr[i & 1] + ((kc + 1) % 5) * 8192 + (i >> 1) * 4096;
        lo[i] = trread(ad); hi[i] = trread(ad + 512);
      }
      SCHED();
    }
  }
  const f32x4 sg = {1.f, -1.f, 1.f, -1.f};
#pragma unroll
  for (int rb = 0; rb < 2; ++rb) {
    const f32x4 re = acc[0][rb][0] + sg * acc[1][rb][0], im = acc[0][rb][1] + sg * acc[1][rb][1];
    st16(x.rs, v4u{pk(re[0], im[0]), pk(re[1], im[1]), pk(re[2], im[2]), pk(re[3], im[3])}, x.lane * 16,
         O_BC + tile[rb] * 102400 + ((4 * nt + (x.w & 3)) * 5 + nb[rb]) * 1024);
  }
}
DI void side_jobs(const Ctx& x, int xcd, int sl, const u16* wr, const u16* wi, const u16* w1, const u16* w2, const u16* w3, const u16* w4) {
  const int y = sl < 2 ? sl * 8 + xcd : (sl < 7 && xcd >= 2) ? 16 + (sl - 2) * 6 + (xcd - 2) : -1;
  const int ct = sl >= 7 ? xcd : 8 + xcd, nt = sl >= 7 ? sl - 7 : sl - 2, xr = nt * 10 + ct, v = sl * 8 + xcd;
  const int j0 = y < 0 ? xr * 14 : min(700 + y * 13, 1267), nj = y < 0 ? 14 : 13;
  if (y >= 0) {
    const int job = min(y * 3 + (x.w >> 2), 127), lo = (x.w >> 2) * 24576;
    wt_dma(x, wr, wi, job, lo);
    BAR();
    if (x.w == 0) tile_dma(x, j0 + 12, L_TJ + 12 * 4096, w2, w3, w4);
    tile_dma(x, j0 + x.w, L_TJ + x.w * 4096, w2, w3, w4);
    VMWAIT(4);
    BAR();
    STAMP(1);
    wt_out(x, job, lo);
    STAMP(5);
    publish(x, 0, F_W + y, 2, 12);
  } else if (x.w < 8) {
    side_w1p(x, w1, ct, nt);
    STAMP(5);
    publish<8, 50>(x, 1, F_S + xr, 3, 12);
  } else {
    BAR();
#pragma unroll 1
    for (int k = x.w & 3; k < 14; k += 4) tile_dma(x, j0 + k, L_TJ + k * 4096, w2, w3, w4);
    side_qc(x, xr * 4 + (x.w & 3), 200);
    publish<8, 50>(x, 1, F_S + xr, 3, 12);
  }
  BAR();
  if (x.w >= 8) return;
  wait_flags<1, 0, 8>(x, x.w == 0, {F_X + v}, 1);
  STAMP(61);
#pragma unroll 1
  for (int k = x.w; k < nj; k += 8) tile_out(x, j0 + k, L_TJ + k * 4096);
  STAMP(62);
  publish<8, 96>(x, 6, F_T + v, 47);
  lds_wait_ge(x.lds + L_CTL + C_PUB + 24, 8);
}

DI v4u negv(v4u a) { return a ^ 0x80008000u; }
DI void phase_b(const Ctx& x, int p) {
  const int w = x.w, lane = x.lane, g = x.g, c = x.c, wq = w & 3, col = w >> 2;
  v4u C64[2], S64[2], N64[2], C32[2], S32[2], N32[2];
  f32x4 tc[2], ts[2];
  {
    const float f1 = (float)(16 * wq + c);
#pragma unroll
    for (int ks = 0; ks < 2; ++ks) {
      float cv[8], sv[8];
#pragma unroll
      for (int j = 0; j < 8; ++j) { const float a = f1 * (float)(32 * ks + 8 * g + j) * (1.f / 64.f); cv[j] = cosr(a); sv[j] = sinr(a); }
      C64[ks] = v4u{pk(cv[0], cv[1]), pk(cv[2], cv[3]), pk(cv[4], cv[5]), pk(cv[6], cv[7])};
      S64[ks] = v4u{pk(sv[0], sv[1]), pk(sv[2], sv[3]), pk(sv[4], sv[5]), pk(sv[6], sv[7])};
      N64[ks] = negv(S64[ks]);
#pragma unroll
      for (int j = 0; j < 8; ++j) {
        const float a = (float)(16 * ks + c) * (float)(16 * (j >> 2) + 4 * g + (j & 3)) * (1.f / 32.f);
        cv[j] = cosr(a); sv[j] = sinr(a);
      }
      C32[ks] = v4u{pk(cv[0], cv[1]), pk(cv[2], cv[3]), pk(cv[4], cv[5]), pk(cv[6], cv[7])};
      S32[ks] = v4u{pk(sv[0], sv[1]), pk(sv[2], sv[3]), pk(sv[4], sv[5]), pk(sv[6], sv[7])};
      N32[ks] = negv(S32[ks]);
#pragma unroll
      for (int r = 0; r < 4; ++r) {
        const float a = f1 * (float)(16 * ks + 4 * g + r) * (1.f / 2048.f);
        tc[ks][r] = 0.02209708691f * cosr(a); ts[ks][r] = 0.02209708691f * sinr(a);
      }
    }
  }
  const int nt = p >> 5;
#pragma unroll
  for (int i = 0; i < 2; ++i) { PIN(C64[i]); PIN(S64[i]); PIN(N64[i]); PIN(C32[i]); PIN(S32[i]); PIN(N32[i]); PIN(tc[i]); PIN(ts[i]); }
  STAMP(4);
  const int wo = O_WT + (g * 16 + 4 * wq + (c >> 2)) * 20480 + (p % 20) * 1024 + (p / 20) * 128 + col * 64 + (c & 3) * 16;
  const int zo = O_ZT + ((nt * 32 + 4 * w + (lane >> 5)) * 32 + (p & 31)) * 512 + (lane & 31) * 16;
  LAS char* xd = pinp(x.lds + L_XS + ((lane >> 2) & 3) * 4096 + (w >> 2) * 1024 + ((w & 3) * 16 + 4 * (lane >> 4) + (lane & 3)) * 16);
  LAS char* xs = pinp(x.lds + L_XS + col * 8192 + lane * 16);
  const int so = O_SIG + nt * 8192 + stat_at(16 * wq + 4 * g, 16 * col + c) * 4;
  lds_wait_ge(x.lds + L_CTL + C_GO, 2);
  STAMP(6);
  const v4u l0 = ld16(x.rs, zo, 0), l1 = ld16(x.rs, zo, 32768);
  const f32x4 sg = __builtin_bit_cast(f32x4, ld16(x.rs, so, 0));
  SCHED();
  lds_wait_ge(x.lds + L_CTL + C_GOW, 1);
  STAMP(5);
  v4u wv[2];
#pragma unroll
  for (int t = 0; t < 2; ++t) wv[t] = ld16(x.rs, wo, t * 64 * 20480);
  SCHED();
  *(LAS v4u*)xd = l0;
  *(LAS v4u*)(xd + 2048) = l1;
  soft_barrier(x, C_SB0, 8);
  STAMP(50);
  v4u xr[2][2], xi[2][2];
#pragma unroll
  for (int i = 0; i < 4; ++i) {
    xr[i >> 1][i & 1] = *(LAS v4u*)(xs + i * 1024);
    xi[i >> 1][i & 1] = *(LAS v4u*)(xs + 4096 + i * 1024);
  }
  SCHED();
  const f32x4 zero = {0.f, 0.f, 0.f, 0.f};
  f32x4 ar[2], ai[2], br[2], bi[2];
#pragma unroll
  for (int b = 0; b < 2; ++b) {
    ar[b] = mfma(xr[b][0], C64[0], zero); ai[b] = mfma(xi[b][0], C64[0], zero);
    ar[b] = mfma(xi[b][0], S64[0], ar[b]); ai[b] = mfma(xr[b][0], N64[0], ai[b]);
    ar[b] = mfma(xr[b][1], C64[1], ar[b]); ai[b] = mfma(xi[b][1], C64[1], ai[b]);
    ar[b] = mfma(xi[b][1], S64[1], ar[b]); ai[b] = mfma(xr[b][1], N64[1], ai[b]);
  }
#pragma unroll
  for (int b = 0; b < 2; ++b) { br[b] = ar[b] * tc[b] + ai[b] * ts[b]; bi[b] = ai[b] * tc[b] - ar[b] * ts[b]; }
  v4u pr = pack8(br[0], br[1]), pi = pack8(bi[0], bi[1]);
  STAMP(49);
#pragma unroll
  for (int t = 0; t < 2; ++t) {
    ar[t] = mfma(C32[t], pr, zero); ai[t] = mfma(C32[t], pi, zero);
    ar[t] = mfma(S32[t], pi, ar[t]); ai[t] = mfma(N32[t], pr, ai[t]);
  }
#pragma unroll
  for (int t = 0; t < 2; ++t)
#pragma unroll
    for (int r = 0; r < 4; ++r) {
      const float wre = blo(wv[t][r]), wim = bhi(wv[t][r]);
      br[t][r] = ar[t][r] * wre - ai[t][r] * wim;
      bi[t][r] = ar[t][r] * wim + ai[t][r] * wre;
    }
  pr = pack8(br[0], br[1]); pi = pack8(bi[0], bi[1]);
#pragma unroll
  for (int t = 0; t < 2; ++t) {
    ar[t] = mfma(C32[t], pr, zero); ai[t] = mfma(C32[t], pi, zero);
    ar[t] = mfma(N32[t], pi, ar[t]); ai[t] = mfma(S32[t], pr, ai[t]);
  }
  {
    LAS char* img = pinp(x.lds + L_XB + col * 9216 + (16 * wq + c) * 72 + g * 8);
#pragma unroll
    for (int t = 0; t < 2; ++t) {
      f32x4 ur = ar[t] * tc[t] - ai[t] * ts[t], ui = ai[t] * tc[t] + ar[t] * ts[t];
      *(LAS v2u*)(img + t * 32) = v2u{pk(ur[0], ur[1]), pk(ur[2], ur[3])};
      *(LAS v2u*)(img + 4608 + t * 32) = v2u{pk(ui[0], ui[1]), pk(ui[2], ui[3])};
    }
  }
  STAMP(38);
  soft_barrier(x, C_SB, 8);
  STAMP(39);
  LAS char* rd = pinp(x.lds + L_XB + (8 * g + (c >> 2)) * 72 + col * 32 + (c & 3) * 8);
#pragma unroll
  for (int cc = 0; cc < 2; ++cc) {
    ar[cc] = zero; ai[cc] = zero;
#pragma unroll
    for (int ks = 0; ks < 2; ++ks) {
      v2u r0 = trread(rd + cc * 9216 + ks * 2304), r1 = trread(rd + cc * 9216 + ks * 2304 + 288);
      v2u i0 = trread(rd + cc * 9216 + 4608 + ks * 2304), i1 = trread(rd + cc * 9216 + 4608 + ks * 2304 + 288);
      const v4u ur = {r0.x, r0.y, r1.x, r1.y}, ui = {i0.x, i0.y, i1.x, i1.y};
      ar[cc] = mfma(C64[ks], ur, ar[cc]); ai[cc] = mfma(C64[ks], ui, ai[cc]);
      ar[cc] = mfma(N64[ks], ui, ar[cc]); ai[cc] = mfma(S64[ks], ur, ai[cc]);
    }
  }
  v2u z[4];
  {
    LAS char* zp = pinp(x.lds + L_XS + (col * 2 + (wq >> 1)) * 1024 + ((2 * (wq & 1) + (g >> 1)) * 16 + c) * 16 + (g & 1) * 8);
#pragma unroll
    for (int i = 0; i < 4; ++i) z[i] = *(LAS v2u*)(zp + i * 4096);
  }
  if (p == 0) ai[0] = zero;
  u32 o[8];
#pragma unroll
  for (int r = 0; r < 4; ++r) {
    float zz[4];
#pragma unroll
    for (int i = 0; i < 4; ++i) zz[i] = (r & 1) ? bhi(z[i][r >> 1]) : blo(z[i][r >> 1]);
    o[2 * r] = pk(sg[r] * zz[0] + ar[0][r], sg[r] * zz[1] + ai[0][r]);
    o[2 * r + 1] = pk(sg[r] * zz[2] + ar[1][r], sg[r] * zz[3] + ai[1][r]);
  }
  const int ob = O_HB + p * HPL + (((8 * wq + 2 * g) * 2 + col) * 16 + c) * 16;
  const bool sw = p & 8;
  st16(x.rs, sw ? v4u{o[2], o[3], o[0], o[1]} : v4u{o[0], o[1], o[2], o[3]}, ob, 0);
  st16(x.rs, sw ? v4u{o[6], o[7], o[4], o[5]} : v4u{o[4], o[5], o[6], o[7]}, ob + 512, 0);
}

#define DLY 6
#define XWORD C_GO
#define XVAL 2
#define DLYA 6
#define DLYB 10
#define ABL 0
#define HOLD 0
#define LA4 2
#define LA5 5
#define LA6 2
#define LA7 2
#define PACE 6
#define STR2(x) #x
#define STR(x) STR2(x)
DI void dma_lin(const Ctx& x, int ws_off, int lds_off, int p0, int p1) {
  const char* src = x.ws + ws_off + x.lane * 16;
#pragma unroll 1
  for (int pc = p0 + (x.w & 3); pc < p1; pc += 4) {
    dma16_sc1(src + pc * 1024, (u32)(uintptr_t)x.lds + lds_off + pc * 1024);
    asm volatile("s_waitcnt vmcnt(" STR(PACE) ")" ::: "memory");
  }
}
DI int ft_pair(int rowblk, int KS, int gam, int g, int c) { return (rowblk * KS + gam) * 1024 + (g * 16 + c) * 16; }
DI int ft_single(int rowblk, int KS, int gam, int h, int g, int c) {
  return (rowblk * KS + gam) * 1024 + ((2 * h + (g >> 1)) * 16 + c) * 16 + (g & 1) * 8;
}

template <int PH, int NB, class F>
DI void gemm(const Ctx& x, int ao0, int ao1, f32x4 (&o)[NB], float& ssq, F pre) {
  constexpr int KW = (PH & 1) ? 4 : 2, NIT = PH == 5 ? 5 : PH == 7 ? 14 : 10, LA = PH == 5 ? 5 : PH == 4 ? LA4 : PH == 6 ? LA6 : LA7;
  constexpr int AS = PH == 4 ? 16 * HPL : PH == 5 ? 1024 : KW * 1024, BS = (PH == 5 ? 1 : KW) * NB * 1024, PER = PH == 7 ? 3 : 4;
  constexpr int EXO = PH == 4 ? 100 * 1024 : PH == 5 ? 92 * 1024 : 0, EXW = PH == 4 ? 6 : PH == 6 ? 7 : 8;
  const int kw = KW == 4 ? x.w >> 1 : x.w >> 2;
  v4u a[NIT][2];
  auto lda = [&](int it, int i) { a[it][i] = ld16(x.rs, i ? ao1 : ao0, it * AS); };
  auto fix = [&](int it) {
    if (PH == 4) {
      typedef u64 v2l __attribute__((ext_vector_type(2)));
      v2l p0 = __builtin_bit_cast(v2l, a[it][0]), p1 = __builtin_bit_cast(v2l, a[it][1]);
      u64 tmp;
      asm volatile("v_mov_b64 %2, %0\n\tv_mov_b64 %0, %1\n\tv_mov_b64 %1, %2\n\ts_nop 1" : "+v"(p0.y), "+v"(p1.x), "=&v"(tmp));
      a[it][0] = __builtin_bit_cast(v4u, p0); a[it][1] = __builtin_bit_cast(v4u, p1);
    }
  };
#pragma unroll
  for (int it = 0; it < LA; ++it) { lda(it, 0); lda(it, 1); }
  SCHED();
  wait_landed(x, PH - 3);
  STAMP(22 + PH);
  f32x4 acc[2][NB], gr[2];
#pragma unroll
  for (int i = 0; i < 2; ++i) {
    gr[i] = f32x4{0.f, 0.f, 0.f, 0.f};
#pragma unroll
    for (int n = 0; n < NB; ++n) acc[i][n] = gr[i];
  }
  LAS char* bs = x.lds + kw * (PH == 5 ? 5 : 1) * NB * 1024 + x.lane * 16;
  LAS char* bq[3] = {bs + (kw >> 1) * 2048, bs + ((kw >> 1) ^ 1) * 2048, bs + 4096};
  auto frag = [&](int n, int off) { return *(LAS v4u*)((KW == 2 ? bs + n * 1024 : bq[n >> 1] + (n & 1) * 1024) + off); };
  v4u f[NB];
#pragma unroll
  for (int n = 0; n < NB; ++n) f[n] = frag(n, 0);
  pre();
  fix(0);
  LAS char* ring = x.lds + L_CTL + C_RL;
  u32 seen = 0;
#pragma unroll
  for (int it = 0; it < NIT - 1; ++it) {
    SCHED();
    if (PH == 7 && it >= 6) lds_wait_ge_pre(ring + 4 * (it - 6), 4, seen);
    const int st = PH == 7 ? (it + 1) % 7 : it + 1;
    if (PH != 5 && st % PER == 0) {
      const int d = st ? PER * BS : -2 * PER * BS;
      if (KW == 2) bs = pinp(bs + d);
      else {
#pragma unroll
        for (int n = 0; n < 3; ++n) bq[n] = pinp(bq[n] + d);
      }
    }
#pragma unroll
    for (int n = 0; n < NB; ++n) {
      acc[0][n] = mfma(f[n], a[it][0], acc[0][n]);
      acc[1][n] = mfma(f[n], a[it][1], acc[1][n]);
      SCHED();
      f[n] = frag(n, (PH == 5 ? st : st % PER) * BS);
      if (n < 2 && it + LA < NIT) lda(it + LA, n);
      if (PH == 7 && n == 1 && it >= 5 && it < 12) seen = *(volatile LAS u32*)(ring + 4 * (it - 5));
      if (PH >= 6 && n == 1 && it == NIT - 2) seen = *(volatile LAS u32*)(x.lds + L_CTL + C_MID + (PH - 6) * 4);
      if (n == NB - 1) {
        if (PH == 4) { gr[0] = mfma(a[it][0], a[it][0], gr[0]); gr[1] = mfma(a[it][1], a[it][1], gr[1]); SCHED(); }
        fix(it + 1);
      }
      SCHED();
    }
    if (PH == 7 && it < 7) lds_inc(x, C_RING + 4 * it);
    if ((PH == 6 && it == 3) || (PH == 7 && it == 10)) lds_inc(x, C_MID + (PH - 6) * 4);
  }
  if (PH != 4) lds_wait_ge_pre(x.lds + L_CTL + C_MID + (PH - 6) * 4, 8, seen);
  LAS char* mine = pinp(x.lds + EXO + x.w * EXW * 1024 + x.lane * 16);
  const bool live = PH != 7 || kw < 2;
  if (PH == 4) { gr[1] = mfma(a[NIT - 1][1], a[NIT - 1][1], gr[1]); gr[0] = mfma(a[NIT - 1][0], a[NIT - 1][0], gr[0]); }
  if (live) {
#pragma unroll
    for (int n = 0; n < NB; ++n) acc[1][n] = mfma(f[n], a[NIT - 1][1], acc[1][n]);
#pragma unroll
    for (int n = NB - 1; n >= 0; --n) acc[0][n] = mfma(f[n], a[NIT - 1][0], acc[0][n]);
  }
  STAMP(PH == 4 ? 12 : PH == 5 ? 16 : PH == 6 ? 20 : 24);
#pragma unroll
  for (int n = 0; n < NB; ++n) *(LAS f32x4*)(mine + n * 1024) = acc[1][n];
  float s0 = 0.f;
  if (PH == 4) {
    const int r = x.c & 3;
    const bool dg = (x.c >> 2) == x.g;
    *(LAS float*)(mine + NB * 1024) = dg ? (r == 0 ? gr[1][0] : r == 1 ? gr[1][1] : r == 2 ? gr[1][2] : gr[1][3]) : 0.f;
    s0 = dg ? (r == 0 ? gr[0][0] : r == 1 ? gr[0][1] : r == 2 ? gr[0][2] : gr[0][3]) : 0.f;
  }
  if (KW == 4) {
#pragma unroll
    for (int n = 4; n >= 2; --n) *(LAS f32x4*)(mine + (3 + n) * 1024) = acc[0][n];
  }
  asm volatile("s_waitcnt lgkmcnt(0)" ::: "memory");
  lds_st(x.lds + L_CTL + C_EX + x.w * 4, PH);
  if (PH != 7) lds_inc(x, C_FREE + 4 * (PH - 4));
  lds_wait_ge<0>(x.lds + L_CTL + C_EX + (KW == 2 ? x.w ^ 4 : (x.w & 1) + 2 * (x.lane & 3)) * 4, PH);
  STAMP(28 + PH);
  if (KW == 2) {
    LAS char* other = pinp(x.lds + EXO + (x.w ^ 4) * EXW * 1024 + x.lane * 16);
    if (PH == 4) ssq = xsum16_32(s0 + *(LAS float*)(other + NB * 1024));
#pragma unroll
    for (int n = 0; n < NB; ++n) o[n] = acc[0][n] + *(LAS f32x4*)(other + n * 1024);
  } else {
    o[0] = acc[0][0]; o[1] = acc[0][1]; o[4] = acc[0][4];
#pragma unroll
    for (int m = 1; m < 4; ++m) {
      LAS char* s = pinp(x.lds + EXO + (x.w ^ (2 * m)) * EXW * 1024 + x.lane * 16);
      const int b0 = m == 1 ? 0 : m == 3 ? 2 : 5;
      o[0] += *(LAS f32x4*)(s + b0 * 1024);
      o[1] += *(LAS f32x4*)(s + (b0 + 1) * 1024);
      o[4] += *(LAS f32x4*)(s + (m == 2 ? 7 : 4) * 1024);
    }
  }
  if (PH == 5 || PH == 6) { asm volatile("s_waitcnt lgkmcnt(0)" ::: "memory"); lds_inc(x, C_EXD + (PH - 5) * 4); }
}

DI void gemm5(const Ctx& x, int a0, int a1, f32x4 (&acc)[5]) {
  constexpr int LA = LA5;
  const int kw = x.w >> 2;
  v4u a[10];
  auto lda = [&](int it) { a[it] = ld16(x.rs, it >= 5 ? a1 : a0, it * 1024); };
#pragma unroll
  for (int it = 0; it < LA; ++it) lda(it);
  SCHED();
  wait_landed(x, 2);
  STAMP(27);
#pragma unroll
  for (int n = 0; n < 5; ++n) acc[n] = f32x4{0.f, 0.f, 0.f, 0.f};
  LAS char* bs = x.lds + kw * 51200 + x.lane * 16;
  LAS char* bq[3] = {bs + kw * 2048, bs + (kw ^ 1) * 2048, bs + 4096};
  v4u f[2][5];
#pragma unroll
  for (int d = 0; d < 2; ++d)
#pragma unroll
    for (int n = 0; n < 5; ++n) f[d][n] = *(LAS v4u*)(bq[n >> 1] + (n & 1) * 1024 + d * 5120);
#pragma unroll
  for (int it = 0; it < 10; ++it) {
#pragma unroll
    for (int m = 0; m < 5; ++m) {
      const int n = it == 9 ? 4 - m : m;
      SCHED();
      acc[n] = mfma(f[it & 1][n], a[it], acc[n]);
      SCHED();
      if (it + 2 < 10) f[it & 1][n] = *(LAS v4u*)(bq[n >> 1] + (n & 1) * 1024 + (it + 2) * 5120);
      if (m == 0 && it + LA < 10) lda(it + LA);
    }
  }
  STAMP(16);
  LAS char* ex = pinp(x.lds + 136 * 1024 + (x.w & 3) * 5120 + x.lane * 16);
  LAS char* mine = ex + kw * 2048;
  if (kw) *(LAS f32x4*)(ex + 4096) = acc[4];
  *(LAS f32x4*)(mine + 1024) = acc[3];
  *(LAS f32x4*)mine = acc[2];
  asm volatile("s_waitcnt lgkmcnt(0)" ::: "memory");
  lds_st(x.lds + L_CTL + C_EX + x.w * 4, 5);
  lds_inc(x, C_FREE + 4);
  lds_wait_ge<0>(x.lds + L_CTL + C_EX + (x.w ^ 4) * 4, 5);
  STAMP(33);
  LAS char* other = ex + (kw ^ 1) * 2048;
  acc[0] += *(LAS f32x4*)other;
  acc[1] += *(LAS f32x4*)(other + 1024);
  acc[4] += *(LAS f32x4*)(ex + 4096);
  asm volatile("s_waitcnt lgkmcnt(0)" ::: "memory");
  lds_inc(x, C_EXD);
}

DI f32x4 gelu4(f32x4 v) { return f32x4{gelu(v[0]), gelu(v[1]), gelu(v[2]), gelu(v[3])}; }
DI v2u pack4(f32x4 a) { return v2u{pk(a[0], a[1]), pk(a[2], a[3])}; }
DI f32x4 unlo(v4u v) { return f32x4{blo(v.x), bhi(v.x), blo(v.y), bhi(v.y)}; }
DI f32x4 unhi(v4u v) { return f32x4{blo(v.z), bhi(v.z), blo(v.w), bhi(v.w)}; }
DI f32x4 un4(v2u v) { return f32x4{blo(v.x), bhi(v.x), blo(v.y), bhi(v.y)}; }

DI void phase_c(const Ctx& x, int P, int t) {
  const int mw = x.w & 3, kw = x.w >> 2, g = x.g, c = x.c;
  const int np = 2 * P + (mw >> 1), b = mw & 1;
  const int rowblk = 4 * np + b + 2 * kw;
  const float mu = __builtin_bit_cast(float, ld4(x.rs, O_MU + stat_at(rowblk >> 1, 16 * (rowblk & 1) + c) * 4, 0));
  f32x4 acc[1][5];
  float ssq;
  const int ao = O_HB + (8 * kw + 2 * g) * HPL + ((np * 2 + b) * 16 + c) * 16;
  gemm<4, 5>(x, ao, ao + HPL, acc[0], ssq, [] {});
  const float vh = ssq * (1.f / 640.f);
  float sc = __builtin_amdgcn_rsqf(vh + EPS);
  if (g == 0) *(LAS f32x2*)(x.lds + L_MISC + 1024 + ((rowblk & 7) * 16 + c) * 8) = f32x2{(vh + EPS) * sc, mu};
  const int t8 = t & 7, g0 = (t8 >> 1) * 5 + (t8 & 1) * 3, gs = (t8 >> 1) * 5 + 2;
  if (t < 8) {
    sc *= __builtin_amdgcn_rsqf(vh * sc * sc + EPS);
#pragma unroll
    for (int n = 0; n < 5; ++n) acc[0][n] = gelu4(acc[0][n] * sc);
  } else {
#pragma unroll
    for (int n = 0; n < 5; ++n) acc[0][n] *= sc;
  }
  const int dst = t < 8 ? O_G : O_XB;
#pragma unroll
  for (int i = 0; i < 2; ++i) st16(x.rs, pack8(acc[0][2 * i], acc[0][2 * i + 1]), dst + ft_pair(rowblk, 20, g0 + i, g, c), 0);
  st8(x.rs, pack4(acc[0][4]), dst + ft_single(rowblk, 20, gs, t8 & 1, g, c), 0);
}

DI void phase_5(const Ctx& x, int P, int hf, int t8, u32 (&res)[6]) {
  const int kw = x.w >> 2, g = x.g, c = x.c;
  const int rowblk = 8 * P + 4 * hf + 2 * (x.w & 1) + ((x.w >> 1) & 1);
  const int gp = (t8 >> 1) * 5 + (t8 & 1) * 3 + kw, gs = (t8 >> 1) * 5 + 2;
  const int op = ft_pair(rowblk, 20, gp, g, c), os = ft_single(rowblk, 20, gs, t8 & 1, g, c);
  int ao[2];
#pragma unroll
  for (int i = 0; i < 2; ++i) {
    const int r = rowblk * 16 + c - (i ? -1 - kw : 1 + kw);
    ao[i] = (u32)r < 2048u ? O_G + ft_pair(r >> 4, 20, 10 * kw, g, r & 15) : 0x7f000000;
  }
  const v4u xp = ld16(x.rs, O_XB + op, 0);
  const v2u xs = ld8(x.rs, O_XB + os, 0);
  f32x4 o[5];
  gemm5(x, ao[0], ao[1], o);
  const f32x2 st = *(LAS f32x2*)(x.lds + L_MISC + 1024 + ((rowblk & 7) * 16 + c) * 8);
  const f32x4 b0 = unlo(xp), b1 = unhi(xp), b2 = un4(xs);
  o[0] += b0; o[1] += b1; o[4] += b2;
  const v4u r = pack8(o[0], o[1]);
  st16(x.rs, r, O_X1 + op, 0);
  const v4u ones = {0x3f803f80u, 0x3f803f80u, 0x3f803f80u, 0x3f803f80u};
  float s1 = dot8(r, ones, 0.f), s2 = dot8(r, r, 0.f);
  if (kw == 0) {
    const v2u q = pack4(o[4]);
    st8(x.rs, q, O_X1 + os, 0);
    s1 = dot2(q.y, ones.x, dot2(q.x, ones.x, s1));
    s2 = dot2(q.y, q.y, dot2(q.x, q.x, s2));
  }
  s1 = xsum16_32(s1); s2 = xsum16_32(s2);
  if (g == 0) st8(x.rs, v2u{__builtin_bit_cast(u32, s1), __builtin_bit_cast(u32, s2)}, O_ST1 + (rowblk * 16 + t8 * 2 + kw) * 128 + c * 8, 0);
  const v4u rp = pack8(o[0] + b0 * st[0] + st[1], o[1] + b1 * st[0] + st[1]);
  const v2u rq = pack4(o[4] + b2 * st[0] + st[1]);
  res[0] = rp.x; res[1] = rp.y; res[2] = rp.z; res[3] = rp.w; res[4] = rq.x; res[5] = rq.y;
#pragma unroll
  for (int i = 0; i < 6; ++i) PIN(res[i]);
}

DI void phase_6(const Ctx& x, int P, int t) {
  const int g = x.g, c = x.c, kw = x.w >> 2, rowblk = 8 * P + 2 * (x.w & 3) + kw;
  f32x2 p[4];
#pragma unroll
  for (int i = 0; i < 4; ++i) p[i] = __builtin_bit_cast(f32x2, ld8(x.rs, O_ST1 + rowblk * 2048 + x.lane * 8 + i * 512, 0));
  f32x4 acc[7];
  float dummy, r1, nm;
  gemm<6, 7>(x, O_X1 + (rowblk * 20 + kw) * 1024 + x.lane * 16, O_X1 + ((rowblk ^ 1) * 20 + kw) * 1024 + x.lane * 16, acc, dummy, [&] {
    const f32x2 ps = (p[0] + p[1]) + (p[2] + p[3]);
    const float m1 = xsum16_32(ps[0]) * (1.f / 640.f), q1 = xsum16_32(ps[1]) * (1.f / 640.f);
    r1 = __builtin_amdgcn_rsqf(fmaxf(q1 - m1 * m1, 0.f) + EPS);
    nm = -m1 * r1;
    PIN(r1); PIN(nm);
  });
  const int g0 = (t >> 1) * 7 + (t & 1) * 4, gs = (t >> 1) * 7 + 3;
  LAS f32x4* cs = (LAS f32x4*)pinp(x.lds + L_MISC + g * 16);
#pragma unroll
  for (int i = 0; i < 3; ++i) {
    const v4u r = pack8(gelu4(acc[2 * i] * r1 + cs[8 * i] * nm), gelu4(acc[2 * i + 1] * r1 + cs[8 * i + 4] * nm));
    if (g0 + i < 54) st16(x.rs, r, O_U + ft_pair(rowblk, 54, g0 + i, g, c), 0);
  }
  st8(x.rs, pack4(gelu4(acc[6] * r1 + cs[24] * nm)), O_U + ft_single(rowblk, 54, gs, t & 1, g, c), 0);
}
DI void colsum_6(const Ctx& x, int t) {
  if (x.lane < 56) {
    f32x2 s = {0.f, 0.f};
#pragma unroll
    for (int k = 0; k < 20; ++k) s += __builtin_bit_cast(f32x2, ld8(x.rs, O_CS3 + t * 8960 + x.lane * 8 + k * 448, 0));
    *(LAS f32x2*)(x.lds + L_MISC + x.lane * 8) = s;
  }
}

DI void phase_7(const Ctx& x, int P, int hf, int t8, const u32 (&res)[6], u16* out) {
  const int kw = x.w >> 1, g = x.g, c = x.c;
  const int rowblk = 8 * P + 4 * hf + 2 * (x.w & 1) + (kw & 1);
  const int gp = (t8 >> 1) * 5 + (t8 & 1) * 3 + (kw >> 1), gs = (t8 >> 1) * 5 + 2;
  f32x4 o[5];
  float dummy;
  gemm<7, 5>(x, O_U + (rowblk * 54 + kw) * 1024 + x.lane * 16, O_U + ((rowblk ^ 1) * 54 + kw) * 1024 + x.lane * 16, o, dummy, [] {});
  char* orow = (char*)out + (rowblk * 16 + c) * 1280;
  *(v4u*)(orow + gp * 64 + g * 16) = pack8(o[0] + un4(v2u{res[0], res[1]}), o[1] + un4(v2u{res[2], res[3]}));
  if (kw < 2) *(v2u*)(orow + gs * 64 + (t8 & 1) * 32 + g * 8) = pack4(o[4] + un4(v2u{res[4], res[5]}));
}

template <int R0, int R1> DI void stage_load(const Ctx& x, int voff, u64 skip, v4u (&r)[35]) {
#pragma unroll
  for (int k = R0; k < R1; ++k) {
    r[k] = ld16(x.rs, voff, ((skip >> k) & 1) ? 0x7f000000 : k * 4096);
    asm volatile("s_waitcnt vmcnt(" STR(PACE) ")" ::: "memory");
  }
}
template <int R0, int R1> DI void stage_drop(LAS char* p, const v4u (&r)[35]) {
#pragma unroll
  for (int k = R0; k < R1; ++k) {
    if (k % 16 == 0 || k == R0) p = pinp(p + (k == R0 ? (R0 / 16) * 65536 : 65536));
    *(LAS v4u*)(p + (k % 16) * 4096) = r[k];
  }
}
DI void dropped(const Ctx& x) { asm volatile("s_waitcnt lgkmcnt(0)" ::: "memory"); lds_inc(x, C_LAND); }
template <int S> DI void ring_drop(const Ctx& x, LAS char* p, const v4u (&r)[35]) {
  lds_wait_ge(x.lds + L_CTL + C_RING + 4 * S, 8);
  stage_drop<5 * S, S < 6 ? 5 * S + 5 : 33>(p, r);
  asm volatile("s_waitcnt lgkmcnt(0)" ::: "memory");
  lds_inc(x, C_RL + 4 * S);
}
DI void weight_stream(const Ctx& x, int t, int t8) {
  const int lo = (x.w & 3) * 1024 + x.lane * 16;
  LAS char* p = x.lds + lo;
  v4u r[35];
#pragma unroll 1
  for (int s = 0; s < 4; ++s) {
    const int voff = lo + (s == 0 ? O_B5 + t8 * 102400 : s == 1 ? O_B6 + t * 143360 : O_B7 + t8 * 276480 + (s - 2) * 143360);
    const int lw = x.w & 3;
    const u64 skip = (s == 1 && t == 15) ? 0x810204081ull * (lw == 0 ? 0x5a : lw == 1 ? 0x56 : lw == 2 ? 0x55 : 0x35)
                                         : s == 3 ? (lw < 2 ? 6ull : 7ull) << 32 : 0;
    stage_load<0, 25>(x, voff, skip, r);
    if (s) stage_load<25, 35>(x, voff, skip, r);
    STAMPL(41 + s);
    if (s == 3) break;
    lds_wait_ge(x.lds + L_CTL + C_FREE + 4 * s, 8);
    stage_drop<14, 25>(p, r);
    if (s) stage_drop<25, 34>(p, r);
    if (s) lds_wait_ge(x.lds + L_CTL + C_EXD + 4 * (s - 1), 8);
    stage_drop<0, 14>(p, r);
    if (s) stage_drop<34, 35>(p, r);
    dropped(x);
    STAMPL(51 + s);
  }
  ring_drop<0>(x, p, r); ring_drop<1>(x, p, r); ring_drop<2>(x, p, r); ring_drop<3>(x, p, r);
  ring_drop<4>(x, p, r); ring_drop<5>(x, p, r); ring_drop<6>(x, p, r);
}

DI void wait_b(const Ctx& x, bool poller, int xcd) {
  wait_flags<3, DLYB>(x, poller, {F_B + xcd * 160 + x.lane, F_B + xcd * 160 + 64 + x.lane, F_B + xcd * 160 + 128 + (x.lane & 31)}, 3);
}
#define CODE_KB 48
#define MAP 1
__global__ __launch_bounds__(NTHR, 3) void fused(const u16* __restrict__ xin, const u16* __restrict__ wr, const u16* __restrict__ wi,
                                                 const u16* __restrict__ w1, const u16* __restrict__ w2, const u16* __restrict__ w3,
                                                 const u16* __restrict__ w4, u16* __restrict__ out, char* ws) {
  extern __shared__ __attribute__((aligned(16))) char smem[];
  if (blockIdx.x >= 240 && threadIdx.x >= 512) {
    u64 pc;
    asm volatile("s_getpc_b64 %0" : "=s"(pc));
    const char* code = (const char*)(pc & ~255ull) + (threadIdx.x - 512) * 16;
#pragma unroll 1
    for (int k = 0; k < CODE_KB / 4; ++k) dma16_sc1(code + k * 4096, (u32)(uintptr_t)(LAS char*)smem + L_MISC + 2048);
  }
  Ctx x;
  x.ws = ws; x.rs = __builtin_amdgcn_make_buffer_rsrc(ws, 0, WS_END, 0x00020000); x.lds = (LAS char*)smem;
  x.tid = threadIdx.x; x.lane = x.tid & 63; x.w = __builtin_amdgcn_readfirstlane(x.tid >> 6); x.g = x.lane >> 4; x.c = x.lane & 15;
  GAS u64* ep = (GAS u64*)(ws + O_EPOCH);
  x.epoch = __hip_atomic_load(ep, __ATOMIC_RELAXED, __HIP_MEMORY_SCOPE_AGENT);
  const int b = blockIdx.x, xcd = b & 7, i = b >> 3;
  const int P = 4 * (xcd >> 1) + (i >> 3), t = 8 * (xcd & 1) + (i & 7), hf = (i >> 2) & 1, t8 = 4 * (xcd & 1) + (i & 3);
  STAMP(0);
  if (x.w == 8) lds_st(x.lds + L_CTL + x.lane * 4, 0);
  if (b >= 160) side_jobs(x, xcd, i - 20, wr, wi, w1, w2, w3, w4);
  if (x.w >= 8) {
    if (b < 160) {
      VMWAIT(0);
      phase_a_stats(x);
      if (x.w == 9) {
        lds_wait_ge(x.lds + L_CTL + C_PUB, 8);
        wait_flags<1, DLYA>(x, true, {F_A + xcd * 160 + (b >> 5) * 32 + (x.lane & 31)}, 2);
      }
      if (x.w == 10) {
        lds_wait_ge(x.lds + L_CTL + C_PUB, 8);
        poll<1, 4>(x, {F_W + min(x.lane, 45)});
        lds_st(x.lds + L_CTL + C_GOW, 1);
      }
      if (x.w == 11 && b < 96) {
        lds_wait_ge(x.lds + L_CTL + XWORD, XVAL);
        if (x.lane < 8) __hip_atomic_store((GAS u64*)(x.ws + flag_at(F_X + b) + x.lane * 8), x.tag(), __ATOMIC_RELAXED, __HIP_MEMORY_SCOPE_AGENT);
      }
      if (x.w == 8) lds_wait_ge(x.lds + L_CTL + C_GO, 2);
    }
    if (x.w == 8) {
      poll<1, 8>(x, {F_S + xcd * 50 + min(x.lane, 49)});
      if (b < 160) lds_wait_ge(x.lds + L_CTL + C_SB0, 8);
      lds_st(x.lds + L_CTL + C_LGO, 1);
      STAMPW(9);
    } else lds_wait_ge(x.lds + L_CTL + C_LGO, 1);
    dma_lin(x, O_BC + t * 102400, 0, 0, 100);
    LANDED(0);
    STAMPL(40);
    if (x.w == 8) {
      poll<2, 16>(x, {F_T + xcd * 96 + x.lane, F_T + xcd * 96 + 64 + (x.lane & 31)});
      lds_st(x.lds + L_CTL + C_LGO, 2);
      colsum_6(x, t);
    } else lds_wait_ge(x.lds + L_CTL + C_LGO, 2);
    STAMPL(45);
    weight_stream(x, t, t8);
    return;
  }
  bool last;
  if (b < 160) {
    phase_a(x, xin, xcd, i & 3, i >> 2);
    STAMP(2);
    publish<8, 160>(x, 0, F_A + (i >> 2) * 32 + xcd * 4 + (i & 3), 3);
    refresh(x);
    phase_b(x, b);
    STAMP(7);
    last = publish<8, 160>(x, 1, F_B + b, 8);
  } else {
    last = x.w == 0;
    if (last) poll<1, 8>(x, {F_B + xcd * 160 + i});
  }
  wait_b(x, last, xcd);
  STAMP(11);
  refresh(x);
  phase_c(x, P, t);
  STAMP(13);
  last = publish(x, 2, F_C + P * 16 + t, 14);
  wait_flags<1, DLY>(x, last, {F_C + (x.lane < 32 ? P * 16 + x.c : ((P + (hf ? 1 : 15)) & 15) * 16 + (x.lane & 7))}, 4);
  STAMP(15);
  refresh(x);
  u32 res[6];
  phase_5(x, P, hf, t8, res);
  STAMP(17);
  last = publish(x, 3, F_5 + P * 16 + t, 18);
  wait_flags<1, DLY>(x, last, {F_5 + P * 16 + x.c}, 5);
  STAMP(19);
  refresh(x);
  phase_6(x, P, t);
  STAMP(21);
  last = publish(x, 4, F_6 + P * 16 + t, 22);
  wait_flags<1, DLY>(x, last, {F_6 + P * 16 + x.c}, 6);
  STAMP(23);
  refresh(x);
  phase_7(x, P, hf, t8, res, out);
  STAMP(25);
  if (b == 0 && x.tid == 0) __hip_atomic_store(ep, x.tag(), __ATOMIC_RELAXED, __HIP_MEMORY_SCOPE_AGENT);
}

#define WSOFF 0
extern "C" void kernel_launch(void* const* d_in, const int* in_sizes, int n_in, void* d_out, int out_size, void* d_ws,
                              size_t ws_size, hipStream_t stream) {
  (void)hipFuncSetAttribute((const void*)fused, hipFuncAttributeMaxDynamicSharedMemorySize, LDS_BYTES);
  fused<<<256, NTHR, LDS_BYTES, stream>>>((const u16*)d_in[0], (const u16*)d_in[1], (const u16*)d_in[2], (const u16*)d_in[4],
                                          (const u16*)d_in[5], (const u16*)d_in[6], (const u16*)d_in[7], (u16*)d_out, (char*)d_ws + WSOFF);
}
```

```cpp
#include <hip/hip_runtime.h>
#include <stdint.h>

typedef unsigned u32;
typedef unsigned long long u64;
typedef unsigned short u16;
typedef u32 v4u __attribute__((ext_vector_type(4)));
typedef u32 v2u __attribute__((ext_vector_type(2)));
typedef float f32x4 __attribute__((ext_vector_type(4)));
typedef float f32x2 __attribute__((ext_vector_type(2)));
typedef __bf16 bf16x8 __attribute__((ext_vector_type(8)));
typedef __bf16 bf16x2 __attribute__((ext_vector_type(2)));
typedef short s16x4 __attribute__((ext_vector_type(4)));
typedef __amdgpu_buffer_rsrc_t rsrc_t;
#define LAS __attribute__((address_space(3)))
#define GAS __attribute__((address_space(1)))
#define DI __device__ __forceinline__

constexpr int ACT = 2048 * 640 * 2;
constexpr int O_EPOCH = 0;
constexpr int O_FLAG = 4096;
enum { F_C = 240, F_5 = 496, F_6 = 752, F_X = 1008, F_S = 1104, F_T = F_S + 8 * 50, F_B = F_T + 8 * 96, F_A = F_B + 8 * 160, F_W = F_A + 8 * 160, F_END = F_W + 8 * 46 };
__host__ __device__ constexpr int flag_at(int i) { return O_FLAG + i * 256; }
constexpr int O_SIG = 2 << 20;
static_assert(flag_at(F_END) <= O_SIG, "flag area");
constexpr int O_MU = O_SIG + 5 * 8192;
constexpr int O_CS3 = O_MU + 8192;
constexpr int O_ST1 = O_CS3 + 20 * 1792 * 4;
constexpr int O_ZT = O_ST1 + 2048 * 128;
constexpr int O_WT = O_ZT + ACT;
#define HPL 16384
constexpr int O_HB = O_WT + ACT;
constexpr int O_BC = O_HB + ACT + 131072;
constexpr int O_B5 = O_BC + 16 * 102400 + 8192;
constexpr int O_B6 = O_B5 + 8 * 102400 + 8192;
constexpr int O_B7 = O_B6 + 16 * 143360 + 8192;
constexpr int O_G = O_B7 + 8 * 276480 + 8192;
constexpr int O_XB = O_G + ACT;
constexpr int O_X1 = O_XB + ACT;
constexpr int O_U = O_X1 + ACT;
constexpr int WS_END = O_U + 2048 * 1728 * 2;

constexpr int LDS_BYTES = 160 * 1024;
constexpr int L_MISC = 156 * 1024;
constexpr int L_CTL = 159 * 1024;
constexpr int L_TJ = 84 * 1024;
constexpr int L_XS = 116 * 1024;
constexpr int L_XB = 132 * 1024;
enum { C_SB = 4, C_GO = 8, C_LAND = 12, C_LGO = 16, C_GOW = 20, C_SB0 = 24, C_SB1 = 28, C_PUB = 32, C_FREE = 64, C_MID = 84, C_EX = 96, C_EXD = 128, C_RING = 136, C_RL = 164, C_XL = 192 };
constexpr int NTHR = 768;
constexpr float EPS = 1e-5f;
constexpr int SC1 = 16;

DI u32 pk(float a, float b) { f32x2 v = {a, b}; return __builtin_bit_cast(u32, __builtin_convertvector(v, bf16x2)); }
DI float blo(u32 v) { return __builtin_bit_cast(float, v << 16); }
DI float bhi(u32 v) { return __builtin_bit_cast(float, v & 0xffff0000u); }
DI f32x4 mfma(v4u a, v4u b, f32x4 c) {
  return __builtin_amdgcn_mfma_f32_16x16x32_bf16(__builtin_bit_cast(bf16x8, a), __builtin_bit_cast(bf16x8, b), c, 0, 0, 0);
}
DI float dot2(u32 a, u32 b, float c) {
  return __builtin_amdgcn_fdot2_f32_bf16(__builtin_bit_cast(bf16x2, a), __builtin_bit_cast(bf16x2, b), c, false);
}
DI float dot8(v4u a, v4u b, float c) { return dot2(a.w, b.w, dot2(a.z, b.z, dot2(a.y, b.y, dot2(a.x, b.x, c)))); }
DI v2u trread(LAS char* p) { return __builtin_bit_cast(v2u, __builtin_amdgcn_ds_read_tr16_b64_v4i16((LAS s16x4*)p)); }
DI float cosr(float rev) { return __builtin_amdgcn_cosf(rev); }
DI float sinr(float rev) { return __builtin_amdgcn_sinf(rev); }
typedef float f32x3 __attribute__((ext_vector_type(3)));
DI f32x3 gelu_k(float r, float inv_r) { return f32x3{-2.302208198f * r, -0.1029432397f * r * r * r, inv_r}; }
DI u32 gelu(f32x3 k, f32x2 s) {
  const f32x2 y = s * (s * s * k[1] + k[0]);
  const f32x2 d = f32x2{__builtin_amdgcn_exp2f(y[0]), __builtin_amdgcn_exp2f(y[1])} * k[2] + k[2];
  const f32x2 o = s * f32x2{__builtin_amdgcn_rcpf(d[0]), __builtin_amdgcn_rcpf(d[1])};
  return pk(o[0], o[1]);
}
DI v2u gelu(f32x3 k, f32x4 a) { return v2u{gelu(k, a.xy), gelu(k, a.zw)}; }
DI v4u gelu(f32x3 k, f32x4 a, f32x4 b) { return v4u{gelu(k, a.xy), gelu(k, a.zw), gelu(k, b.xy), gelu(k, b.zw)}; }
DI void dma16(const void* gsrc, u32 lds_dst) {
  u32 keep;
  asm volatile("s_mov_b32 %0, m0\n\ts_mov_b32 m0, %2\n\ts_nop 0\n\tglobal_load_lds_dwordx4 %1, off nt\n\ts_mov_b32 m0, %0"
               : "=&s"(keep) : "v"(gsrc), "s"(lds_dst) : "memory");
}
DI void dma16_sc1(const void* gsrc, u32 lds_dst) {
  u32 keep;
  asm volatile("s_mov_b32 %0, m0\n\ts_mov_b32 m0, %2\n\ts_nop 0\n\tglobal_load_lds_dwordx4 %1, off sc1\n\ts_mov_b32 m0, %0"
               : "=&s"(keep) : "v"(gsrc), "s"(lds_dst) : "memory");
}
#define VMWAIT(n) asm volatile("s_waitcnt vmcnt(" #n ")" ::: "memory")
#define BAR() asm volatile("s_waitcnt lgkmcnt(0)\n\ts_barrier" ::: "memory")
#define SCHED() __builtin_amdgcn_sched_barrier(0)
DI v4u ld16(rsrc_t r, int voff, int soff) { return __builtin_amdgcn_raw_buffer_load_b128(r, voff, soff, SC1); }
DI v2u ld8(rsrc_t r, int voff, int soff) { return __builtin_amdgcn_raw_buffer_load_b64(r, voff, soff, SC1); }
DI u32 ld4(rsrc_t r, int voff, int soff) { return __builtin_amdgcn_raw_buffer_load_b32(r, voff, soff, SC1); }
DI void st16(rsrc_t r, v4u v, int voff, int soff) { __builtin_amdgcn_raw_buffer_store_b128(v, r, voff + soff, 0, SC1); }
DI void st8(rsrc_t r, v2u v, int voff, int soff) { __builtin_amdgcn_raw_buffer_store_b64(v, r, voff, soff, SC1); }
DI void st4(rsrc_t r, u32 v, int voff, int soff) { __builtin_amdgcn_raw_buffer_store_b32(v, r, voff, soff, SC1); }
DI float xsum16_32(float v) {
  float u = v;
  asm volatile("s_nop 1\n\tv_permlane16_swap_b32 %0, %1" : "+v"(u), "+v"(v));
  v += u; u = v;
  asm volatile("s_nop 1\n\tv_permlane32_swap_b32 %0, %1" : "+v"(u), "+v"(v));
  return v + u;
}
template <int CTRL> DI float dppadd(float v) {
  return v + __builtin_bit_cast(float, __builtin_amdgcn_mov_dpp(__builtin_bit_cast(int, v), CTRL, 0xf, 0xf, true));
}
DI float xsum8(float v) { return dppadd<0x141>(dppadd<0x4E>(dppadd<0xB1>(v))); }
#define PIN(v) asm volatile("" : "+v"(v))

#define STAMP(i)
#define STAMP2(i)
#define STAMPA(i)
#define STAMPW(i)
#define STAMPL(i)
struct Ctx {
  char* ws; rsrc_t rs; LAS char* lds; u64 epoch;
  int tid, lane, w, g, c;
  DI u64 tag() const { u64 e = epoch; asm volatile("" : "+v"(e)); return e + 0x9E3779B97F4A7C15ull; }
};
DI void refresh(Ctx& x) {
  int l = x.lane;
  asm volatile("" : "+v"(l));
  x.lane = l; x.g = l >> 4; x.c = l & 15;
}
DI LAS char* pinp(LAS char* p) { asm volatile("" : "+v"(p)); return p; }
#define LSLP 1
#define GSLP 1
#define PSLP 1
template <int SLP = LSLP> DI void lds_wait_ge(LAS char* p, u32 want) {
  u32 v, n = 1u << 20;
  asm volatile("1:\n\tds_read_b32 %0, %2\n\ts_waitcnt lgkmcnt(0)\n\tv_cmp_gt_u32 vcc, %3, %0\n\ts_sub_u32 %1, %1, 1\n\ts_cbranch_scc1 2f\n\t"
               "s_cbranch_vccz 2f\n\ts_sleep %4\n\ts_branch 1b\n2:"
               : "=&v"(v), "+s"(n) : "v"(p), "s"(want), "n"(SLP) : "vcc", "scc", "memory");
}
DI void lds_wait_ge_pre(LAS char* p, u32 want, u32 v) {
  u32 n = 1u << 20;
  asm volatile("1:\n\tv_cmp_gt_u32 vcc, %3, %0\n\ts_sub_u32 %1, %1, 1\n\ts_cbranch_scc1 2f\n\ts_cbranch_vccz 2f\n\t"
               "ds_read_b32 %0, %2\n\ts_waitcnt lgkmcnt(0)\n\ts_branch 1b\n2:"
               : "+v"(v), "+s"(n) : "v"(p), "n"(want) : "vcc", "scc", "memory");
}
DI void lds_inc(const Ctx& x, int word) {
  u64 keep;
  asm volatile("s_mov_b64 %0, exec\n\ts_mov_b64 exec, 1\n\tds_add_u32 %1, %2\n\ts_mov_b64 exec, %0"
               : "=&s"(keep) : "v"(x.lds + L_CTL + word), "v"(1u) : "memory");
}
template <int REP = 1, int STR = 0> DI bool publish(const Ctx& x, int idx, int flag, int stamp, int NW = 8) {
  __builtin_amdgcn_s_setprio(3);
  __builtin_amdgcn_s_waitcnt(0x0070);
  asm volatile("s_waitcnt vmcnt(0) lgkmcnt(0)" ::: "memory");
  u32 old;
  u64 keep;
  asm volatile("s_mov_b64 %1, exec\n\ts_mov_b64 exec, 1\n\tds_add_rtn_u32 %0, %2, %3\n\ts_mov_b64 exec, %1\n\ts_waitcnt lgkmcnt(0)"
               : "=&v"(old), "=&s"(keep) : "v"(x.lds + L_CTL + C_PUB + 4 * idx), "v"(1u) : "memory");
  const bool last = __builtin_amdgcn_readfirstlane(old) == NW - 1;
  if (last) {
    if (x.lane < 8 * REP)
      __hip_atomic_store((GAS u64*)(x.ws + flag_at(flag + (x.lane >> 3) * STR) + (x.lane & 7) * 8), x.tag(), __ATOMIC_RELAXED, __HIP_MEMORY_SCOPE_AGENT);
    STAMPW(stamp);
  }
  asm volatile("" ::: "memory");
  __builtin_amdgcn_s_setprio(0);
  return last;
}
DI void soft_barrier(const Ctx& x, int word, u32 target) {
  asm volatile("s_waitcnt lgkmcnt(0)" ::: "memory");
  lds_inc(x, word);
  lds_wait_ge<0>(x.lds + L_CTL + word, target);
}
template <int N, int SLP = PSLP> DI void poll(const Ctx& x, const int (&off)[N]) {
  const u64 tag = x.tag();
#pragma unroll 1
  for (int spin = 0; spin < (1 << 16); ++spin) {
    u64 v[N];
#pragma unroll
    for (int i = 0; i < N; ++i) v[i] = __hip_atomic_load((GAS u64*)(x.ws + flag_at(off[i])), __ATOMIC_RELAXED, __HIP_MEMORY_SCOPE_AGENT);
    bool ok = true;
#pragma unroll
    for (int i = 0; i < N; ++i) ok &= v[i] == tag;
    if (__all(ok)) break;
    __builtin_amdgcn_s_sleep(SLP);
  }
  __builtin_amdgcn_fence(__ATOMIC_ACQUIRE, "wavefront");
  asm volatile("" ::: "memory");
}
DI void lds_st(LAS char* p, u32 v) { __hip_atomic_store((LAS u32*)p, v, __ATOMIC_RELAXED, __HIP_MEMORY_SCOPE_WORKGROUP); }
template <int N, int DELAY, int SLP = PSLP> DI void wait_flags(const Ctx& x, bool poller, const int (&off)[N], u32 gen) {
  if (poller) {
    if (DELAY) __builtin_amdgcn_s_sleep(DELAY);
    poll<N, SLP>(x, off);
    lds_st(x.lds + L_CTL + C_GO, gen);
  } else lds_wait_ge<GSLP>(x.lds + L_CTL + C_GO, gen);
}
#define LANDED(n) do { VMWAIT(n); lds_inc(x, C_LAND); } while (0)
DI void wait_landed(const Ctx& x, u32 n) { lds_wait_ge(x.lds + L_CTL + C_LAND, 4 * n); }

template <int GP> DI void group_dst(int gam, int h, int& tile, int& nb, bool& single) {
  int sup = gam / (2 * GP + 1), e = gam - sup * (2 * GP + 1);
  single = (e == GP);
  tile = 2 * sup + (e > GP ? 1 : (single ? h : 0));
  nb = single ? 2 * GP : 2 * (e > GP ? e - GP - 1 : e) + h;
}

DI v2u pack4(f32x4 a) { return v2u{pk(a[0], a[1]), pk(a[2], a[3])}; }
DI f32x4 unlo(v4u v) { return f32x4{blo(v.x), bhi(v.x), blo(v.y), bhi(v.y)}; }
DI f32x4 unhi(v4u v) { return f32x4{blo(v.z), bhi(v.z), blo(v.w), bhi(v.w)}; }
DI f32x4 un4(v2u v) { return f32x4{blo(v.x), bhi(v.x), blo(v.y), bhi(v.y)}; }
DI v4u pack8(const float (&v)[8]) { return v4u{pk(v[0], v[1]), pk(v[2], v[3]), pk(v[4], v[5]), pk(v[6], v[7])}; }
DI v4u pack8(f32x4 a, f32x4 b) { return v4u{pk(a[0], a[1]), pk(a[2], a[3]), pk(b[0], b[1]), pk(b[2], b[3])}; }
DI void gen_q(int kap, int g, int n0, v4u (&qc)[10], v4u (&qs)[10]) {
  const int k = kap ? kap : 320;
  const float amp = kap ? 0.05590169944f : 0.03952847075f;
  const float kf = (float)k * (1.f / 640.f), a0 = (float)(((n0 + 8 * g) * k) % 640) * (1.f / 640.f);
  const float rc = cosr(32.f * kf), rs = sinr(32.f * kf), c2 = rc + rc;
  f32x2 c0[4], s0[4], c1[4], s1[4];
#pragma unroll
  for (int j = 0; j < 4; ++j) {
    const float u = a0 + (float)(2 * j) * kf, v = u + kf;
    c0[j] = f32x2{cosr(u), cosr(v)} * amp;
    s0[j] = f32x2{sinr(u), sinr(v)} * (kap ? -amp : 0.f);
    c1[j] = c0[j] * rc + s0[j] * rs;
    s1[j] = s0[j] * rc - c0[j] * (kap ? rs : 0.f);
  }
  auto pk4 = [](const f32x2 (&v)[4]) { return v4u{pk(v[0][0], v[0][1]), pk(v[1][0], v[1][1]), pk(v[2][0], v[2][1]), pk(v[3][0], v[3][1])}; };
#pragma unroll
  for (int ks = 0; ks < 10; ks += 2) {
    qc[ks] = pk4(c0); qs[ks] = pk4(s0); qc[ks + 1] = pk4(c1); qs[ks + 1] = pk4(s1);
    if (ks + 2 < 10) {
#pragma unroll
      for (int j = 0; j < 4; ++j) {
        c0[j] = c2 * c1[j] - c0[j]; c1[j] = c2 * c0[j] - c1[j];
        s0[j] = c2 * s1[j] - s0[j]; s1[j] = c2 * s0[j] - s1[j];
      }
    }
  }
#pragma unroll
  for (int i = 0; i < 10; ++i) { PIN(qc[i]); PIN(qs[i]); }
}

#define XDMA dma16_sc1
template <int K0, int K1> DI void x_fetch(const Ctx& x, const u16* xin, int mm, int q4, int nt, int rg) {
  const int lam = 8 * rg + (x.lane >> 3);
  const int srow = 256 * mm + 32 * (4 * ((lam >> 4) & 1) + (lam & 3)) + 8 * q4 + 4 * (lam >> 5) + ((lam >> 2) & 3);
  const char* src = (const char*)xin + srow * 1280 + (((x.lane & 7) ^ (lam & 7)) << 4);
#pragma unroll
  for (int kc = K0; kc < K1; ++kc) {
    const int kr = kc + 2 * nt;
    XDMA(src + (kr < 10 ? kr : kr - 10) * 128, (u32)(uintptr_t)x.lds + (kc * 8 + rg) * 1024);
  }
}
DI void phase_a(const Ctx& x, const u16* xin, int mm, int q4, int nt) {
  const int w = x.w, g = x.g, c = x.c;
  x_fetch<0, 4>(x, xin, mm, q4, nt, w);
  BAR();
  const int u = w >> 2;
  v4u qc[10], qs[10];
  gen_q(64 * nt + 16 * (w & 3) + c, g, 128 * nt, qc, qs);
  f32x4 acc[2][2][2];
#pragma unroll
  for (int i = 0; i < 8; ++i) acc[i >> 2][(i >> 1) & 1][i & 1] = f32x4{0.f, 0.f, 0.f, 0.f};
  LAS char* a0 = x.lds + u * 4096 + (c >> 3) * 1024 + (c & 7) * 128 + ((g ^ (c & 7)) << 4);
  LAS char* a1 = x.lds + u * 4096 + (c >> 3) * 1024 + (c & 7) * 128 + (((g + 4) ^ (c & 7)) << 4);
  v4u f[4];
  f32x4 r0, r1;
  u32 seen = 0;
  SCHED();
  STAMP(36);
  VMWAIT(0); soft_barrier(x, C_XL, 8); STAMP(1);
#pragma unroll
  for (int i = 0; i < 4; ++i) f[i] = *(LAS v4u*)((i < 2 ? a0 : a1) + (i & 1) * 2048);
#pragma unroll
  for (int kc = 0; kc < 10; ++kc) {
    if (kc == 2 || kc == 5) seen = *(volatile LAS u32*)(x.lds + L_CTL + C_XL + (kc == 2 ? 8 : 12));
    if (kc == 3 || kc == 6) lds_wait_ge_pre(x.lds + L_CTL + C_XL + (kc == 3 ? 8 : 12), 4, seen);
    if (kc == 4) { a0 = pinp(a0 + 40960); a1 = pinp(a1 + 40960); }
    if (kc == 8) seen = *(volatile LAS u32*)(x.lds + L_CTL + C_XL + 4);
    if (kc == 9) {
      lds_wait_ge_pre(x.lds + L_CTL + C_XL + 4, 4, seen);
      LAS char* rp = pinp(x.lds + L_MISC + 1024 + (32 * u + 4 * g) * 4);
      r0 = *(LAS f32x4*)rp; r1 = *(LAS f32x4*)(rp + 64);
    }
    SCHED();
#pragma unroll
    for (int i = 0; i < 4; ++i) {
      const int qi = (2 * kc + (i >> 1)) % 10;
      acc[kc / 5][i & 1][0] = mfma(f[i], qc[qi], acc[kc / 5][i & 1][0]);
      acc[kc / 5][i & 1][1] = mfma(f[i], qs[qi], acc[kc / 5][i & 1][1]);
      SCHED();
      if (kc < 9) f[i] = *(LAS v4u*)((i < 2 ? a0 : a1) + ((kc + 1) % 5) * 8192 + (i & 1) * 2048);
      SCHED();
    }
  }
  __builtin_amdgcn_s_setprio(2);
  STAMP(37);
  STAMPA(L_CTL + 256 + (x.w == 7 ? 58 * 4 : 512));
  const float sg = (c & 1) ? -1.f : 1.f;
  const int off = O_ZT + ((nt * 32 + mm * 4 + q4) * 32 + 8 * (w & 3) + (c >> 1)) * 512 + u * 256 + (c & 1) * 128 + g * 16;
#pragma unroll
  for (int i = 0; i < 2; ++i) st16(x.rs, pack8((acc[0][0][i] + sg * acc[1][0][i]) * r0, (acc[0][1][i] + sg * acc[1][1][i]) * r1), off, i * 64);
}

DI void phase_a_stats(const Ctx& x, const u16* xin, int mm, int q4, int nt) {
  const int lw = x.w & 3, g = x.g, c = x.c, rt = mm * 4 + q4;
  x_fetch<4, 7>(x, xin, mm, q4, nt, 2 * lw);
  x_fetch<4, 7>(x, xin, mm, q4, nt, 2 * lw + 1);
  x_fetch<7, 10>(x, xin, mm, q4, nt, 2 * lw);
  x_fetch<7, 10>(x, xin, mm, q4, nt, 2 * lw + 1);
  const v4u ones = {0x3f803f80u, 0x3f803f80u, 0x3f803f80u, 0x3f803f80u};
  f32x4 gr = {0.f, 0.f, 0.f, 0.f}, sm = gr;
  LAS char* a0 = x.lds + lw * 2048 + (c >> 3) * 1024 + (c & 7) * 128 + ((g ^ (c & 7)) << 4);
  LAS char* a1 = x.lds + lw * 2048 + (c >> 3) * 1024 + (c & 7) * 128 + (((g + 4) ^ (c & 7)) << 4);
#pragma unroll
  for (int st = 0; st < 3; ++st) {
    constexpr int k0[3] = {4, 7, 0}, k1[3] = {7, 10, 4};
    if (st == 0) { VMWAIT(6); lds_inc(x, C_XL + 8); }
    if (st == 1) { VMWAIT(0); lds_inc(x, C_XL + 12); STAMPL(60); }
    if (st == 2) lds_wait_ge<0>(x.lds + L_CTL + C_XL, 8);
    LAS char* p0 = pinp(a0 + k0[st] * 8192);
    LAS char* p1 = pinp(a1 + k0[st] * 8192);
    v4u f[8];
#pragma unroll
    for (int k = 0; k < k1[st] - k0[st]; ++k) { f[2 * k] = *(LAS v4u*)(p0 + k * 8192); f[2 * k + 1] = *(LAS v4u*)(p1 + k * 8192); }
#pragma unroll
    for (int k = 0; k < 2 * (k1[st] - k0[st]); ++k) { gr = mfma(f[k], f[k], gr); sm = mfma(ones, f[k], sm); }
    SCHED();
  }
  const int r = c & 3;
  const float mu = sm[0] * (1.f / 640.f);
  const float var = fmaxf((r == 0 ? gr[0] : r == 1 ? gr[1] : r == 2 ? gr[2] : gr[3]) * (1.f / 640.f) - mu * mu, 0.f) + EPS;
  const float rstd = __builtin_amdgcn_rsqf(var);
  if ((c >> 2) == g) {
    *(LAS float*)(x.lds + L_MISC + 1024 + (lw * 16 + c) * 4) = rstd;
    st4(x.rs, __builtin_bit_cast(u32, var * rstd), O_SIG + nt * 8192 + (rt * 64 + lw * 16 + c) * 4, 0);
    if (nt == 0) st4(x.rs, __builtin_bit_cast(u32, mu), O_MU + (rt * 64 + lw * 16 + c) * 4, 0);
  }
  asm volatile("s_waitcnt lgkmcnt(0)" ::: "memory");
  lds_inc(x, C_XL + 4);
}
DI int stat_at(int n1, int n2) {
  return ((n1 >> 3) * 4 + (n2 >> 3)) * 64 + (2 * ((n2 >> 2) & 1) + ((n1 >> 2) & 1)) * 16 + 4 * (n2 & 3) + (n1 & 3);
}

DI void wt_dma(const Ctx& x, const u16* wr, const u16* wi, int v, int lo) {
  const int fq = v >> 4, fh = v & 15, lw = x.w & 3;
#pragma unroll
  for (int i = 0; i < 2; ++i) {
    const char* src = (const char*)(i ? wi : wr) + (4 * fq + lw) * 41088 + ((fh * 2568) & ~15);
#pragma unroll
    for (int pc = 0; pc < 3; ++pc) dma16(src + min(pc * 64 + x.lane, 160) * 16, (u32)(uintptr_t)x.lds + lo + (lw * 2 + i) * 3072 + pc * 1024);
  }
}
DI void wt_out(const Ctx& x, int v, int lo) {
  const int k0 = 2 * ((x.lane >> 3) * 20 + (x.w & 3) * 5) + ((x.lane >> 2) & 1);
  LAS const u16* sp = (LAS const u16*)(x.lds + lo) + (v & 1) * 4 + (x.lane & 3) * 321;
#pragma unroll
  for (int it = 0; it < 5; ++it) {
    LAS const u16* q = sp + ((it || k0) ? k0 + 2 * it : 320);
    v4u o;
#pragma unroll
    for (int r = 0; r < 4; ++r) o[r] = (u32)q[r * 3072] | ((u32)q[r * 3072 + 1536] << 16);
    st16(x.rs, o, O_WT + v * 20480 + ((x.w & 3) * 5 + it) * 1024 + x.lane * 16, 0);
  }
}
template <int GP> DI int blk_addr(const Ctx& x, int gam0, int rb, int& tile, int& nb) {
  const int p = x.c & 3, r0 = 8 * x.g + (x.c >> 2);
  bool single;
  group_dst<GP>(gam0 + (rb >> 1), rb & 1, tile, nb, single);
  const int ch = (rb >> 1) * 4 + (single ? 2 * (rb & 1) + (p >> 1) : p), hb = single ? (p & 1) : (rb & 1);
  return r0 * 128 + ((ch ^ (((r0 >> 1) & 1) << 2)) << 4) + hb * 8;
}
DI void tile_pos(int j, int& ks, int& ct) {
  const bool is3 = j < 540;
  const int jj = j - (is3 ? 0 : j < 740 ? 540 : 740);
  ks = (jj * (is3 ? 2428 : 6554)) >> 16;
  ct = jj - ks * (is3 ? 27 : 10);
}
DI void tile_dma(const Ctx& x, int j, int lds_off, const u16* w2, const u16* w3, const u16* w4) {
  const int row = x.lane >> 3, ch = ((x.lane & 7) ^ (((row >> 1) & 1) << 2)) << 4, rs = j < 540 ? 3456 : 1280;
  int ks, ct;
  tile_pos(j, ks, ct);
  const char* src = (const char*)(j < 540 ? w3 : j < 740 ? w2 : w4) + (ks * 32 * rs + ct * 128) + (row * rs + ch);
#pragma unroll
  for (int i = 0; i < 4; ++i) dma16(src + i * 8 * rs, (u32)(uintptr_t)x.lds + lds_off + i * 1024);
}
DI void tile_out(const Ctx& x, int j, int lds_off) {
  const bool is3 = j < 540;
  const int GP = is3 ? 3 : 2, tb = is3 ? 143360 : j < 740 ? 102400 : 276480, base = is3 ? O_B6 : j < 740 ? O_B5 : O_B7;
  int ks, ct;
  tile_pos(j, ks, ct);
  const int p = x.c & 3, r0 = 8 * x.g + (x.c >> 2), sw = ((r0 >> 1) & 1) << 6;
  LAS char* lp = x.lds + lds_off + r0 * 128;
  const v4u ones = {0x3f803f80u, 0x3f803f80u, 0x3f803f80u, 0x3f803f80u};
  v4u f[4];
  int dst[4];
#pragma unroll
  for (int rb = 0; rb < 4; ++rb) {
    const int gam = 2 * ct + (rb >> 1), h = rb & 1, sup = (gam * (is3 ? 37 : 52)) >> 8, e = gam - sup * (2 * GP + 1);
    const bool single = e == GP, hi = e > GP;
    dst[rb] = (2 * sup + (int)(hi || (single && h))) * tb + (ks * (2 * GP + 1) + (single ? 2 * GP : 2 * (e - (hi ? GP + 1 : 0)) + h)) * 1024;
    LAS char* ad = lp + (((rb >> 1) * 64 + (single ? h * 32 + p * 8 : p * 16 + h * 8)) ^ sw);
    const v2u lo = trread(ad), hi2 = trread(ad + 512);
    f[rb] = v4u{lo.x, lo.y, hi2.x, hi2.y};
  }
#pragma unroll
  for (int rb = 0; rb < 4; ++rb) st16(x.rs, f[rb], x.lane * 16, base + dst[rb]);
  if (is3) {
#pragma unroll
    for (int rb = 0; rb < 4; ++rb) {
      const f32x4 s = mfma(f[rb], ones, f32x4{0.f, 0.f, 0.f, 0.f});
      if (x.c == 0) st16(x.rs, __builtin_bit_cast(v4u, s), x.g * 16, O_CS3 + (dst[rb] >> 4));
    }
  }
}
DI void side_qc(const Ctx& x, int job, int njobs) {
#pragma unroll 1
  for (int pc = job; pc < 800; pc += njobs) {
    const int ks = pc / 40, blk = pc - ks * 40;
    int tile, nb; bool single;
    group_dst<2>(blk >> 1, blk & 1, tile, nb, single);
    const int n = (blk >> 1) * 32 + (single ? 16 * (blk & 1) + x.c : 8 * (x.c >> 2) + 4 * (blk & 1) + (x.c & 3));
    const float a0 = (float)((n * (16 * ks + 4 * x.g)) % 640) * (1.f / 640.f), da = (float)n * (1.f / 640.f);
    v4u f;
#pragma unroll
    for (int i = 0; i < 4; ++i) {
      const float a = a0 + (float)i * da;
      f[i] = pk(0.05590169944f * cosr(a), -0.05590169944f * sinr(a));
    }
    if (ks == 0 && x.g == 0) f[0] = pk((n & 1) ? -0.03952847075f : 0.03952847075f, 0.f);
    st16(x.rs, f, x.lane * 16, O_BC + (8 + tile) * 102400 + (ks * 5 + nb) * 1024);
  }
}
DI void side_w1p(const Ctx& x, const u16* w1, int ct, int nt) {
  {
    const int row = 8 * x.w + (x.lane >> 3);
    const char* src = (const char*)w1 + row * 1280 + ct * 128 + (((x.lane & 7) ^ (((row >> 1) & 1) << 2)) << 4);
#pragma unroll
    for (int i = 0; i < 10; ++i) {
      const int kr = i + 2 * nt;
      XDMA(src + (kr < 10 ? kr : kr - 10) * 64 * 1280, (u32)(uintptr_t)x.lds + (i * 8 + x.w) * 1024);
    }
  }
  BAR();
  v4u qc[10], qs[10];
  gen_q(64 * nt + 16 * (x.w & 3) + x.c, x.g, 128 * nt, qc, qs);
  f32x4 acc[2][2][2];
#pragma unroll
  for (int i = 0; i < 8; ++i) acc[i >> 2][(i >> 1) & 1][i & 1] = f32x4{0.f, 0.f, 0.f, 0.f};
  int addr[2], tile[2], nb[2];
#pragma unroll
  for (int rb = 0; rb < 2; ++rb) addr[rb] = blk_addr<2>(x, 2 * ct, 2 * (x.w >> 2) + rb, tile[rb], nb[rb]);
  SCHED();
  VMWAIT(0);
  soft_barrier(x, C_SB, 8);
  STAMP(1);
  v2u lo[4], hi[4];
#pragma unroll
  for (int i = 0; i < 4; ++i) { lo[i] = trread(x.lds + addr[i & 1] + (i >> 1) * 4096); hi[i] = trread(x.lds + addr[i & 1] + (i >> 1) * 4096 + 512); }
#pragma unroll
  for (int kc = 0; kc < 10; ++kc) {
    if (kc == 4) { addr[0] += 40960; addr[1] += 40960; PIN(addr[0]); PIN(addr[1]); }
#pragma unroll
    for (int i = 0; i < 4; ++i) {
      const int qi = (2 * kc + (i >> 1)) % 10;
      const v4u f = {lo[i].x, lo[i].y, hi[i].x, hi[i].y};
      acc[kc / 5][i & 1][0] = mfma(qc[qi], f, acc[kc / 5][i & 1][0]);
      acc[kc / 5][i & 1][1] = mfma(qs[qi], f, acc[kc / 5][i & 1][1]);
      if (kc < 9) {
        LAS char* ad = x.lds + addr[i & 1] + ((kc + 1) % 5) * 8192 + (i >> 1) * 4096;
        lo[i] = trread(ad); hi[i] = trread(ad + 512);
      }
      SCHED();
    }
  }
  const f32x4 sg = {1.f, -1.f, 1.f, -1.f};
#pragma unroll
  for (int rb = 0; rb < 2; ++rb) {
    const f32x4 re = acc[0][rb][0] + sg * acc[1][rb][0], im = acc[0][rb][1] + sg * acc[1][rb][1];
    st16(x.rs, v4u{pk(re[0], im[0]), pk(re[1], im[1]), pk(re[2], im[2]), pk(re[3], im[3])}, x.lane * 16,
         O_BC + tile[rb] * 102400 + ((4 * nt + (x.w & 3)) * 5 + nb[rb]) * 1024);
  }
}
DI void side_jobs(const Ctx& x, int xcd, int sl, const u16* wr, const u16* wi, const u16* w1, const u16* w2, const u16* w3, const u16* w4) {
  const int y = sl < 2 ? sl * 8 + xcd : (sl < 7 && xcd >= 2) ? 16 + (sl - 2) * 6 + (xcd - 2) : -1;
  const int ct = sl >= 7 ? xcd : 8 + xcd, nt = sl >= 7 ? sl - 7 : sl - 2, xr = nt * 10 + ct;
  const int v = sl * 8 + xcd, j0 = y < 0 ? xr * 12 : min(600 + y * 15, 1265);
  if (y >= 0) {
    const int job = y * 3 + (x.w >> 2), lo = (x.w >> 2) * 24576;
    if (job < 128) wt_dma(x, wr, wi, job, lo);
    BAR();
    tile_dma(x, j0 + x.w, L_TJ + x.w * 4096, w2, w3, w4);
    VMWAIT(4);
    BAR();
    STAMP(1);
    if (job < 128) wt_out(x, job, lo);
    STAMP(5);
    publish<8, 46>(x, 0, F_W + y, 2, 12);
    if (x.w < 3) {
      lds_wait_ge(x.lds + L_CTL + C_PUB, 12);
      tile_dma(x, j0 + 12 + x.w, L_TJ + (12 + x.w) * 4096, w2, w3, w4);
      VMWAIT(0);
    }
  } else {
    if (x.w < 8) side_w1p(x, w1, ct, nt);
    else {
      BAR();
      __builtin_amdgcn_s_setprio(3);
#pragma unroll 1
      for (int k = x.w & 3; k < 12; k += 4) tile_dma(x, j0 + k, L_TJ + k * 4096, w2, w3, w4);
      side_qc(x, xr * 4 + (x.w & 3), 200);
    }
    STAMP(5);
    publish<8, 50>(x, 1, F_S + xr, 3, 12);
  }
  if (x.w == 11) poll<1, 8>(x, {F_X + v});
  BAR();
  STAMP(61);
#pragma unroll 1
  for (int k = x.w; k < (y < 0 ? 12 : 15); k += 12) tile_out(x, j0 + k, L_TJ + k * 4096);
  STAMP(62);
  publish<8, 96>(x, 6, F_T + v, 47, 12);
  BAR();
}

DI v4u negv(v4u a) { return a ^ 0x80008000u; }
DI void phase_b(const Ctx& x, int p) {
  const int w = x.w, lane = x.lane, g = x.g, c = x.c, wq = w & 3, col = w >> 2;
  v4u C64[2], S64[2], N64[2], C32[2], S32[2], N32[2];
  f32x4 tc[2], ts[2];
  {
    const float f1 = (float)(16 * wq + c);
#pragma unroll
    for (int ks = 0; ks < 2; ++ks) {
      float cv[8], sv[8];
#pragma unroll
      for (int j = 0; j < 8; ++j) { const float a = f1 * (float)(32 * ks + 8 * g + j) * (1.f / 64.f); cv[j] = cosr(a); sv[j] = sinr(a); }
      C64[ks] = v4u{pk(cv[0], cv[1]), pk(cv[2], cv[3]), pk(cv[4], cv[5]), pk(cv[6], cv[7])};
      S64[ks] = v4u{pk(sv[0], sv[1]), pk(sv[2], sv[3]), pk(sv[4], sv[5]), pk(sv[6], sv[7])};
      N64[ks] = negv(S64[ks]);
#pragma unroll
      for (int j = 0; j < 8; ++j) {
        const float a = (float)(16 * ks + c) * (float)(16 * (j >> 2) + 4 * g + (j & 3)) * (1.f / 32.f);
        cv[j] = cosr(a); sv[j] = sinr(a);
      }
      C32[ks] = v4u{pk(cv[0], cv[1]), pk(cv[2], cv[3]), pk(cv[4], cv[5]), pk(cv[6], cv[7])};
      S32[ks] = v4u{pk(sv[0], sv[1]), pk(sv[2], sv[3]), pk(sv[4], sv[5]), pk(sv[6], sv[7])};
      N32[ks] = negv(S32[ks]);
#pragma unroll
      for (int r = 0; r < 4; ++r) {
        const float a = f1 * (float)(16 * ks + 4 * g + r) * (1.f / 2048.f);
        tc[ks][r] = 0.02209708691f * cosr(a); ts[ks][r] = 0.02209708691f * sinr(a);
      }
    }
  }
  const int nt = p >> 5;
#pragma unroll
  for (int i = 0; i < 2; ++i) { PIN(C64[i]); PIN(S64[i]); PIN(N64[i]); PIN(C32[i]); PIN(S32[i]); PIN(N32[i]); PIN(tc[i]); PIN(ts[i]); }
  STAMP(4);
  const int wo = O_WT + (g * 16 + 4 * wq + (c >> 2)) * 20480 + (p % 20) * 1024 + (p / 20) * 128 + col * 64 + (c & 3) * 16;
  const int zo = O_ZT + ((nt * 32 + 4 * w + (lane >> 5)) * 32 + (p & 31)) * 512 + (lane & 31) * 16;
  LAS char* xd = pinp(x.lds + L_XS + ((lane >> 2) & 3) * 4096 + (w >> 2) * 1024 + ((w & 3) * 16 + 4 * (lane >> 4) + (lane & 3)) * 16);
  LAS char* xs = pinp(x.lds + L_XS + col * 8192 + lane * 16);
  const int so = O_SIG + nt * 8192 + stat_at(16 * wq + 4 * g, 16 * col + c) * 4;
  lds_wait_ge(x.lds + L_CTL + C_GO, 2);
  STAMP(6);
  const v4u l0 = ld16(x.rs, zo, 0), l1 = ld16(x.rs, zo, 32768);
  *(LAS v4u*)xd = l0;
  *(LAS v4u*)(xd + 2048) = l1;
  asm volatile("s_waitcnt lgkmcnt(0)" ::: "memory");
  lds_inc(x, C_SB0);
  lds_wait_ge<0>(x.lds + L_CTL + C_GOW, 1);
  STAMP(5);
  v4u wv[2];
#pragma unroll
  for (int t = 0; t < 2; ++t) wv[t] = ld16(x.rs, wo, t * 64 * 20480);
  const f32x4 sg = __builtin_bit_cast(f32x4, ld16(x.rs, so, 0));
  SCHED();
  lds_wait_ge<0>(x.lds + L_CTL + C_SB0, 8);
  STAMP(50);
  v4u xr[2][2], xi[2][2];
#pragma unroll
  for (int i = 0; i < 4; ++i) {
    xr[i >> 1][i & 1] = *(LAS v4u*)(xs + i * 1024);
    xi[i >> 1][i & 1] = *(LAS v4u*)(xs + 4096 + i * 1024);
  }
  SCHED();
  const f32x4 zero = {0.f, 0.f, 0.f, 0.f};
  f32x4 ar[2], ai[2], br[2], bi[2];
#pragma unroll
  for (int b = 0; b < 2; ++b) {
    ar[b] = mfma(xr[b][0], C64[0], zero); ai[b] = mfma(xi[b][0], C64[0], zero);
    ar[b] = mfma(xi[b][0], S64[0], ar[b]); ai[b] = mfma(xr[b][0], N64[0], ai[b]);
    ar[b] = mfma(xr[b][1], C64[1], ar[b]); ai[b] = mfma(xi[b][1], C64[1], ai[b]);
    ar[b] = mfma(xi[b][1], S64[1], ar[b]); ai[b] = mfma(xr[b][1], N64[1], ai[b]);
  }
#pragma unroll
  for (int b = 0; b < 2; ++b) { br[b] = ar[b] * tc[b] + ai[b] * ts[b]; bi[b] = ai[b] * tc[b] - ar[b] * ts[b]; }
  v4u pr = pack8(br[0], br[1]), pi = pack8(bi[0], bi[1]);
  STAMP(49);
#pragma unroll
  for (int t = 0; t < 2; ++t) {
    ar[t] = mfma(C32[t], pr, zero); ai[t] = mfma(C32[t], pi, zero);
    ar[t] = mfma(S32[t], pi, ar[t]); ai[t] = mfma(N32[t], pr, ai[t]);
  }
#pragma unroll
  for (int t = 0; t < 2; ++t)
#pragma unroll
    for (int r = 0; r < 4; ++r) {
      const float wre = blo(wv[t][r]), wim = bhi(wv[t][r]);
      br[t][r] = ar[t][r] * wre - ai[t][r] * wim;
      bi[t][r] = ar[t][r] * wim + ai[t][r] * wre;
    }
  pr = pack8(br[0], br[1]); pi = pack8(bi[0], bi[1]);
#pragma unroll
  for (int t = 0; t < 2; ++t) {
    ar[t] = mfma(C32[t], pr, zero); ai[t] = mfma(C32[t], pi, zero);
    ar[t] = mfma(N32[t], pi, ar[t]); ai[t] = mfma(S32[t], pr, ai[t]);
  }
  {
    LAS char* img = pinp(x.lds + L_XB + col * 9216 + (16 * wq + c) * 72 + g * 8);
#pragma unroll
    for (int t = 0; t < 2; ++t) {
      f32x4 ur = ar[t] * tc[t] - ai[t] * ts[t], ui = ai[t] * tc[t] + ar[t] * ts[t];
      *(LAS v2u*)(img + t * 32) = v2u{pk(ur[0], ur[1]), pk(ur[2], ur[3])};
      *(LAS v2u*)(img + 4608 + t * 32) = v2u{pk(ui[0], ui[1]), pk(ui[2], ui[3])};
    }
  }
  STAMP(38);
  soft_barrier(x, C_SB, 8);
  STAMP(39);
  LAS char* rd = pinp(x.lds + L_XB + (8 * g + (c >> 2)) * 72 + col * 32 + (c & 3) * 8);
#pragma unroll
  for (int cc = 0; cc < 2; ++cc) {
    ar[cc] = zero; ai[cc] = zero;
#pragma unroll
    for (int ks = 0; ks < 2; ++ks) {
      v2u r0 = trread(rd + cc * 9216 + ks * 2304), r1 = trread(rd + cc * 9216 + ks * 2304 + 288);
      v2u i0 = trread(rd + cc * 9216 + 4608 + ks * 2304), i1 = trread(rd + cc * 9216 + 4608 + ks * 2304 + 288);
      const v4u ur = {r0.x, r0.y, r1.x, r1.y}, ui = {i0.x, i0.y, i1.x, i1.y};
      ar[cc] = mfma(C64[ks], ur, ar[cc]); ai[cc] = mfma(C64[ks], ui, ai[cc]);
      ar[cc] = mfma(N64[ks], ui, ar[cc]); ai[cc] = mfma(S64[ks], ur, ai[cc]);
    }
  }
  v2u z[4];
  {
    LAS char* zp = pinp(x.lds + L_XS + (col * 2 + (wq >> 1)) * 1024 + ((2 * (wq & 1) + (g >> 1)) * 16 + c) * 16 + (g & 1) * 8);
#pragma unroll
    for (int i = 0; i < 4; ++i) z[i] = *(LAS v2u*)(zp + i * 4096);
  }
  if (p == 0) ai[0] = zero;
  u32 o[8];
#pragma unroll
  for (int r = 0; r < 4; ++r) {
    float zz[4];
#pragma unroll
    for (int i = 0; i < 4; ++i) zz[i] = (r & 1) ? bhi(z[i][r >> 1]) : blo(z[i][r >> 1]);
    o[2 * r] = pk(sg[r] * zz[0] + ar[0][r], sg[r] * zz[1] + ai[0][r]);
    o[2 * r + 1] = pk(sg[r] * zz[2] + ar[1][r], sg[r] * zz[3] + ai[1][r]);
  }
  const int ob = O_HB + p * HPL + (((8 * wq + 2 * g) * 2 + col) * 16 + c) * 16;
  const bool sw = p & 8;
  st16(x.rs, sw ? v4u{o[2], o[3], o[0], o[1]} : v4u{o[0], o[1], o[2], o[3]}, ob, 0);
  st16(x.rs, sw ? v4u{o[6], o[7], o[4], o[5]} : v4u{o[4], o[5], o[6], o[7]}, ob + 512, 0);
}

#define DLY 6
#define BCW C_SB0
#define BDLY 1
#define GWAIT 12
#define DLYA 6
#define DLYB 10
#define ABL 0
#define HOLD 0
#define LA4 2
#define LA5 5
#define LA6 2
#define LA7 2
#define PACE 6
#define STR2(x) #x
#define STR(x) STR2(x)
DI void dma_lin(const Ctx& x, int ws_off, int lds_off, int p0, int p1) {
  const char* src = x.ws + ws_off + x.lane * 16;
#pragma unroll 1
  for (int pc = p0 + (x.w & 3); pc < p1; pc += 4) {
    dma16_sc1(src + pc * 1024, (u32)(uintptr_t)x.lds + lds_off + pc * 1024);
    asm volatile("s_waitcnt vmcnt(" STR(PACE) ")" ::: "memory");
  }
}
DI int ft_pair(int rowblk, int KS, int gam, int g, int c) { return (rowblk * KS + gam) * 1024 + (g * 16 + c) * 16; }
DI int ft_single(int rowblk, int KS, int gam, int h, int g, int c) {
  return (rowblk * KS + gam) * 1024 + ((2 * h + (g >> 1)) * 16 + c) * 16 + (g & 1) * 8;
}

template <int PH, int NB, class F>
DI void gemm(const Ctx& x, int ao0, int ao1, f32x4 (&o)[NB], float& ssq, F pre) {
  constexpr int KW = (PH & 1) ? 4 : 2, NIT = PH == 5 ? 5 : PH == 7 ? 14 : 10, LA = PH == 5 ? 5 : PH == 4 ? LA4 : PH == 6 ? LA6 : LA7;
  constexpr int AS = PH == 4 ? 16 * HPL : PH == 5 ? 1024 : KW * 1024, BS = (PH == 5 ? 1 : KW) * NB * 1024, PER = PH == 7 ? 3 : 4;
  constexpr int EXO = PH == 4 ? 100 * 1024 : PH == 5 ? 92 * 1024 : 0, EXW = PH == 4 ? 6 : PH == 6 ? 7 : 8;
  const int kw = KW == 4 ? x.w >> 1 : x.w >> 2;
  v4u a[NIT][2];
  auto lda = [&](int it, int i) { a[it][i] = ld16(x.rs, i ? ao1 : ao0, it * AS); };
  auto fix = [&](int it) {
    if (PH == 4) {
      typedef u64 v2l __attribute__((ext_vector_type(2)));
      v2l p0 = __builtin_bit_cast(v2l, a[it][0]), p1 = __builtin_bit_cast(v2l, a[it][1]);
      u64 tmp;
      asm volatile("v_mov_b64 %2, %0\n\tv_mov_b64 %0, %1\n\tv_mov_b64 %1, %2\n\ts_nop 1" : "+v"(p0.y), "+v"(p1.x), "=&v"(tmp));
      a[it][0] = __builtin_bit_cast(v4u, p0); a[it][1] = __builtin_bit_cast(v4u, p1);
    }
  };
#pragma unroll
  for (int it = 0; it < LA; ++it) { lda(it, 0); lda(it, 1); }
  SCHED();
  wait_landed(x, PH - 3);
  STAMP(22 + PH);
  f32x4 acc[2][NB], gr[2];
#pragma unroll
  for (int i = 0; i < 2; ++i) {
    gr[i] = f32x4{0.f, 0.f, 0.f, 0.f};
#pragma unroll
    for (int n = 0; n < NB; ++n) acc[i][n] = gr[i];
  }
  LAS char* bs = x.lds + kw * (PH == 5 ? 5 : 1) * NB * 1024 + x.lane * 16;
  LAS char* bq[3] = {bs + (kw >> 1) * 2048, bs + ((kw >> 1) ^ 1) * 2048, bs + 4096};
  auto frag = [&](int n, int off) { return *(LAS v4u*)((KW == 2 ? bs + n * 1024 : bq[n >> 1] + (n & 1) * 1024) + off); };
  v4u f[NB];
#pragma unroll
  for (int n = 0; n < NB; ++n) f[n] = frag(n, 0);
  pre(acc[0]);
  fix(0);
  LAS char* ring = x.lds + L_CTL + C_RL;
  u32 seen = 0;
#pragma unroll
  for (int it = 0; it < NIT - 1; ++it) {
    SCHED();
    if (PH == 7 && it >= 6) lds_wait_ge_pre(ring + 4 * (it - 6), 4, seen);
    const int st = PH == 7 ? (it + 1) % 7 : it + 1;
    if (PH != 5 && st % PER == 0) {
      const int d = st ? PER * BS : -2 * PER * BS;
      if (KW == 2) bs = pinp(bs + d);
      else {
#pragma unroll
        for (int n = 0; n < 3; ++n) bq[n] = pinp(bq[n] + d);
      }
    }
#pragma unroll
    for (int n = 0; n < NB; ++n) {
      acc[0][n] = mfma(f[n], a[it][0], acc[0][n]);
      acc[1][n] = mfma(f[n], a[it][1], acc[1][n]);
      SCHED();
      f[n] = frag(n, (PH == 5 ? st : st % PER) * BS);
      if (n < 2 && it + LA < NIT) lda(it + LA, n);
      if (PH == 7 && n == 1 && it >= 5 && it < 12) seen = *(volatile LAS u32*)(ring + 4 * (it - 5));
      if (PH >= 6 && n == 1 && it == NIT - 2) seen = *(volatile LAS u32*)(x.lds + L_CTL + C_MID + (PH - 6) * 4);
      if (n == NB - 1) {
        if (PH == 4) { gr[0] = mfma(a[it][0], a[it][0], gr[0]); gr[1] = mfma(a[it][1], a[it][1], gr[1]); SCHED(); }
        fix(it + 1);
      }
      SCHED();
    }
    if (PH == 7 && it < 7) lds_inc(x, C_RING + 4 * it);
    if ((PH == 6 && it == 3) || (PH == 7 && it == 10)) lds_inc(x, C_MID + (PH - 6) * 4);
  }
  if (PH != 4) lds_wait_ge_pre(x.lds + L_CTL + C_MID + (PH - 6) * 4, 8, seen);
  LAS char* mine = pinp(x.lds + EXO + x.w * EXW * 1024 + x.lane * 16);
  const bool live = PH != 7 || kw < 2;
  if (PH == 4) { gr[1] = mfma(a[NIT - 1][1], a[NIT - 1][1], gr[1]); gr[0] = mfma(a[NIT - 1][0], a[NIT - 1][0], gr[0]); }
  if (live) {
#pragma unroll
    for (int n = 0; n < NB; ++n) acc[1][n] = mfma(f[n], a[NIT - 1][1], acc[1][n]);
#pragma unroll
    for (int n = NB - 1; n >= 0; --n) acc[0][n] = mfma(f[n], a[NIT - 1][0], acc[0][n]);
  }
  STAMP(PH == 4 ? 12 : PH == 5 ? 16 : PH == 6 ? 20 : 24);
#pragma unroll
  for (int n = 0; n < NB; ++n) *(LAS f32x4*)(mine + n * 1024) = acc[1][n];
  float s0 = 0.f;
  if (PH == 4) {
    const int r = x.c & 3;
    const bool dg = (x.c >> 2) == x.g;
    *(LAS float*)(mine + NB * 1024) = dg ? (r == 0 ? gr[1][0] : r == 1 ? gr[1][1] : r == 2 ? gr[1][2] : gr[1][3]) : 0.f;
    s0 = dg ? (r == 0 ? gr[0][0] : r == 1 ? gr[0][1] : r == 2 ? gr[0][2] : gr[0][3]) : 0.f;
  }
  if (KW == 4) {
#pragma unroll
    for (int n = 4; n >= 2; --n) *(LAS f32x4*)(mine + (3 + n) * 1024) = acc[0][n];
  }
  asm volatile("s_waitcnt lgkmcnt(0)" ::: "memory");
  lds_st(x.lds + L_CTL + C_EX + x.w * 4, PH);
  if (PH != 7) lds_inc(x, C_FREE + 4 * (PH - 4));
  lds_wait_ge<0>(x.lds + L_CTL + C_EX + (KW == 2 ? x.w ^ 4 : (x.w & 1) + 2 * (x.lane & 3)) * 4, PH);
  STAMP(28 + PH);
  if (KW == 2) {
    LAS char* other = pinp(x.lds + EXO + (x.w ^ 4) * EXW * 1024 + x.lane * 16);
    if (PH == 4) ssq = xsum16_32(s0 + *(LAS float*)(other + NB * 1024));
#pragma unroll
    for (int n = 0; n < NB; ++n) o[n] = acc[0][n] + *(LAS f32x4*)(other + n * 1024);
  } else {
    o[0] = acc[0][0]; o[1] = acc[0][1]; o[4] = acc[0][4];
#pragma unroll
    for (int m = 1; m < 4; ++m) {
      LAS char* s = pinp(x.lds + EXO + (x.w ^ (2 * m)) * EXW * 1024 + x.lane * 16);
      const int b0 = m == 1 ? 0 : m == 3 ? 2 : 5;
      o[0] += *(LAS f32x4*)(s + b0 * 1024);
      o[1] += *(LAS f32x4*)(s + (b0 + 1) * 1024);
      o[4] += *(LAS f32x4*)(s + (m == 2 ? 7 : 4) * 1024);
    }
  }
  if (PH == 5 || PH == 6) { asm volatile("s_waitcnt lgkmcnt(0)" ::: "memory"); lds_inc(x, C_EXD + (PH - 5) * 4); }
}

template <class F> DI void gemm5(const Ctx& x, int a0, int a1, f32x4 (&acc)[5], F late) {
  constexpr int LA = LA5;
  const int kw = x.w >> 2;
  v4u a[10];
  auto lda = [&](int it) { a[it] = ld16(x.rs, it >= 5 ? a1 : a0, it * 1024); };
#pragma unroll
  for (int it = 0; it < LA; ++it) lda(it);
  SCHED();
  wait_landed(x, 2);
  STAMP(27);
#pragma unroll
  for (int n = 0; n < 5; ++n) acc[n] = f32x4{0.f, 0.f, 0.f, 0.f};
  LAS char* bs = x.lds + kw * 51200 + x.lane * 16;
  LAS char* bq[3] = {bs + kw * 2048, bs + (kw ^ 1) * 2048, bs + 4096};
  v4u f[2][5];
#pragma unroll
  for (int d = 0; d < 2; ++d)
#pragma unroll
    for (int n = 0; n < 5; ++n) f[d][n] = *(LAS v4u*)(bq[n >> 1] + (n & 1) * 1024 + d * 5120);
#pragma unroll
  for (int it = 0; it < 10; ++it) {
#pragma unroll
    for (int m = 0; m < 5; ++m) {
      const int n = it == 9 ? 4 - m : m;
      SCHED();
      acc[n] = mfma(f[it & 1][n], a[it], acc[n]);
      SCHED();
      if (it + 2 < 10) f[it & 1][n] = *(LAS v4u*)(bq[n >> 1] + (n & 1) * 1024 + (it + 2) * 5120);
      if (m == 0 && it + LA < 10) lda(it + LA);
      if (m == 0 && it + LA == 10) late();
    }
  }
  STAMP(16);
  LAS char* ex = pinp(x.lds + 136 * 1024 + (x.w & 3) * 5120 + x.lane * 16);
  LAS char* mine = ex + kw * 2048;
  if (kw) *(LAS f32x4*)(ex + 4096) = acc[4];
  *(LAS f32x4*)(mine + 1024) = acc[3];
  *(LAS f32x4*)mine = acc[2];
  asm volatile("s_waitcnt lgkmcnt(0)" ::: "memory");
  lds_st(x.lds + L_CTL + C_EX + x.w * 4, 5);
  lds_inc(x, C_FREE + 4);
  lds_wait_ge<0>(x.lds + L_CTL + C_EX + (x.w ^ 4) * 4, 5);
  STAMP(33);
  LAS char* other = ex + (kw ^ 1) * 2048;
  acc[0] += *(LAS f32x4*)other;
  acc[1] += *(LAS f32x4*)(other + 1024);
  acc[4] += *(LAS f32x4*)(ex + 4096);
  asm volatile("s_waitcnt lgkmcnt(0)" ::: "memory");
  lds_inc(x, C_EXD);
}

template <class W> DI void phase_c(const Ctx& x, int P, int t, W wait) {
  const int mw = x.w & 3, kw = x.w >> 2, g = x.g, c = x.c;
  const int np = 2 * P + (mw >> 1), b = mw & 1;
  const int rowblk = 4 * np + b + 2 * kw;
  int mo = O_MU + stat_at(rowblk >> 1, 16 * (rowblk & 1) + c) * 4;
  int ao = O_HB + (8 * kw + 2 * g) * HPL + ((np * 2 + b) * 16 + c) * 16, ao1 = ao + HPL;
  PIN(mo); PIN(ao); PIN(ao1);
  wait();
  f32x4 acc[5];
  float ssq, mu;
  gemm<4, 5>(x, ao, ao1, acc, ssq, [&](f32x4 (&)[5]) { mu = __builtin_bit_cast(float, ld4(x.rs, mo, 0)); });
  const float vh = ssq * (1.f / 640.f), sc = __builtin_amdgcn_rsqf(vh + EPS), sg = (vh + EPS) * sc;
  if (g == 0) *(LAS f32x2*)(x.lds + L_MISC + 1024 + ((rowblk & 7) * 16 + c) * 8) = f32x2{sg, mu};
  const int t8 = t & 7, g0 = (t8 >> 1) * 5 + (t8 & 1) * 3, gs = (t8 >> 1) * 5 + 2;
  const int op = ft_pair(rowblk, 20, g0, g, c), os = ft_single(rowblk, 20, gs, t8 & 1, g, c);
  if (t < 8) {
    const float v2 = vh * sc * sc + EPS, s2 = __builtin_amdgcn_rsqf(v2);
    const f32x3 ge = gelu_k(sc * s2, sg * v2 * s2);
    st16(x.rs, gelu(ge, acc[0], acc[1]), O_G + op, 0);
    st16(x.rs, gelu(ge, acc[2], acc[3]), O_G + op, 1024);
    st8(x.rs, gelu(ge, acc[4]), O_G + os, 0);
  } else {
    st16(x.rs, pack8(acc[0] * sc, acc[1] * sc), O_XB + op, 0);
    st16(x.rs, pack8(acc[2] * sc, acc[3] * sc), O_XB + op, 1024);
    st8(x.rs, pack4(acc[4] * sc), O_XB + os, 0);
  }
}

template <class W> DI void phase_5(const Ctx& x, int P, int hf, int t8, u32 (&res)[6], W wait) {
  const int kw = x.w >> 2, g = x.g, c = x.c;
  const int rowblk = 8 * P + 4 * hf + 2 * (x.w & 1) + ((x.w >> 1) & 1);
  const int gp = (t8 >> 1) * 5 + (t8 & 1) * 3 + kw, gs = (t8 >> 1) * 5 + 2;
  int op = ft_pair(rowblk, 20, gp, g, c), os = ft_single(rowblk, 20, gs, t8 & 1, g, c);
  int ao[2];
#pragma unroll
  for (int i = 0; i < 2; ++i) {
    const int r = rowblk * 16 + c - (i ? -1 - kw : 1 + kw);
    ao[i] = (u32)r < 2048u ? O_G + ft_pair(r >> 4, 20, 10 * kw, g, r & 15) : 0x7f000000;
  }
  PIN(ao[0]); PIN(ao[1]); PIN(op); PIN(os);
  wait();
  v4u xp;
  v2u xs;
  f32x4 o[5];
  gemm5(x, ao[0], ao[1], o, [&] { xp = ld16(x.rs, op, O_XB); xs = ld8(x.rs, os, O_XB); });
  const f32x2 st = *(LAS f32x2*)(x.lds + L_MISC + 1024 + ((rowblk & 7) * 16 + c) * 8);
  const f32x4 b0 = unlo(xp), b1 = unhi(xp), b2 = un4(xs);
  o[0] += b0; o[1] += b1; o[4] += b2;
  const v4u r = pack8(o[0], o[1]);
  st16(x.rs, r, O_X1 + op, 0);
  const v4u ones = {0x3f803f80u, 0x3f803f80u, 0x3f803f80u, 0x3f803f80u};
  float s1 = dot8(r, ones, 0.f), s2 = dot8(r, r, 0.f);
  if (kw == 0) {
    const v2u q = pack4(o[4]);
    st8(x.rs, q, O_X1 + os, 0);
    s1 = dot2(q.y, ones.x, dot2(q.x, ones.x, s1));
    s2 = dot2(q.y, q.y, dot2(q.x, q.x, s2));
  }
  s1 = xsum16_32(s1); s2 = xsum16_32(s2);
  if (g == 0) st8(x.rs, v2u{__builtin_bit_cast(u32, s1), __builtin_bit_cast(u32, s2)}, O_ST1 + (rowblk * 16 + t8 * 2 + kw) * 128 + c * 8, 0);
  const v4u rp = pack8(o[0] + b0 * st[0] + st[1], o[1] + b1 * st[0] + st[1]);
  const v2u rq = pack4(o[4] + b2 * st[0] + st[1]);
  res[0] = rp.x; res[1] = rp.y; res[2] = rp.z; res[3] = rp.w; res[4] = rq.x; res[5] = rq.y;
#pragma unroll
  for (int i = 0; i < 6; ++i) PIN(res[i]);
}

template <class W> DI void phase_6(const Ctx& x, int P, int t, W wait) {
  const int g = x.g, c = x.c, kw = x.w >> 2, rowblk = 8 * P + 2 * (x.w & 3) + kw;
  int so = O_ST1 + rowblk * 2048 + x.lane * 8, ao0 = O_X1 + (rowblk * 20 + kw) * 1024 + x.lane * 16, ao1 = O_X1 + ((rowblk ^ 1) * 20 + kw) * 1024 + x.lane * 16;
  PIN(so); PIN(ao0); PIN(ao1);
  wait();
  f32x2 p[4];
#pragma unroll
  for (int i = 0; i < 4; ++i) p[i] = __builtin_bit_cast(f32x2, ld8(x.rs, so + i * 512, 0));
  f32x4 acc[7];
  float dummy, var;
  gemm<6, 7>(x, ao0, ao1, acc, dummy, [&](f32x4 (&a0)[7]) {
    const f32x2 ps = (p[0] + p[1]) + (p[2] + p[3]);
    const float m1 = xsum16_32(ps[0]) * (1.f / 640.f), q1 = xsum16_32(ps[1]) * (1.f / 640.f);
    var = fmaxf(q1 - m1 * m1, 0.f) + EPS;
    PIN(var);
    LAS f32x4* cs = (LAS f32x4*)pinp(x.lds + L_MISC + g * 16);
#pragma unroll
    for (int n = 0; n < 7; ++n) a0[n] = cs[4 * n] * -m1;
  });
  const float r1 = __builtin_amdgcn_rsqf(var);
  const f32x3 ge = gelu_k(r1, var * r1);
  const int g0 = (t >> 1) * 7 + (t & 1) * 4, gs = (t >> 1) * 7 + 3, op = O_U + ft_pair(rowblk, 54, g0, g, c);
  st16(x.rs, gelu(ge, acc[0], acc[1]), op, 0);
  st8(x.rs, gelu(ge, acc[6]), O_U + ft_single(rowblk, 54, gs, t & 1, g, c), 0);
  if (t < 15) {
    st16(x.rs, gelu(ge, acc[2], acc[3]), op, 1024);
    st16(x.rs, gelu(ge, acc[4], acc[5]), op, 2048);
  }
}
DI void colsum_6(const Ctx& x, int t) {
  if (x.lane < 56) {
    f32x2 s = {0.f, 0.f};
#pragma unroll
    for (int k = 0; k < 20; ++k) s += __builtin_bit_cast(f32x2, ld8(x.rs, O_CS3 + t * 8960 + x.lane * 8 + k * 448, 0));
    *(LAS f32x2*)(x.lds + L_MISC + x.lane * 8) = s;
  }
}

template <class W> DI void phase_7(const Ctx& x, int P, int hf, int t8, const u32 (&res)[6], u16* out, W wait) {
  const int kw = x.w >> 1, g = x.g, c = x.c;
  const int rowblk = 8 * P + 4 * hf + 2 * (x.w & 1) + (kw & 1);
  const int gp = (t8 >> 1) * 5 + (t8 & 1) * 3 + (kw >> 1), gs = (t8 >> 1) * 5 + 2;
  int ao0 = O_U + (rowblk * 54 + kw) * 1024 + x.lane * 16, ao1 = O_U + ((rowblk ^ 1) * 54 + kw) * 1024 + x.lane * 16;
  PIN(ao0); PIN(ao1);
  wait();
  f32x4 o[5];
  float dummy;
  gemm<7, 5>(x, ao0, ao1, o, dummy, [](f32x4 (&)[5]) {});
  char* orow = (char*)out + (rowblk * 16 + c) * 1280;
  *(v4u*)(orow + gp * 64 + g * 16) = pack8(o[0] + un4(v2u{res[0], res[1]}), o[1] + un4(v2u{res[2], res[3]}));
  if (kw < 2) *(v2u*)(orow + gs * 64 + (t8 & 1) * 32 + g * 8) = pack4(o[4] + un4(v2u{res[4], res[5]}));
}

template <int R0, int R1> DI void stage_load(const Ctx& x, int voff, u64 skip, v4u (&r)[35]) {
#pragma unroll
  for (int k = R0; k < R1; ++k) {
    r[k] = ld16(x.rs, voff, ((skip >> k) & 1) ? 0x7f000000 : k * 4096);
    asm volatile("s_waitcnt vmcnt(" STR(PACE) ")" ::: "memory");
  }
}
template <int R0, int R1> DI void stage_drop(LAS char* p, const v4u (&r)[35]) {
#pragma unroll
  for (int k = R0; k < R1; ++k) {
    if (k % 16 == 0 || k == R0) p = pinp(p + (k == R0 ? (R0 / 16) * 65536 : 65536));
    *(LAS v4u*)(p + (k % 16) * 4096) = r[k];
  }
}
DI void dropped(const Ctx& x) { asm volatile("s_waitcnt lgkmcnt(0)" ::: "memory"); lds_inc(x, C_LAND); }
template <int S> DI void ring_drop(const Ctx& x, LAS char* p, const v4u (&r)[35]) {
  lds_wait_ge(x.lds + L_CTL + C_RING + 4 * S, 8);
  stage_drop<5 * S, S < 6 ? 5 * S + 5 : 33>(p, r);
  asm volatile("s_waitcnt lgkmcnt(0)" ::: "memory");
  lds_inc(x, C_RL + 4 * S);
}
DI void weight_stream(const Ctx& x, int t, int t8) {
  const int lo = (x.w & 3) * 1024 + x.lane * 16;
  LAS char* p = x.lds + lo;
  v4u r[35];
#pragma unroll 1
  for (int s = 0; s < 4; ++s) {
    const int voff = lo + (s == 0 ? O_B5 + t8 * 102400 : s == 1 ? O_B6 + t * 143360 : O_B7 + t8 * 276480 + (s - 2) * 143360);
    const int lw = x.w & 3;
    const u64 skip = (s == 1 && t == 15) ? 0x810204081ull * (lw == 0 ? 0x5a : lw == 1 ? 0x56 : lw == 2 ? 0x55 : 0x35)
                                         : s == 3 ? (lw < 2 ? 6ull : 7ull) << 32 : 0;
    stage_load<0, 25>(x, voff, skip, r);
    if (s) stage_load<25, 35>(x, voff, skip, r);
    STAMPL(41 + s);
    if (s == 3) break;
    lds_wait_ge(x.lds + L_CTL + C_FREE + 4 * s, 8);
    stage_drop<14, 25>(p, r);
    if (s) stage_drop<25, 34>(p, r);
    if (s) lds_wait_ge(x.lds + L_CTL + C_EXD + 4 * (s - 1), 8);
    stage_drop<0, 14>(p, r);
    if (s) stage_drop<34, 35>(p, r);
    dropped(x);
    STAMPL(51 + s);
  }
  ring_drop<0>(x, p, r); ring_drop<1>(x, p, r); ring_drop<2>(x, p, r); ring_drop<3>(x, p, r);
  ring_drop<4>(x, p, r); ring_drop<5>(x, p, r); ring_drop<6>(x, p, r);
}

DI void wait_b(const Ctx& x, bool poller, int xcd) {
  wait_flags<3, DLYB>(x, poller, {F_B + xcd * 160 + x.lane, F_B + xcd * 160 + 64 + x.lane, F_B + xcd * 160 + 128 + (x.lane & 31)}, 3);
}
#define CODE_KB 48
#define MAP 1
__global__ __launch_bounds__(NTHR, 3) void fused(const u16* __restrict__ xin, const u16* __restrict__ wr, const u16* __restrict__ wi,
                                                 const u16* __restrict__ w1, const u16* __restrict__ w2, const u16* __restrict__ w3,
                                                 const u16* __restrict__ w4, u16* __restrict__ out, char* ws) {
  extern __shared__ __attribute__((aligned(16))) char smem[];
  if (blockIdx.x >= 240 && threadIdx.x >= 512) {
    u64 pc;
    asm volatile("s_getpc_b64 %0" : "=s"(pc));
    const char* code = (const char*)(pc & ~255ull) + (threadIdx.x - 512) * 16;
#pragma unroll 1
    for (int k = 0; k < CODE_KB / 4; ++k) dma16_sc1(code + k * 4096, (u32)(uintptr_t)(LAS char*)smem + L_MISC + 2048);
  }
  Ctx x;
  x.ws = ws; x.rs = __builtin_amdgcn_make_buffer_rsrc(ws, 0, WS_END, 0x00020000); x.lds = (LAS char*)smem;
  x.tid = threadIdx.x; x.lane = x.tid & 63; x.w = __builtin_amdgcn_readfirstlane(x.tid >> 6); x.g = x.lane >> 4; x.c = x.lane & 15;
  GAS u64* ep = (GAS u64*)(ws + O_EPOCH);
  x.epoch = __hip_atomic_load(ep, __ATOMIC_RELAXED, __HIP_MEMORY_SCOPE_AGENT);
  const int b = blockIdx.x, xcd = b & 7, i = b >> 3;
  const int P = 4 * (xcd >> 1) + (i >> 3), t = 8 * (xcd & 1) + (i & 7), hf = (i >> 2) & 1, t8 = 4 * (xcd & 1) + (i & 3);
  STAMP(0);
  if (x.w == 8) lds_st(x.lds + L_CTL + x.lane * 4, 0);
  if (b >= 160) side_jobs(x, xcd, i - 20, wr, wi, w1, w2, w3, w4);
  if (x.w >= 8) {
    auto poll_s = [&]() {
      poll<1, 8>(x, {F_S + xcd * 50 + min(x.lane, 49)});
      lds_st(x.lds + L_CTL + C_LGO, 1);
      STAMPW(9);
    };
    if (b < 160) {
      const int lw = x.w & 3;
      BAR();
      __builtin_amdgcn_s_setprio(3);
      phase_a_stats(x, xin, xcd, i & 3, i >> 2);
      STAMPL(59);
      publish<8, 160>(x, 0, F_A + b, 3, 12);
      __builtin_amdgcn_s_setprio(3);
      if (lw == 3) {
        lds_wait_ge(x.lds + L_CTL + C_PUB, GWAIT);
        poll<1, 4>(x, {F_W + xcd * 46 + min(x.lane, 45)});
        lds_inc(x, C_GOW);
      }
      if (lw == 1) {
        lds_wait_ge(x.lds + L_CTL + C_PUB, 12);
        wait_flags<1, DLYA>(x, true, {F_A + xcd * 160 + (b & ~31) + (x.lane & 31)}, 2);
        if (b < 96 && x.lane < 8) __hip_atomic_store((GAS u64*)(x.ws + flag_at(F_X + b) + x.lane * 8), x.tag(), __ATOMIC_RELAXED, __HIP_MEMORY_SCOPE_AGENT);
      }
      if (lw == 0) { lds_wait_ge(x.lds + L_CTL + C_GO, 2); poll_s(); }
      __builtin_amdgcn_s_setprio(0);
      lds_wait_ge(x.lds + L_CTL + BCW, 8);
      __builtin_amdgcn_s_sleep(BDLY);
    } else if (x.w == 8) poll_s();
    lds_wait_ge(x.lds + L_CTL + C_LGO, 1);
    dma_lin(x, O_BC + t * 102400, 0, 0, 100);
    LANDED(0);
    STAMPL(40);
    if (x.w == 8) {
      poll<2, 16>(x, {F_T + xcd * 96 + x.lane, F_T + xcd * 96 + 64 + (x.lane & 31)});
      lds_st(x.lds + L_CTL + C_LGO, 2);
      colsum_6(x, t);
    } else lds_wait_ge(x.lds + L_CTL + C_LGO, 2);
    STAMPL(45);
    weight_stream(x, t, t8);
    return;
  }
  bool last;
  if (b < 160) {
    phase_a(x, xin, xcd, i & 3, i >> 2);
    STAMP(2);
    publish<8, 160>(x, 0, F_A + b, 3, 12);
    refresh(x);
    phase_b(x, b);
    STAMP(7);
    last = publish<8, 160>(x, 1, F_B + b, 8);
  } else {
    last = x.w == 0;
    if (last) poll<1, 8>(x, {F_B + xcd * 160 + i});
  }
  refresh(x);
  phase_c(x, P, t, [&] { wait_b(x, last, xcd); STAMP(11); });
  STAMP(13);
  last = publish(x, 2, F_C + P * 16 + t, 14);
  refresh(x);
  u32 res[6];
  phase_5(x, P, hf, t8, res, [&] {
    wait_flags<1, DLY>(x, last, {F_C + (x.lane < 32 ? P * 16 + x.c : ((P + (hf ? 1 : 15)) & 15) * 16 + (x.lane & 7))}, 4);
    STAMP(15);
  });
  STAMP(17);
  last = publish(x, 3, F_5 + P * 16 + t, 18);
  refresh(x);
  phase_6(x, P, t, [&] { wait_flags<1, DLY>(x, last, {F_5 + P * 16 + x.c}, 5); STAMP(19); });
  STAMP(21);
  last = publish(x, 4, F_6 + P * 16 + t, 22);
  refresh(x);
  phase_7(x, P, hf, t8, res, out, [&] { wait_flags<1, DLY>(x, last, {F_6 + P * 16 + x.c}, 6); STAMP(23); });
  STAMP(25);
  if (b == 0 && x.tid == 0) __hip_atomic_store(ep, x.tag(), __ATOMIC_RELAXED, __HIP_MEMORY_SCOPE_AGENT);
}

#define WSOFF 0
extern "C" void kernel_launch(void* const* d_in, const int* in_sizes, int n_in, void* d_out, int out_size, void* d_ws,
                              size_t ws_size, hipStream_t stream) {
  (void)hipFuncSetAttribute((const void*)fused, hipFuncAttributeMaxDynamicSharedMemorySize, LDS_BYTES);
  fused<<<256, NTHR, LDS_BYTES, stream>>>((const u16*)d_in[0], (const u16*)d_in[1], (const u16*)d_in[2], (const u16*)d_in[4],
                                          (const u16*)d_in[5], (const u16*)d_in[6], (const u16*)d_in[7], (u16*)d_out, (char*)d_ws + WSOFF);
}
```

```cpp
#define DIAG 0
#include <hip/hip_runtime.h>
#include <stdint.h>

typedef unsigned u32;
typedef unsigned long long u64;
typedef unsigned short u16;
typedef u32 v4u __attribute__((ext_vector_type(4)));
typedef u32 v2u __attribute__((ext_vector_type(2)));
typedef float f32x4 __attribute__((ext_vector_type(4)));
typedef float f32x2 __attribute__((ext_vector_type(2)));
typedef __bf16 bf16x8 __attribute__((ext_vector_type(8)));
typedef __bf16 bf16x2 __attribute__((ext_vector_type(2)));
typedef short s16x4 __attribute__((ext_vector_type(4)));
typedef __amdgpu_buffer_rsrc_t rsrc_t;
#define LAS __attribute__((address_space(3)))
#define GAS __attribute__((address_space(1)))
#define DI __device__ __forceinline__

constexpr int ACT = 2048 * 640 * 2;
constexpr int O_EPOCH = 0;
constexpr int O_FLAG = 4096;
enum { F_C = 240, F_5 = 496, F_6 = 752, F_X = 1008, F_S = 1104, F_T = F_S + 8 * 50, F_B = F_T + 8 * 96, F_A = F_B + 8 * 160, F_W = F_A + 8 * 160, F_END = F_W + 8 * 46 };
__host__ __device__ constexpr int flag_at(int i) { return O_FLAG + i * 256; }
constexpr int O_SIG = 2 << 20;
static_assert(flag_at(F_END) <= O_SIG, "flag area");
constexpr int O_MU = O_SIG + 5 * 8192;
constexpr int O_CS3 = O_MU + 8192;
constexpr int O_ST1 = O_CS3 + 20 * 1792 * 4;
constexpr int O_ZT = O_ST1 + 2048 * 128;
constexpr int O_WT = O_ZT + ACT;
constexpr int O_HB = O_WT + ACT;
constexpr int O_BC = O_HB + ACT;
constexpr int O_B5 = O_BC + 8 * 102400 + 8192;
constexpr int O_B6 = O_B5 + 8 * 102400 + 8192;
constexpr int O_B7 = O_B6 + 16 * 143360 + 8192;
constexpr int O_G = O_B7 + 8 * 276480 + 8192;
constexpr int O_X1 = O_G + ACT;
constexpr int O_U = O_X1 + ACT;
constexpr int WS_END = O_U + 2048 * 1728 * 2;

constexpr int LDS_BYTES = 160 * 1024;
constexpr int L_MISC = 156 * 1024;
constexpr int L_CTL = 159 * 1024;
constexpr int L_TJ = 84 * 1024;
constexpr int L_Q = 100 * 1024;
constexpr int L_SQ = 150 * 1024;
constexpr int L_XS = 116 * 1024;
constexpr int L_XB = 132 * 1024;
enum { C_SB = 4, C_GO = 8, C_LAND = 12, C_LGO = 16, C_GOW = 20, C_SB0 = 24, C_SB1 = 28, C_PUB = 32, C_FREE = 64, C_QOK = 76, C_MID = 80, C_EX = 96, C_QF = 128, C_RING = 136, C_RL = 164, C_XL = 192, C_EXD = 208 };
constexpr int NTHR = 768;
constexpr float EPS = 1e-5f;
constexpr int SC1 = 16;

DI u32 pk(float a, float b) { f32x2 v = {a, b}; return __builtin_bit_cast(u32, __builtin_convertvector(v, bf16x2)); }
DI float blo(u32 v) { return __builtin_bit_cast(float, v << 16); }
DI float bhi(u32 v) { return __builtin_bit_cast(float, v & 0xffff0000u); }
DI f32x4 mfma(v4u a, v4u b, f32x4 c) {
  return __builtin_amdgcn_mfma_f32_16x16x32_bf16(__builtin_bit_cast(bf16x8, a), __builtin_bit_cast(bf16x8, b), c, 0, 0, 0);
}
DI float dot2(u32 a, u32 b, float c) {
  return __builtin_amdgcn_fdot2_f32_bf16(__builtin_bit_cast(bf16x2, a), __builtin_bit_cast(bf16x2, b), c, false);
}
DI float dot8(v4u a, v4u b, float c) { return dot2(a.w, b.w, dot2(a.z, b.z, dot2(a.y, b.y, dot2(a.x, b.x, c)))); }
DI v2u trread(LAS char* p) { return __builtin_bit_cast(v2u, __builtin_amdgcn_ds_read_tr16_b64_v4i16((LAS s16x4*)p)); }
DI float cosr(float rev) { return __builtin_amdgcn_cosf(rev); }
DI float sinr(float rev) { return __builtin_amdgcn_sinf(rev); }
typedef float f32x3 __attribute__((ext_vector_type(3)));
DI f32x3 gelu_k(float r, float inv_r) { return f32x3{-2.302208198f * r, -0.1029432397f * r * r * r, inv_r}; }
DI u32 gelu(f32x3 k, f32x2 s) {
  const f32x2 y = s * (s * s * k[1] + k[0]);
  const f32x2 d = f32x2{__builtin_amdgcn_exp2f(y[0]), __builtin_amdgcn_exp2f(y[1])} * k[2] + k[2];
  const f32x2 o = s * f32x2{__builtin_amdgcn_rcpf(d[0]), __builtin_amdgcn_rcpf(d[1])};
  return pk(o[0], o[1]);
}
DI v2u gelu(f32x3 k, f32x4 a) { return v2u{gelu(k, a.xy), gelu(k, a.zw)}; }
DI v4u gelu(f32x3 k, f32x4 a, f32x4 b) { return v4u{gelu(k, a.xy), gelu(k, a.zw), gelu(k, b.xy), gelu(k, b.zw)}; }
DI void dma16(const void* gsrc, u32 lds_dst) {
  u32 keep;
  asm volatile("s_mov_b32 %0, m0\n\ts_mov_b32 m0, %2\n\ts_nop 0\n\tglobal_load_lds_dwordx4 %1, off\n\ts_mov_b32 m0, %0"
               : "=&s"(keep) : "v"(gsrc), "s"(lds_dst) : "memory");
}
DI void dma16_sc1(const void* gsrc, u32 lds_dst) {
  u32 keep;
  asm volatile("s_mov_b32 %0, m0\n\ts_mov_b32 m0, %2\n\ts_nop 0\n\tglobal_load_lds_dwordx4 %1, off sc1\n\ts_mov_b32 m0, %0"
               : "=&s"(keep) : "v"(gsrc), "s"(lds_dst) : "memory");
}
#define VMWAIT(n) asm volatile("s_waitcnt vmcnt(" #n ")" ::: "memory")
#define BAR() asm volatile("s_waitcnt lgkmcnt(0)\n\ts_barrier" ::: "memory")
#define SCHED() __builtin_amdgcn_sched_barrier(0)
DI v4u ld16(rsrc_t r, int voff, int soff) { return __builtin_amdgcn_raw_buffer_load_b128(r, voff, soff, SC1); }
DI v2u ld8(rsrc_t r, int voff, int soff) { return __builtin_amdgcn_raw_buffer_load_b64(r, voff, soff, SC1); }
DI u32 ld4(rsrc_t r, int voff, int soff) { return __builtin_amdgcn_raw_buffer_load_b32(r, voff, soff, SC1); }
DI void st16(rsrc_t r, v4u v, int voff, int soff) { __builtin_amdgcn_raw_buffer_store_b128(v, r, voff + soff, 0, SC1); }
DI void st8(rsrc_t r, v2u v, int voff, int soff) { __builtin_amdgcn_raw_buffer_store_b64(v, r, voff, soff, SC1); }
DI void st4(rsrc_t r, u32 v, int voff, int soff) { __builtin_amdgcn_raw_buffer_store_b32(v, r, voff, soff, SC1); }
DI float xsum16_32(float v) {
  float u = v;
  asm volatile("s_nop 1\n\tv_permlane16_swap_b32 %0, %1" : "+v"(u), "+v"(v));
  v += u; u = v;
  asm volatile("s_nop 1\n\tv_permlane32_swap_b32 %0, %1" : "+v"(u), "+v"(v));
  return v + u;
}
template <int CTRL> DI float dppadd(float v) {
  return v + __builtin_bit_cast(float, __builtin_amdgcn_mov_dpp(__builtin_bit_cast(int, v), CTRL, 0xf, 0xf, true));
}
DI float xsum8(float v) { return dppadd<0x141>(dppadd<0x4E>(dppadd<0xB1>(v))); }
#define PIN(v) asm volatile("" : "+v"(v))

#define STAMP(i) do { if ((i) == DIAG) { u64 t_; u32 a_, b_; asm volatile("s_memrealtime %0\n\ts_waitcnt lgkmcnt(0)" : "=s"(t_) :: "memory"); asm volatile("v_mov_b32 %0, %2\n\tv_mov_b32 %1, %3\n\tds_write_b32 %0, %1" : "=&v"(a_), "=&v"(b_) : "s"(L_CTL + (x.w == 0 ? 512 : 516)), "s"((u32)t_) : "memory"); } } while (0)
#define STAMP2(i)
#define STAMPA(i)
#define STAMPW(i)
#define STAMPL(i)
struct Ctx {
  char* ws; rsrc_t rs; LAS char* lds; u64 epoch;
  int tid, lane, w, g, c;
  DI u64 tag() const { u64 e = epoch; asm volatile("" : "+v"(e)); return e + 0x9E3779B97F4A7C15ull; }
};
DI void refresh(Ctx& x) {
  int l = x.lane;
  asm volatile("" : "+v"(l));
  x.lane = l; x.g = l >> 4; x.c = l & 15;
}
DI LAS char* pinp(LAS char* p) { asm volatile("" : "+v"(p)); return p; }
#define LSLP 1
#define GSLP 1
#define PSLP 1
template <int SLP = LSLP> DI void lds_wait_ge(LAS char* p, u32 want) {
  u32 v, n = 1u << 20;
  asm volatile("1:\n\tds_read_b32 %0, %2\n\ts_waitcnt lgkmcnt(0)\n\tv_cmp_gt_u32 vcc, %3, %0\n\ts_sub_u32 %1, %1, 1\n\ts_cbranch_scc1 2f\n\t"
               "s_cbranch_vccz 2f\n\ts_sleep %4\n\ts_branch 1b\n2:"
               : "=&v"(v), "+s"(n) : "v"(p), "s"(want), "n"(SLP) : "vcc", "scc", "memory");
}
DI void lds_wait_ge_pre(LAS char* p, u32 want, u32 v) {
  u32 n = 1u << 20;
  asm volatile("1:\n\tv_cmp_gt_u32 vcc, %3, %0\n\ts_sub_u32 %1, %1, 1\n\ts_cbranch_scc1 2f\n\ts_cbranch_vccz 2f\n\t"
               "ds_read_b32 %0, %2\n\ts_waitcnt lgkmcnt(0)\n\ts_branch 1b\n2:"
               : "+v"(v), "+s"(n) : "v"(p), "n"(want) : "vcc", "scc", "memory");
}
DI void lds_inc(const Ctx& x, int word) {
  u64 keep;
  asm volatile("s_mov_b64 %0, exec\n\ts_mov_b64 exec, 1\n\tds_add_u32 %1, %2\n\ts_mov_b64 exec, %0"
               : "=&s"(keep) : "v"(x.lds + L_CTL + word), "v"(1u) : "memory");
}
template <int REP = 1, int STR = 0> DI bool publish(const Ctx& x, int idx, int flag, int stamp, int NW = 8) {
  __builtin_amdgcn_s_setprio(3);
  __builtin_amdgcn_s_waitcnt(0x0070);
  asm volatile("s_waitcnt vmcnt(0) lgkmcnt(0)" ::: "memory");
  u32 old;
  u64 keep;
  asm volatile("s_mov_b64 %1, exec\n\ts_mov_b64 exec, 1\n\tds_add_rtn_u32 %0, %2, %3\n\ts_mov_b64 exec, %1\n\ts_waitcnt lgkmcnt(0)"
               : "=&v"(old), "=&s"(keep) : "v"(x.lds + L_CTL + C_PUB + 4 * idx), "v"(1u) : "memory");
  const bool last = __builtin_amdgcn_readfirstlane(old) == NW - 1;
  if (last) {
    if (x.lane < 8 * REP)
      __hip_atomic_store((GAS u64*)(x.ws + flag_at(flag + (x.lane >> 3) * STR) + (x.lane & 7) * 8), x.tag(), __ATOMIC_RELAXED, __HIP_MEMORY_SCOPE_AGENT);
    STAMPW(stamp);
  }
  asm volatile("" ::: "memory");
  __builtin_amdgcn_s_setprio(0);
  return last;
}
DI void publish_inline(const Ctx& x, int idx, int flag) {
  u32 old;
  u64 keep;
  asm volatile("s_waitcnt vmcnt(0)\n\ts_mov_b64 %1, exec\n\ts_mov_b64 exec, 1\n\tds_add_rtn_u32 %0, %2, %3\n\ts_mov_b64 exec, %1\n\ts_waitcnt lgkmcnt(0)"
               : "=&v"(old), "=&s"(keep) : "v"(x.lds + L_CTL + C_PUB + 4 * idx), "v"(1u) : "memory");
  const bool last = __builtin_amdgcn_readfirstlane(old) == 7;
  st8(x.rs, __builtin_bit_cast(v2u, x.tag()), (last && x.lane < 8) ? flag_at(flag) + x.lane * 8 : 0x7f000000, 0);
  asm volatile("" ::: "memory");
}
DI void soft_barrier(const Ctx& x, int word, u32 target) {
  asm volatile("s_waitcnt lgkmcnt(0)" ::: "memory");
  lds_inc(x, word);
  lds_wait_ge<0>(x.lds + L_CTL + word, target);
}
template <int N, int SLP = PSLP> DI void poll(const Ctx& x, const int (&off)[N]) {
  const u64 tag = x.tag();
  int o[N];
#pragma unroll
  for (int i = 0; i < N; ++i) o[i] = flag_at(off[i]);
#pragma unroll 1
  for (int spin = 0; spin < (1 << 16); ++spin) {
    v2u v[N];
#pragma unroll
    for (int i = 0; i < N; ++i) v[i] = ld8(x.rs, o[i], 0);
    bool ok = true;
#pragma unroll
    for (int i = 0; i < N; ++i) {
      if (__builtin_bit_cast(u64, v[i]) == tag) o[i] = 0x7f000000;
      ok &= o[i] == 0x7f000000;
    }
    if (__all(ok)) break;
    __builtin_amdgcn_s_sleep(SLP);
    asm volatile("" ::: "memory");
  }
  __builtin_amdgcn_fence(__ATOMIC_ACQUIRE, "wavefront");
  asm volatile("" ::: "memory");
}
DI void lds_st(LAS char* p, u32 v) { __hip_atomic_store((LAS u32*)p, v, __ATOMIC_RELAXED, __HIP_MEMORY_SCOPE_WORKGROUP); }
template <int N, int DELAY, int SLP = PSLP> DI void wait_flags(const Ctx& x, bool poller, const int (&off)[N], u32 gen) {
  if (poller) {
    if (DELAY) __builtin_amdgcn_s_sleep(DELAY);
    poll<N, SLP>(x, off);
    lds_st(x.lds + L_CTL + C_GO, gen);
  } else lds_wait_ge<GSLP>(x.lds + L_CTL + C_GO, gen);
}
#define LANDED(n) do { VMWAIT(n); lds_inc(x, C_LAND); } while (0)
DI void wait_landed(const Ctx& x, u32 n) { lds_wait_ge(x.lds + L_CTL + C_LAND, 4 * n); }

template <int GP> DI void group_dst(int gam, int h, int& tile, int& nb, bool& single) {
  int sup = gam / (2 * GP + 1), e = gam - sup * (2 * GP + 1);
  single = (e == GP);
  tile = 2 * sup + (e > GP ? 1 : (single ? h : 0));
  nb = single ? 2 * GP : 2 * (e > GP ? e - GP - 1 : e) + h;
}

DI v2u pack4(f32x4 a) { return v2u{pk(a[0], a[1]), pk(a[2], a[3])}; }
DI f32x4 unlo(v4u v) { return f32x4{blo(v.x), bhi(v.x), blo(v.y), bhi(v.y)}; }
DI f32x4 unhi(v4u v) { return f32x4{blo(v.z), bhi(v.z), blo(v.w), bhi(v.w)}; }
DI f32x4 un4(v2u v) { return f32x4{blo(v.x), bhi(v.x), blo(v.y), bhi(v.y)}; }
DI v4u pack8(const float (&v)[8]) { return v4u{pk(v[0], v[1]), pk(v[2], v[3]), pk(v[4], v[5]), pk(v[6], v[7])}; }
DI v4u pack8(f32x4 a, f32x4 b) { return v4u{pk(a[0], a[1]), pk(a[2], a[3]), pk(b[0], b[1]), pk(b[2], b[3])}; }
DI int bin_of(int pi) { return pi == 0 ? 160 : pi < 160 ? pi : 480 - pi; }
DI void gen_q(int kap, int g, int n0, v4u (&qc)[10], v4u (&qs)[10]) {
  const int k = bin_of(kap);
  const bool ny = kap == 160;
  const float amp = ny ? 0.03952847075f : 0.05590169944f;
  const float kf = (float)k * (1.f / 640.f), a0 = (float)(((n0 + 8 * g) * k) % 640) * (1.f / 640.f);
  const float rc = cosr(32.f * kf), rs = sinr(32.f * kf), c2 = rc + rc;
  f32x2 c0[4], s0[4], c1[4], s1[4];
#pragma unroll
  for (int j = 0; j < 4; ++j) {
    const float u = a0 + (float)(2 * j) * kf, v = u + kf;
    c0[j] = f32x2{cosr(u), cosr(v)} * amp;
    s0[j] = f32x2{sinr(u), sinr(v)} * (ny ? 0.f : -amp);
    c1[j] = c0[j] * rc + s0[j] * rs;
    s1[j] = s0[j] * rc - c0[j] * (ny ? 0.f : rs);
  }
  auto pk4 = [](const f32x2 (&v)[4]) { return v4u{pk(v[0][0], v[0][1]), pk(v[1][0], v[1][1]), pk(v[2][0], v[2][1]), pk(v[3][0], v[3][1])}; };
#pragma unroll
  for (int ks = 0; ks < 10; ks += 2) {
    qc[ks] = pk4(c0); qs[ks] = pk4(s0); qc[ks + 1] = pk4(c1); qs[ks + 1] = pk4(s1);
    if (ks + 2 < 10) {
#pragma unroll
      for (int j = 0; j < 4; ++j) {
        c0[j] = c2 * c1[j] - c0[j]; c1[j] = c2 * c0[j] - c1[j];
        s0[j] = c2 * s1[j] - s0[j]; s1[j] = c2 * s0[j] - s1[j];
      }
    }
  }
#pragma unroll
  for (int i = 0; i < 10; ++i) { PIN(qc[i]); PIN(qs[i]); }
}

template <int K0, int K1> DI void x_fetch(const Ctx& x, const u16* xin, int mm, int q4, int nt, int rg) {
  const int lam = 8 * rg + (x.lane >> 3);
  const int srow = 256 * mm + 32 * (4 * ((lam >> 4) & 1) + (lam & 3)) + 8 * q4 + 4 * (lam >> 5) + ((lam >> 2) & 3);
  const char* src = (const char*)xin + srow * 1280 + (((x.lane & 7) ^ (lam & 7)) << 4);
#pragma unroll
  for (int kc = K0; kc < K1; ++kc) {
    const int kr = kc + 2 * nt;
    dma16(src + (kr < 10 ? kr : kr - 10) * 128, (u32)(uintptr_t)x.lds + (kc * 8 + rg) * 1024);
  }
}
DI void phase_a(const Ctx& x, const u16* xin, int mm, int q4, int nt) {
  const int w = x.w, g = x.g, c = x.c;
  x_fetch<0, 4>(x, xin, mm, q4, nt, w);
  BAR();
  const int u = w >> 2;
  v4u qc[10], qs[10];
  gen_q(64 * nt + 16 * (w & 3) + c, g, 128 * nt, qc, qs);
  f32x4 acc[2][2][2];
#pragma unroll
  for (int i = 0; i < 8; ++i) acc[i >> 2][(i >> 1) & 1][i & 1] = f32x4{0.f, 0.f, 0.f, 0.f};
  LAS char* a0 = x.lds + u * 4096 + (c >> 3) * 1024 + (c & 7) * 128 + ((g ^ (c & 7)) << 4);
  LAS char* a1 = x.lds + u * 4096 + (c >> 3) * 1024 + (c & 7) * 128 + (((g + 4) ^ (c & 7)) << 4);
  v4u f[4];
  f32x4 r0, r1;
  u32 seen = 0;
  SCHED();
  STAMP(36);
  VMWAIT(0); soft_barrier(x, C_XL, 8); STAMP(1);
#pragma unroll
  for (int i = 0; i < 4; ++i) f[i] = *(LAS v4u*)((i < 2 ? a0 : a1) + (i & 1) * 2048);
#pragma unroll
  for (int kc = 0; kc < 10; ++kc) {
    if (kc == 2 || kc == 5) seen = *(volatile LAS u32*)(x.lds + L_CTL + C_XL + (kc == 2 ? 8 : 12));
    if (kc == 3 || kc == 6) lds_wait_ge_pre(x.lds + L_CTL + C_XL + (kc == 3 ? 8 : 12), 4, seen);
    if (kc == 4) { a0 = pinp(a0 + 40960); a1 = pinp(a1 + 40960); }
    if (kc == 8) seen = *(volatile LAS u32*)(x.lds + L_CTL + C_XL + 4);
    if (kc == 9) {
      lds_wait_ge_pre(x.lds + L_CTL + C_XL + 4, 4, seen);
      LAS char* rp = pinp(x.lds + L_MISC + 1024 + (32 * u + 4 * g) * 4);
      r0 = *(LAS f32x4*)rp; r1 = *(LAS f32x4*)(rp + 64);
    }
    SCHED();
#pragma unroll
    for (int i = 0; i < 4; ++i) {
      const int qi = (2 * kc + (i >> 1)) % 10;
      acc[kc / 5][i & 1][0] = mfma(f[i], qc[qi], acc[kc / 5][i & 1][0]);
      acc[kc / 5][i & 1][1] = mfma(f[i], qs[qi], acc[kc / 5][i & 1][1]);
      SCHED();
      if (kc < 9) f[i] = *(LAS v4u*)((i < 2 ? a0 : a1) + ((kc + 1) % 5) * 8192 + (i & 1) * 2048);
      SCHED();
    }
  }
  __builtin_amdgcn_s_setprio(2);
  STAMP(37);
  STAMPA(L_CTL + 256 + (x.w == 7 ? 58 * 4 : 512));
  const float sg = (c & 1) ? -1.f : 1.f;
  const int off = O_ZT + ((nt * 32 + mm * 4 + q4) * 32 + 8 * (w & 3) + (c >> 1)) * 512 + u * 256 + (c & 1) * 128 + g * 16;
#pragma unroll
  for (int i = 0; i < 2; ++i) st16(x.rs, pack8((acc[0][0][i] + sg * acc[1][0][i]) * r0, (acc[0][1][i] + sg * acc[1][1][i]) * r1), off, i * 64);
}

DI void phase_a_stats(const Ctx& x, const u16* xin, int mm, int q4, int nt) {
  const int lw = x.w & 3, g = x.g, c = x.c, rt = mm * 4 + q4;
  x_fetch<4, 7>(x, xin, mm, q4, nt, 2 * lw);
  x_fetch<4, 7>(x, xin, mm, q4, nt, 2 * lw + 1);
  x_fetch<7, 10>(x, xin, mm, q4, nt, 2 * lw);
  x_fetch<7, 10>(x, xin, mm, q4, nt, 2 * lw + 1);
  const v4u ones = {0x3f803f80u, 0x3f803f80u, 0x3f803f80u, 0x3f803f80u};
  f32x4 gr = {0.f, 0.f, 0.f, 0.f}, sm = gr;
  LAS char* a0 = x.lds + lw * 2048 + (c >> 3) * 1024 + (c & 7) * 128 + ((g ^ (c & 7)) << 4);
  LAS char* a1 = x.lds + lw * 2048 + (c >> 3) * 1024 + (c & 7) * 128 + (((g + 4) ^ (c & 7)) << 4);
#pragma unroll
  for (int st = 0; st < 3; ++st) {
    constexpr int k0[3] = {4, 7, 0}, k1[3] = {7, 10, 4};
    if (st == 0) { VMWAIT(6); lds_inc(x, C_XL + 8); }
    if (st == 1) { VMWAIT(0); lds_inc(x, C_XL + 12); STAMPL(60); }
    if (st == 2) lds_wait_ge<0>(x.lds + L_CTL + C_XL, 8);
    LAS char* p0 = pinp(a0 + k0[st] * 8192);
    LAS char* p1 = pinp(a1 + k0[st] * 8192);
    v4u f[8];
#pragma unroll
    for (int k = 0; k < k1[st] - k0[st]; ++k) { f[2 * k] = *(LAS v4u*)(p0 + k * 8192); f[2 * k + 1] = *(LAS v4u*)(p1 + k * 8192); }
#pragma unroll
    for (int k = 0; k < 2 * (k1[st] - k0[st]); ++k) { gr = mfma(f[k], f[k], gr); sm = mfma(ones, f[k], sm); }
    SCHED();
  }
  const int r = c & 3;
  const float mu = sm[0] * (1.f / 640.f);
  const float var = fmaxf((r == 0 ? gr[0] : r == 1 ? gr[1] : r == 2 ? gr[2] : gr[3]) * (1.f / 640.f) - mu * mu, 0.f) + EPS;
  const float rstd = __builtin_amdgcn_rsqf(var);
  if ((c >> 2) == g) {
    *(LAS float*)(x.lds + L_MISC + 1024 + (lw * 16 + c) * 4) = rstd;
    st4(x.rs, __builtin_bit_cast(u32, var * rstd), O_SIG + nt * 8192 + (rt * 64 + lw * 16 + c) * 4, 0);
    if (nt == 0) st4(x.rs, __builtin_bit_cast(u32, mu), O_MU + (rt * 64 + lw * 16 + c) * 4, 0);
  }
  asm volatile("s_waitcnt lgkmcnt(0)" ::: "memory");
  lds_inc(x, C_XL + 4);
}
DI int stat_at(int n1, int n2) {
  return ((n1 >> 3) * 4 + (n2 >> 3)) * 64 + (2 * ((n2 >> 2) & 1) + ((n1 >> 2) & 1)) * 16 + 4 * (n2 & 3) + (n1 & 3);
}

DI void wt_dma(const Ctx& x, const u16* wr, const u16* wi, int v, int lo) {
  const int fq = v >> 4, fh = v & 15, lw = x.w & 3;
#pragma unroll
  for (int i = 0; i < 2; ++i) {
    const char* src = (const char*)(i ? wi : wr) + (4 * fq + lw) * 41088 + ((fh * 2568) & ~15);
#pragma unroll
    for (int pc = 0; pc < 3; ++pc) dma16(src + min(pc * 64 + x.lane, 160) * 16, (u32)(uintptr_t)x.lds + lo + (lw * 2 + i) * 3072 + pc * 1024);
  }
}
DI void wt_out(const Ctx& x, int v, int lo) {
  const int k0 = 2 * ((x.lane >> 3) * 20 + (x.w & 3) * 5) + ((x.lane >> 2) & 1);
  LAS const u16* sp = (LAS const u16*)(x.lds + lo) + (v & 1) * 4 + (x.lane & 3) * 321;
#pragma unroll
  for (int it = 0; it < 5; ++it) {
    LAS const u16* q = sp + bin_of(k0 + 2 * it);
    v4u o;
#pragma unroll
    for (int r = 0; r < 4; ++r) o[r] = (u32)q[r * 3072] | ((u32)q[r * 3072 + 1536] << 16);
    st16(x.rs, o, O_WT + v * 20480 + ((x.w & 3) * 5 + it) * 1024 + x.lane * 16, 0);
  }
}
template <int GP> DI int blk_addr(const Ctx& x, int gam0, int rb, int& tile, int& nb) {
  const int p = x.c & 3, r0 = 8 * x.g + (x.c >> 2);
  bool single;
  group_dst<GP>(gam0 + (rb >> 1), rb & 1, tile, nb, single);
  const int ch = (rb >> 1) * 4 + (single ? 2 * (rb & 1) + (p >> 1) : p), hb = single ? (p & 1) : (rb & 1);
  return r0 * 128 + ((ch ^ (((r0 >> 1) & 1) << 2)) << 4) + hb * 8;
}
DI void tile_pos(int j, int& ks, int& ct) {
  const bool is3 = j < 540;
  const int jj = j - (is3 ? 0 : j < 1080 ? 540 : 1080);
  ks = (jj * (is3 ? 2428 : 6554)) >> 16;
  ct = jj - ks * (is3 ? 27 : 10);
}
DI void tile_dma(const Ctx& x, int j, int lds_off, const u16* w2, const u16* w3, const u16* w4) {
  const int row = x.lane >> 3, ch = ((x.lane & 7) ^ (((row >> 1) & 1) << 2)) << 4, rs = j < 540 ? 3456 : 1280;
  int ks, ct;
  tile_pos(j, ks, ct);
  const char* src = (const char*)(j < 540 ? w3 : j < 1080 ? w4 : w2) + (ks * 32 * rs + ct * 128) + (row * rs + ch);
#pragma unroll
  for (int i = 0; i < 4; ++i) dma16(src + i * 8 * rs, (u32)(uintptr_t)x.lds + lds_off + i * 1024);
}
struct Tile { v4u f[4], cs[4]; int dst[4], cso[4]; };
DI void tile_read(const Ctx& x, int j, bool live, int lds_off, Tile& t) {
  const bool is3 = j < 540;
  const int GP = is3 ? 3 : 2, tb = is3 ? 143360 : j < 1080 ? 276480 : 102400, base = is3 ? O_B6 : j < 1080 ? O_B7 : O_B5;
  int ks, ct;
  tile_pos(j, ks, ct);
  const int p = x.c & 3, r0 = 8 * x.g + (x.c >> 2), sw = ((r0 >> 1) & 1) << 6;
  LAS char* lp = x.lds + lds_off + r0 * 128;
  const v4u ones = {0x3f803f80u, 0x3f803f80u, 0x3f803f80u, 0x3f803f80u};
#pragma unroll
  for (int rb = 0; rb < 4; ++rb) {
    const int gam = 2 * ct + (rb >> 1), h = rb & 1, sup = (gam * (is3 ? 37 : 52)) >> 8, e = gam - sup * (2 * GP + 1);
    const bool single = e == GP, hi = e > GP;
    const int d = (2 * sup + (int)(hi || (single && h))) * tb + (ks * (2 * GP + 1) + (single ? 2 * GP : 2 * (e - (hi ? GP + 1 : 0)) + h)) * 1024;
    LAS char* ad = lp + (((rb >> 1) * 64 + (single ? h * 32 + p * 8 : p * 16 + h * 8)) ^ sw);
    const v2u lo = trread(ad), hi2 = trread(ad + 512);
    t.f[rb] = v4u{lo.x, lo.y, hi2.x, hi2.y};
    t.cs[rb] = __builtin_bit_cast(v4u, mfma(t.f[rb], ones, f32x4{0.f, 0.f, 0.f, 0.f}));
    t.dst[rb] = live ? base + d + x.lane * 16 : 0x7f000000;
    t.cso[rb] = (live && is3 && x.c == 0) ? O_CS3 + (d >> 4) + x.g * 16 : 0x7f000000;
    PIN(t.dst[rb]); PIN(t.cso[rb]);
  }
}
DI void tile_store(const Ctx& x, const Tile& t) {
#pragma unroll
  for (int rb = 0; rb < 4; ++rb) st16(x.rs, t.f[rb], t.dst[rb], 0);
#pragma unroll
  for (int rb = 0; rb < 4; ++rb) st16(x.rs, t.cs[rb], t.cso[rb], 0);
}
DI void side_w1p(const Ctx& x, const u16* w1, int ct, int nt) {
  {
    const int row = 8 * x.w + (x.lane >> 3);
    const char* src = (const char*)w1 + row * 1280 + ct * 128 + (((x.lane & 7) ^ (((row >> 1) & 1) << 2)) << 4);
#pragma unroll
    for (int i = 0; i < 10; ++i) {
      const int kr = i + 2 * nt;
      dma16(src + (kr < 10 ? kr : kr - 10) * 64 * 1280, (u32)(uintptr_t)x.lds + (i * 8 + x.w) * 1024);
    }
  }
  BAR();
  v4u qc[10], qs[10];
  gen_q(64 * nt + 16 * (x.w & 3) + x.c, x.g, 128 * nt, qc, qs);
  f32x4 acc[2][2][2];
#pragma unroll
  for (int i = 0; i < 8; ++i) acc[i >> 2][(i >> 1) & 1][i & 1] = f32x4{0.f, 0.f, 0.f, 0.f};
  int addr[2], tile[2], nb[2];
#pragma unroll
  for (int rb = 0; rb < 2; ++rb) addr[rb] = blk_addr<2>(x, 2 * ct, 2 * (x.w >> 2) + rb, tile[rb], nb[rb]);
  SCHED();
  VMWAIT(0);
  soft_barrier(x, C_SB, 8);
  STAMP(1);
  v2u lo[4], hi[4];
#pragma unroll
  for (int i = 0; i < 4; ++i) { lo[i] = trread(x.lds + addr[i & 1] + (i >> 1) * 4096); hi[i] = trread(x.lds + addr[i & 1] + (i >> 1) * 4096 + 512); }
#pragma unroll
  for (int kc = 0; kc < 10; ++kc) {
    if (kc == 4) { addr[0] += 40960; addr[1] += 40960; PIN(addr[0]); PIN(addr[1]); }
#pragma unroll
    for (int i = 0; i < 4; ++i) {
      const int qi = (2 * kc + (i >> 1)) % 10;
      const v4u f = {lo[i].x, lo[i].y, hi[i].x, hi[i].y};
      acc[kc / 5][i & 1][0] = mfma(qc[qi], f, acc[kc / 5][i & 1][0]);
      acc[kc / 5][i & 1][1] = mfma(qs[qi], f, acc[kc / 5][i & 1][1]);
      if (kc < 9) {
        LAS char* ad = x.lds + addr[i & 1] + ((kc + 1) % 5) * 8192 + (i >> 1) * 4096;
        lo[i] = trread(ad); hi[i] = trread(ad + 512);
      }
      SCHED();
    }
  }
  const f32x4 sg = {1.f, -1.f, 1.f, -1.f};
#pragma unroll
  for (int rb = 0; rb < 2; ++rb) {
    const f32x4 re = acc[0][rb][0] + sg * acc[1][rb][0], im = acc[0][rb][1] + sg * acc[1][rb][1];
    const u32 flip = 4 * nt + (x.w & 3) >= 10 ? 0x80000000u : 0u;
    st16(x.rs, v4u{pk(re[0], im[0]), pk(re[1], im[1]), pk(re[2], im[2]), pk(re[3], im[3])} ^ flip, x.lane * 16,
         O_BC + tile[rb] * 102400 + ((4 * nt + (x.w & 3)) * 5 + nb[rb]) * 1024);
  }
}
DI void side_jobs(const Ctx& x, int xcd, int sl, const u16* wr, const u16* wi, const u16* w1, const u16* w2, const u16* w3, const u16* w4) {
  const int y = sl < 2 ? sl * 8 + xcd : (sl < 7 && xcd >= 2) ? 16 + (sl - 2) * 6 + (xcd - 2) : -1;
  const int ct = sl >= 7 ? xcd : 8 + xcd, nt = sl >= 7 ? sl - 7 : sl - 2, xr = nt * 10 + ct;
  const int v = sl * 8 + xcd, j0 = y < 0 ? xr * 8 : min(400 + y * 15, 1065);
  if (y >= 0) {
    const int job = y * 3 + (x.w >> 2), lo = (x.w >> 2) * 24576;
    if (job < 128) wt_dma(x, wr, wi, job, lo);
    BAR();
    tile_dma(x, j0 + x.w, L_TJ + x.w * 4096, w2, w3, w4);
    VMWAIT(4);
    BAR();
    STAMP(1);
    if (job < 128) wt_out(x, job, lo);
    STAMP(5);
    publish<8, 46>(x, 0, F_W + y, 2, 12);
    if (x.w < 3) {
      lds_wait_ge(x.lds + L_CTL + C_PUB, 12);
      tile_dma(x, j0 + 12 + x.w, L_TJ + (12 + x.w) * 4096, w2, w3, w4);
      VMWAIT(0);
    }
  } else {
    if (x.w < 8) side_w1p(x, w1, ct, nt);
    else {
      BAR();
      __builtin_amdgcn_s_setprio(3);
      tile_dma(x, 1080 + xr * 4 + x.w - 8, L_TJ + x.w * 4096, w2, w3, w4);
#pragma unroll 1
      for (int k = x.w & 3; k < 8; k += 4) tile_dma(x, j0 + k, L_TJ + k * 4096, w2, w3, w4);
      VMWAIT(8);
      Tile t;
      tile_read(x, 1080 + xr * 4 + x.w - 8, true, L_TJ + x.w * 4096, t);
      tile_store(x, t);
    }
    STAMP(5);
    publish<8, 50>(x, 1, F_S + xr, 3, 12);
  }
  BAR();
  if (x.w >= 8) { BAR(); return; }
  Tile t0, t1;
  tile_read(x, j0 + x.w, true, L_TJ + x.w * 4096, t0);
  tile_read(x, j0 + 8 + x.w, y >= 0 && x.w < 7, L_TJ + (8 + x.w) * 4096, t1);
  BAR();
  STAMP(59);
  wait_flags<1, 0, 4>(x, x.w == 7, {F_X + v}, 1);
  STAMP(61);
  tile_store(x, t0);
  tile_store(x, t1);
  STAMP(62);
  publish<8, 96>(x, 6, F_T + v, 47);
}

#define BSLP 1
#define WEARLY 0
DI v4u negv(v4u a) { return a ^ 0x80008000u; }
constexpr int L_BK = 80 * 1024;
DI void phase_b_consts(const Ctx& x) {
  const int g = x.g, c = x.c, wq = x.w & 3;
  LAS char* dst = pinp(x.lds + L_BK + wq * 9216 + x.lane * 16);
  const float f1 = (float)(16 * wq + c);
  float cv[8], sv[8];
#pragma unroll
  for (int ks = 0; ks < 2; ++ks) {
#pragma unroll
    for (int j = 0; j < 8; ++j) { const float a = f1 * (float)(32 * ks + 8 * g + j) * (1.f / 64.f); cv[j] = cosr(a); sv[j] = sinr(a); }
    *(LAS v4u*)(dst + ks * 1024) = pack8(cv);
    *(LAS v4u*)(dst + 2048 + ks * 1024) = pack8(sv);
    f32x4 tc, ts;
#pragma unroll
    for (int r = 0; r < 4; ++r) {
      const float a = f1 * (float)(16 * ks + 4 * g + r) * (1.f / 2048.f);
      tc[r] = 0.02209708691f * cosr(a); ts[r] = 0.02209708691f * sinr(a);
    }
    *(LAS f32x4*)(dst + 4096 + ks * 1024) = tc;
    *(LAS f32x4*)(dst + 6144 + ks * 1024) = ts;
  }
#pragma unroll
  for (int j = 0; j < 8; ++j)
    cv[j] = cosr((float)(16 * (wq & 1) + c) * (float)(16 * (j >> 2) + 4 * g + (j & 3)) * (1.f / 32.f) - (wq >> 1) * 0.25f);
  *(LAS v4u*)(dst + 8192) = pack8(cv);
  asm volatile("s_waitcnt lgkmcnt(0)" ::: "memory");
  lds_inc(x, C_SB1);
}
DI void phase_b(const Ctx& x, int p) {
  const int w = x.w, lane = x.lane, g = x.g, c = x.c, wq = w & 3, col = w >> 2;
  v4u C64[2], S64[2], N64[2], C32[2], S32[2], N32[2];
  f32x4 tc[2], ts[2];
  lds_wait_ge(x.lds + L_CTL + C_SB1, 4);
  {
    LAS char* k0 = pinp(x.lds + L_BK + lane * 16);
    LAS char* kw = k0 + wq * 9216;
#pragma unroll
    for (int i = 0; i < 2; ++i) {
      C64[i] = *(LAS v4u*)(kw + i * 1024); S64[i] = *(LAS v4u*)(kw + 2048 + i * 1024); N64[i] = negv(S64[i]);
      tc[i] = *(LAS f32x4*)(kw + 4096 + i * 1024); ts[i] = *(LAS f32x4*)(kw + 6144 + i * 1024);
      C32[i] = *(LAS v4u*)(k0 + i * 9216 + 8192); S32[i] = *(LAS v4u*)(k0 + (2 + i) * 9216 + 8192); N32[i] = negv(S32[i]);
    }
  }
  const int nt = p >> 5;
#pragma unroll
  for (int i = 0; i < 2; ++i) { PIN(C64[i]); PIN(S64[i]); PIN(N64[i]); PIN(C32[i]); PIN(S32[i]); PIN(N32[i]); PIN(tc[i]); PIN(ts[i]); }
  STAMP(4);
  int wo = O_WT + (g * 16 + 4 * wq + (c >> 2)) * 20480 + (p % 20) * 1024 + (p / 20) * 128 + col * 64 + (c & 3) * 16;
  int zo = O_ZT + ((nt * 32 + 4 * w + (lane >> 5)) * 32 + (p & 31)) * 512 + (lane & 31) * 16;
  LAS char* xd = pinp(x.lds + L_XS + ((lane >> 2) & 3) * 4096 + (w >> 2) * 1024 + ((w & 3) * 16 + 4 * (lane >> 4) + (lane & 3)) * 16);
  LAS char* xs = pinp(x.lds + L_XS + col * 8192 + lane * 16);
  int so = O_SIG + nt * 8192 + stat_at(16 * wq + 4 * g, 16 * col + c) * 4;
  LAS char* zp = pinp(x.lds + L_XS + (col * 2 + (wq >> 1)) * 1024 + ((2 * (wq & 1) + (g >> 1)) * 16 + c) * 16 + (g & 1) * 8);
  LAS char* img = pinp(x.lds + L_XB + col * 9216 + (16 * wq + c) * 72 + g * 8);
  LAS char* rd = pinp(x.lds + L_XB + (8 * g + (c >> 2)) * 72 + col * 32 + (c & 3) * 8);
  int ob = O_HB + p * 16384 + ((16 * wq + 4 * g) * 32 + 16 * col + c) * 8;
  PIN(ob); PIN(zo); PIN(wo); PIN(so);
  const u32 keep = p != 80 ? ~0u : 0xffffu;
  const u32 flip = p >= 80 ? 0x80000000u : 0u;
  v4u wv[2];
  lds_wait_ge(x.lds + L_CTL + C_GO, 2);
  STAMP(6);
  const v4u l0 = ld16(x.rs, zo, 0), l1 = ld16(x.rs, zo, 32768);
  *(LAS v4u*)xd = l0;
  *(LAS v4u*)(xd + 2048) = l1;
  asm volatile("s_waitcnt lgkmcnt(0)" ::: "memory");
  lds_inc(x, C_SB0);
  lds_wait_ge<0>(x.lds + L_CTL + C_GOW, 1);
  STAMP(5);
#pragma unroll
  for (int t = 0; t < 2; ++t) wv[t] = ld16(x.rs, wo, t * 64 * 20480);
  const f32x4 sg = __builtin_bit_cast(f32x4, ld16(x.rs, so, 0));
  SCHED();
  lds_wait_ge<0>(x.lds + L_CTL + C_SB0, 8);
  STAMP(50);
  v4u xr[2][2], xi[2][2];
#pragma unroll
  for (int i = 0; i < 4; ++i) {
    xr[i >> 1][i & 1] = *(LAS v4u*)(xs + i * 1024);
    xi[i >> 1][i & 1] = *(LAS v4u*)(xs + 4096 + i * 1024);
  }
  v2u z[4];
#pragma unroll
  for (int i = 0; i < 4; ++i) z[i] = *(LAS v2u*)(zp + i * 4096);
  SCHED();
  const f32x4 zero = {0.f, 0.f, 0.f, 0.f};
  f32x4 ar[2], ai[2], br[2], bi[2];
#pragma unroll
  for (int b = 0; b < 2; ++b) {
    ar[b] = mfma(xr[b][0], C64[0], zero); ai[b] = mfma(xi[b][0], C64[0], zero);
    ar[b] = mfma(xi[b][0], S64[0], ar[b]); ai[b] = mfma(xr[b][0], N64[0], ai[b]);
    ar[b] = mfma(xr[b][1], C64[1], ar[b]); ai[b] = mfma(xi[b][1], C64[1], ai[b]);
    ar[b] = mfma(xi[b][1], S64[1], ar[b]); ai[b] = mfma(xr[b][1], N64[1], ai[b]);
  }
  SCHED();
#pragma unroll
  for (int b = 0; b < 2; ++b) { br[b] = ar[b] * tc[b] + ai[b] * ts[b]; bi[b] = ai[b] * tc[b] - ar[b] * ts[b]; }
  v4u pr = pack8(br[0], br[1]), pi = pack8(bi[0], bi[1]);
  SCHED();
#pragma unroll
  for (int t = 0; t < 2; ++t) {
    ar[t] = mfma(C32[t], pr, zero); ai[t] = mfma(C32[t], pi, zero);
    ar[t] = mfma(S32[t], pi, ar[t]); ai[t] = mfma(N32[t], pr, ai[t]);
  }
  f32x4 wre[2], wim[2];
#pragma unroll
  for (int t = 0; t < 2; ++t)
#pragma unroll
    for (int r = 0; r < 4; ++r) { wre[t][r] = blo(wv[t][r]); wim[t][r] = bhi(wv[t][r]); }
  SCHED();
#pragma unroll
  for (int t = 0; t < 2; ++t) { br[t] = ar[t] * wre[t] - ai[t] * wim[t]; bi[t] = ar[t] * wim[t] + ai[t] * wre[t]; }
  pr = pack8(br[0], br[1]); pi = pack8(bi[0], bi[1]);
  SCHED();
#pragma unroll
  for (int t = 0; t < 2; ++t) {
    ar[t] = mfma(C32[t], pr, zero); ai[t] = mfma(C32[t], pi, zero);
    ar[t] = mfma(N32[t], pi, ar[t]); ai[t] = mfma(S32[t], pr, ai[t]);
  }
  SCHED();
#pragma unroll
  for (int t = 0; t < 2; ++t) {
    const f32x4 ur = ar[t] * tc[t] - ai[t] * ts[t], ui = ai[t] * tc[t] + ar[t] * ts[t];
    *(LAS v2u*)(img + t * 32) = pack4(ur);
    *(LAS v2u*)(img + 4608 + t * 32) = pack4(ui);
  }
  STAMP(38);
  asm volatile("s_waitcnt lgkmcnt(0)" ::: "memory");
  lds_inc(x, C_SB);
#pragma unroll
  for (int cc = 0; cc < 2; ++cc) { ar[cc] = sg * un4(z[2 * cc]); ai[cc] = sg * un4(z[2 * cc + 1]); PIN(ar[cc]); PIN(ai[cc]); }
  lds_wait_ge<BSLP>(x.lds + L_CTL + C_SB, 8);
  STAMP(39);
  v4u ur[2][2], ui[2][2];
#pragma unroll
  for (int i = 0; i < 4; ++i) {
    LAS char* q = rd + (i >> 1) * 9216 + (i & 1) * 2304;
    const v2u r0 = trread(q), r1 = trread(q + 288), i0 = trread(q + 4608), i1 = trread(q + 4608 + 288);
    ur[i >> 1][i & 1] = v4u{r0.x, r0.y, r1.x, r1.y};
    ui[i >> 1][i & 1] = v4u{i0.x, i0.y, i1.x, i1.y};
  }
  SCHED();
#pragma unroll
  for (int cc = 0; cc < 2; ++cc)
#pragma unroll
    for (int ks = 0; ks < 2; ++ks) {
      ar[cc] = mfma(C64[ks], ur[cc][ks], ar[cc]); ai[cc] = mfma(C64[ks], ui[cc][ks], ai[cc]);
      ar[cc] = mfma(N64[ks], ui[cc][ks], ar[cc]); ai[cc] = mfma(S64[ks], ur[cc][ks], ai[cc]);
    }
#pragma unroll
  for (int r = 0; r < 4; ++r) st8(x.rs, v2u{(pk(ar[0][r], ai[0][r]) ^ flip) & keep, pk(ar[1][r], ai[1][r]) ^ flip}, ob + r * 256, 0);
}

#define DLY 6
#define B5W 1
#define BCW C_SB0
#define BDLY 1
#define GWAIT 12
#define DLYA 6
#define DLYB 10
#define ABL 0
#define HOLD 4
#define LA3 4
#define PUBIT -1
#define PEEK 9
#define PWAVE 0
#define LA5 5
#define LA6 2
#define LA7 2
#define PACE 10
#define STR2(x) #x
#define STR(x) STR2(x)
DI void dma_lin(const Ctx& x, int ws_off, int lds_off, int p0, int p1) {
  const char* src = x.ws + ws_off + x.lane * 16;
#pragma unroll 1
  for (int pc = p0 + (x.w & 3); pc < p1; pc += 4) {
    dma16_sc1(src + pc * 1024, (u32)(uintptr_t)x.lds + lds_off + pc * 1024);
    asm volatile("s_waitcnt vmcnt(" STR(PACE) ")" ::: "memory");
  }
}
DI int ft_pair(int rowblk, int KS, int gam, int g, int c) { return (rowblk * KS + gam) * 1024 + (g * 16 + c) * 16; }
DI int ft_single(int rowblk, int KS, int gam, int h, int g, int c) {
  return (rowblk * KS + gam) * 1024 + ((2 * h + (g >> 1)) * 16 + c) * 16 + (g & 1) * 8;
}

template <int PH, int NB, class F>
DI void gemm(const Ctx& x, int ao0, int ao1, f32x4 (&o)[NB], float& ssq, F pre) {
  constexpr int KW = (PH & 1) ? 4 : 2, NIT = PH == 5 ? 5 : PH == 7 ? 14 : 10, LA = PH == 6 ? LA6 : LA7;
  constexpr int AS = KW * 1024, BS = (PH == 5 ? 1 : KW) * NB * 1024, PER = PH == 7 ? 3 : 4;
  constexpr int EXO = PH == 4 ? 100 * 1024 : PH == 5 ? 92 * 1024 : 0, EXW = PH == 4 ? 6 : PH == 6 ? 7 : 8;
  const int kw = KW == 4 ? x.w >> 1 : x.w >> 2;
  v4u a[NIT][2];
  auto lda = [&](int it, int i) { a[it][i] = ld16(x.rs, i ? ao1 : ao0, it * AS); };
  auto fix = [&](int it) {
    if (PH == 4) {
      typedef u64 v2l __attribute__((ext_vector_type(2)));
      v2l p0 = __builtin_bit_cast(v2l, a[it][0]), p1 = __builtin_bit_cast(v2l, a[it][1]);
      u64 tmp;
      asm volatile("v_mov_b64 %2, %0\n\tv_mov_b64 %0, %1\n\tv_mov_b64 %1, %2\n\ts_nop 1" : "+v"(p0.y), "+v"(p1.x), "=&v"(tmp));
      a[it][0] = __builtin_bit_cast(v4u, p0); a[it][1] = __builtin_bit_cast(v4u, p1);
    }
  };
#pragma unroll
  for (int it = 0; it < LA; ++it) { lda(it, 0); lda(it, 1); }
  SCHED();
  wait_landed(x, PH - 3);
  STAMP(22 + PH);
  f32x4 acc[2][NB], gr[2];
#pragma unroll
  for (int i = 0; i < 2; ++i) {
    gr[i] = f32x4{0.f, 0.f, 0.f, 0.f};
#pragma unroll
    for (int n = 0; n < NB; ++n) acc[i][n] = gr[i];
  }
  LAS char* bs = x.lds + kw * (PH == 5 ? 5 : 1) * NB * 1024 + x.lane * 16;
  LAS char* bq[3] = {bs + (kw >> 1) * 2048, bs + ((kw >> 1) ^ 1) * 2048, bs + 4096};
  auto frag = [&](int n, int off) { return *(LAS v4u*)((KW == 2 ? bs + n * 1024 : bq[n >> 1] + (n & 1) * 1024) + off); };
  v4u f[NB];
#pragma unroll
  for (int n = 0; n < NB; ++n) f[n] = frag(n, 0);
  pre(acc[0]);
  fix(0);
  LAS char* ring = x.lds + L_CTL + C_RL;
  u32 seen = 0;
#pragma unroll
  for (int it = 0; it < NIT - 1; ++it) {
    SCHED();
    if (PH == 7 && it >= 6) lds_wait_ge_pre(ring + 4 * (it - 6), 4, seen);
    const int st = PH == 7 ? (it + 1) % 7 : it + 1;
    if (PH != 5 && st % PER == 0) {
      const int d = st ? PER * BS : -2 * PER * BS;
      if (KW == 2) bs = pinp(bs + d);
      else {
#pragma unroll
        for (int n = 0; n < 3; ++n) bq[n] = pinp(bq[n] + d);
      }
    }
#pragma unroll
    for (int n = 0; n < NB; ++n) {
      acc[0][n] = mfma(f[n], a[it][0], acc[0][n]);
      acc[1][n] = mfma(f[n], a[it][1], acc[1][n]);
      SCHED();
      f[n] = frag(n, (PH == 5 ? st : st % PER) * BS);
      if (n < 2 && it + LA < NIT) lda(it + LA, n);
      if (PH == 7 && n == 1 && it >= 5 && it < 12) seen = *(volatile LAS u32*)(ring + 4 * (it - 5));
      if (PH >= 6 && n == 1 && it == NIT - 2) seen = *(volatile LAS u32*)(x.lds + L_CTL + C_MID + (PH - 6) * 4);
      if (n == NB - 1) {
        if (PH == 4) { gr[0] = mfma(a[it][0], a[it][0], gr[0]); gr[1] = mfma(a[it][1], a[it][1], gr[1]); SCHED(); }
        fix(it + 1);
      }
      SCHED();
    }
    if (PH == 7 && it < 7) lds_inc(x, C_RING + 4 * it);
    if ((PH == 6 && it == 3) || (PH == 7 && it == 10)) lds_inc(x, C_MID + (PH - 6) * 4);
  }
  if (PH != 4) lds_wait_ge_pre(x.lds + L_CTL + C_MID + (PH - 6) * 4, 8, seen);
  LAS char* mine = pinp(x.lds + EXO + x.w * EXW * 1024 + x.lane * 16);
  const bool live = PH != 7 || kw < 2;
  if (PH == 4) { gr[1] = mfma(a[NIT - 1][1], a[NIT - 1][1], gr[1]); gr[0] = mfma(a[NIT - 1][0], a[NIT - 1][0], gr[0]); }
  if (live) {
#pragma unroll
    for (int n = 0; n < NB; ++n) acc[1][n] = mfma(f[n], a[NIT - 1][1], acc[1][n]);
#pragma unroll
    for (int n = NB - 1; n >= 0; --n) acc[0][n] = mfma(f[n], a[NIT - 1][0], acc[0][n]);
  }
  STAMP(PH == 4 ? 12 : PH == 5 ? 16 : PH == 6 ? 20 : 24);
#pragma unroll
  for (int n = 0; n < NB; ++n) *(LAS f32x4*)(mine + n * 1024) = acc[1][n];
  float s0 = 0.f;
  if (PH == 4) {
    const int r = x.c & 3;
    const bool dg = (x.c >> 2) == x.g;
    *(LAS float*)(mine + NB * 1024) = dg ? (r == 0 ? gr[1][0] : r == 1 ? gr[1][1] : r == 2 ? gr[1][2] : gr[1][3]) : 0.f;
    s0 = dg ? (r == 0 ? gr[0][0] : r == 1 ? gr[0][1] : r == 2 ? gr[0][2] : gr[0][3]) : 0.f;
  }
  if (KW == 4) {
#pragma unroll
    for (int n = 4; n >= 2; --n) *(LAS f32x4*)(mine + (3 + n) * 1024) = acc[0][n];
  }
  asm volatile("s_waitcnt lgkmcnt(0)" ::: "memory");
  lds_st(x.lds + L_CTL + C_EX + x.w * 4, PH);
  if (PH != 7) lds_inc(x, C_FREE + 4 * (PH - 4));
  lds_wait_ge<0>(x.lds + L_CTL + C_EX + (KW == 2 ? x.w ^ 4 : (x.w & 1) + 2 * (x.lane & 3)) * 4, PH);
  STAMP(28 + PH);
  if (KW == 2) {
    LAS char* other = pinp(x.lds + EXO + (x.w ^ 4) * EXW * 1024 + x.lane * 16);
    if (PH == 4) ssq = xsum16_32(s0 + *(LAS float*)(other + NB * 1024));
#pragma unroll
    for (int n = 0; n < NB; ++n) o[n] = acc[0][n] + *(LAS f32x4*)(other + n * 1024);
  } else {
    o[0] = acc[0][0]; o[1] = acc[0][1]; o[4] = acc[0][4];
#pragma unroll
    for (int m = 1; m < 4; ++m) {
      LAS char* s = pinp(x.lds + EXO + (x.w ^ (2 * m)) * EXW * 1024 + x.lane * 16);
      const int b0 = m == 1 ? 0 : m == 3 ? 2 : 5;
      o[0] += *(LAS f32x4*)(s + b0 * 1024);
      o[1] += *(LAS f32x4*)(s + (b0 + 1) * 1024);
      o[4] += *(LAS f32x4*)(s + (m == 2 ? 7 : 4) * 1024);
    }
  }
  if (PH == 5 || PH == 6) { asm volatile("s_waitcnt lgkmcnt(0)" ::: "memory"); lds_inc(x, C_EXD + (PH - 5) * 4); }
}

template <int PH, class L> DI void gemm16(const Ctx& x, v4u (&a)[10], L lda, f32x4 (&acc)[5], float& ssq) {
  constexpr int LA = PH == 4 ? 10 : PH == 3 ? LA3 : LA5, EXO = PH == 3 ? 0 : PH == 4 ? L_Q : 136 * 1024;
  constexpr int MID = PH == 5 ? C_QF : C_MID + (PH - 1) * 4;
  const int kw = x.w >> 2;
  if (PH == 3) {
#pragma unroll
    for (int it = 0; it < LA; ++it) lda(it);
  }
  SCHED();
  if (PH == 4) lds_wait_ge(x.lds + L_CTL + C_QOK, 4);
  else wait_landed(x, PH == 3 ? 1 : 2);
  STAMP(PH == 3 ? 26 : PH == 4 ? 30 : 27);
  LAS char* bs = x.lds + (PH == 4 ? L_Q : kw * 51200) + x.lane * 16;
  LAS char* bq[3] = {pinp(bs + kw * 2048), pinp(bs + (kw ^ 1) * 2048), pinp(bs + 4096)};
  v4u f[2][5];
#pragma unroll
  for (int d = 0; d < 2; ++d)
#pragma unroll
    for (int n = 0; n < 5; ++n) f[d][n] = *(LAS v4u*)(bq[n >> 1] + (n & 1) * 1024 + d * 5120);
  if (PH == 4) {
#pragma unroll
    for (int n = 0; n < 5; ++n) f[0][n].x = (kw && x.g == 0) ? 0x3d22u : f[0][n].x;
  }
  f32x4 gr = {0.f, 0.f, 0.f, 0.f};
  u32 seen = 0;
#pragma unroll
  for (int it = 0; it < 10; ++it) {
    if (it == 9) lds_wait_ge_pre(x.lds + L_CTL + MID, 8, seen);
#pragma unroll
    for (int m = 0; m < 5; ++m) {
      const int n = it == 9 ? 4 - m : m;
      SCHED();
      if (PH == 3 && m == 0) gr = mfma(a[it], a[it], gr);
      acc[n] = mfma(f[it & 1][n], a[it], acc[n]);
      SCHED();
      if (it + 2 < 10) f[it & 1][n] = *(LAS v4u*)(bq[n >> 1] + (n & 1) * 1024 + (it + 2) * 5120);
      if (m == 0 && it + LA < 10) lda(it + LA);
      if (it == 8 && m == 0) seen = *(volatile LAS u32*)(x.lds + L_CTL + MID);
      if (PH == 4 && it == PEEK && m == 1) lda(0);
    }
    if (PH != 5 && it == 3) lds_inc(x, MID);
    if (PH == 4 && it == PUBIT) lda(2);
  }
  STAMP(PH == 3 ? 12 : PH == 4 ? 31 : 16);
  if (PH == 4 && kw) {
#pragma unroll
    for (int n = 0; n < 5; ++n) acc[n] *= f32x4{1.f, -1.f, 1.f, -1.f};
  }
  LAS char* ex = pinp(x.lds + EXO + (x.w & 3) * 5120 + x.lane * 16);
  LAS char* mine = ex + kw * 2048;
  float s0 = 0.f;
  if (PH == 3) {
    const int r = x.c & 3;
    s0 = (x.c >> 2) == x.g ? (r == 0 ? gr[0] : r == 1 ? gr[1] : r == 2 ? gr[2] : gr[3]) : 0.f;
    *(LAS float*)(x.lds + L_SQ + x.w * 256 + x.lane * 4) = s0;
  }
  if (kw) *(LAS f32x4*)(ex + 4096) = acc[4];
  *(LAS f32x4*)(mine + 1024) = acc[3];
  *(LAS f32x4*)mine = acc[2];
  asm volatile("s_waitcnt lgkmcnt(0)" ::: "memory");
  lds_st(x.lds + L_CTL + C_EX + x.w * 4, PH);
  lds_inc(x, PH == 3 ? C_FREE : PH == 4 ? C_QF : C_FREE + 4);
  if (PH == 4) lda(1);
  lds_wait_ge<0>(x.lds + L_CTL + C_EX + (x.w ^ 4) * 4, PH);
  STAMP(PH == 3 ? 32 : PH == 4 ? 10 : 33);
  LAS char* other = ex + (kw ^ 1) * 2048;
  if (PH == 3) ssq = xsum16_32(s0 + *(LAS float*)(x.lds + L_SQ + (x.w ^ 4) * 256 + x.lane * 4));
  acc[0] += *(LAS f32x4*)other;
  acc[1] += *(LAS f32x4*)(other + 1024);
  acc[4] += *(LAS f32x4*)(ex + 4096);
  if (PH != 4) {
    asm volatile("s_waitcnt lgkmcnt(0)" ::: "memory");
    lds_inc(x, PH == 3 ? C_EXD + 8 : C_EXD);
  }
}
DI void gen_q_tile(const Ctx& x, int t8) {
#pragma unroll 1
  for (int u = x.w & 3; u < 10; u += 4) {
    const int nb = u >> 1, k0 = 80 * (u & 1) + 4 * x.g;
    const int n = (t8 >> 1) * 160 + (nb == 4 ? 64 + (t8 & 1) * 16 + x.c : (t8 & 1) * 96 + (nb >> 1) * 32 + 8 * (x.c >> 2) + 4 * (nb & 1) + (x.c & 3));
    const float da = (float)n * (1.f / 640.f), a0 = __builtin_amdgcn_fractf((float)(n * k0) * (1.f / 640.f));
    const float rc = cosr(16.f * da), rs = sinr(16.f * da), c2 = rc + rc;
    LAS char* dst = pinp(x.lds + L_Q + ((u & 1) * 25 + nb) * 1024 + x.lane * 16);
    f32x2 v0[4], v1[4];
#pragma unroll
    for (int i = 0; i < 4; ++i) {
      const float a = a0 + (float)i * da;
      v0[i] = f32x2{cosr(a), sinr(a)} * f32x2{0.05590169944f, -0.05590169944f};
      v1[i] = v0[i] * rc + v0[i].yx * f32x2{rs, -rs};
    }
#pragma unroll
    for (int ks = 0; ks < 5; ++ks) {
      v4u f = {pk(v0[0][0], v0[0][1]), pk(v0[1][0], v0[1][1]), pk(v0[2][0], v0[2][1]), pk(v0[3][0], v0[3][1])};
      if (ks == 0) f.x = k0 ? f.x : (n & 1) ? ((n & 2) ? 0x3d650000u : 0xbd650000u) : ((n & 2) ? 0xbd65u : 0x3d65u);
      *(LAS v4u*)(dst + ks * 5120) = f;
#pragma unroll
      for (int i = 0; i < 4; ++i) { const f32x2 t = c2 * v1[i] - v0[i]; v0[i] = v1[i]; v1[i] = t; }
    }
  }
  asm volatile("s_waitcnt lgkmcnt(0)" ::: "memory");
  lds_inc(x, C_QOK);
}

template <class W> DI void phase_c5(Ctx& x, int grp, int t8, u32 (&res)[6], W wait) {
  const int kw = x.w >> 2;
  const int rowblk = 4 * grp + 2 * (x.w & 1) + ((x.w >> 1) & 1);
  const int gp = (t8 >> 1) * 5 + (t8 & 1) * 3 + kw, gs = (t8 >> 1) * 5 + 2;
  int op = ft_pair(rowblk, 20, gp, x.g, x.c), os = ft_single(rowblk, 20, gs, t8 & 1, x.g, x.c);
  int mo = O_MU + stat_at(rowblk >> 1, 16 * (rowblk & 1) + x.c) * 4;
  int ho = O_HB + (80 * kw + 2 * x.g) * 16384 + (rowblk * 16 + x.c) * 8;
  PIN(op); PIN(os); PIN(mo); PIN(ho);
  wait();
  const f32x4 zero = {0.f, 0.f, 0.f, 0.f};
  v4u a[10];
  f32x4 o[5] = {zero, zero, zero, zero, zero};
  float ssq, mu;
  gemm16<3>(x, a, [&](int it) {
    const v2u lo = ld8(x.rs, ho, it * 131072), hi = ld8(x.rs, ho, it * 131072 + 16384);
    a[it] = v4u{lo.x, lo.y, hi.x, hi.y};
    if (it == 9) mu = __builtin_bit_cast(float, ld4(x.rs, mo, 0));
  }, o, ssq);
  const float vh = ssq * (1.f / 640.f);
  float sc = __builtin_amdgcn_rsqf(vh + EPS);
  {
    const float v2 = vh * sc * sc + EPS, s2 = __builtin_amdgcn_rsqf(v2);
    const f32x3 ge = gelu_k(sc * s2, (vh + EPS) * sc * v2 * s2);
    st16(x.rs, gelu(ge, o[0], o[1]), O_G + op, 0);
    if (kw == 0) st8(x.rs, gelu(ge, o[4]), O_G + os, 0);
  }
  PIN(sc); PIN(mu);
  STAMP(13);
  publish(x, 2, F_C + grp * 8 + t8, 14);
  int ao[2];
#pragma unroll
  for (int i = 0; i < 2; ++i) {
    const int r = rowblk * 16 + x.c - (i ? -1 - kw : 1 + kw);
    ao[i] = (u32)r < 2048u ? O_G + ft_pair(r >> 4, 20, 10 * kw, x.g, r & 15) : 0x7f000000;
  }
  const int fl = F_C + min(max(grp - 1 + min(x.lane >> 3, 2), 0), 31) * 8 + (x.lane & 7);
  int fo = x.w == PWAVE ? flag_at(fl) : 0x7f000000;
  PIN(ao[0]); PIN(ao[1]); PIN(fo);
  auto lda5 = [&](int it) { a[it] = ld16(x.rs, it >= 5 ? ao[1] : ao[0], it * 1024); };
#pragma unroll
  for (int n = 0; n < 5; ++n) o[n] = zero;
  v2u peek;
  gemm16<4>(x, a, [&](int step) {
    if (step == 0) { peek = ld8(x.rs, fo, 0); return; }
    if (step == 2) { publish_inline(x, 2, F_C + grp * 8 + t8); STAMPW(14); return; }
    if (x.w == PWAVE) {
      if (!__all(__builtin_bit_cast(u64, peek) == x.tag())) poll<1>(x, {fl});
      __builtin_amdgcn_fence(__ATOMIC_ACQUIRE, "wavefront");
      lds_st(x.lds + L_CTL + C_GO, 4);
    } else lds_wait_ge<GSLP>(x.lds + L_CTL + C_GO, 4);
    STAMP(15);
#pragma unroll
    for (int it = 0; it < LA5; ++it) lda5(it);
    SCHED();
  }, o, ssq);
  {
    const v4u hp = pack8(o[0] + mu, o[1] + mu);
    const v2u hq = pack4(o[4] + mu);
    res[0] = hp.x; res[1] = hp.y; res[2] = hp.z; res[3] = hp.w; res[4] = hq.x; res[5] = hq.y;
  }
  o[0] *= sc; o[1] *= sc; o[2] = zero; o[3] = zero; o[4] *= kw ? 0.f : sc;
#pragma unroll
  for (int i = 0; i < 6; ++i) PIN(res[i]);
  gemm16<5>(x, a, lda5, o, ssq);
  const v4u r = pack8(o[0], o[1]);
  st16(x.rs, r, O_X1 + op, 0);
  const v4u ones = {0x3f803f80u, 0x3f803f80u, 0x3f803f80u, 0x3f803f80u};
  float s1 = dot8(r, ones, 0.f), s2 = dot8(r, r, 0.f);
  if (kw == 0) {
    const v2u q = pack4(o[4]);
    st8(x.rs, q, O_X1 + os, 0);
    s1 = dot2(q.y, ones.x, dot2(q.x, ones.x, s1));
    s2 = dot2(q.y, q.y, dot2(q.x, q.x, s2));
  }
  s1 = xsum16_32(s1); s2 = xsum16_32(s2);
  if (x.g == 0) st8(x.rs, v2u{__builtin_bit_cast(u32, s1), __builtin_bit_cast(u32, s2)}, O_ST1 + (rowblk * 16 + t8 * 2 + kw) * 128 + x.c * 8, 0);
  const v4u rp = pack8(o[0] + un4(v2u{res[0], res[1]}), o[1] + un4(v2u{res[2], res[3]}));
  const v2u rq = pack4(o[4] + un4(v2u{res[4], res[5]}));
  res[0] = rp.x; res[1] = rp.y; res[2] = rp.z; res[3] = rp.w; res[4] = rq.x; res[5] = rq.y;
#pragma unroll
  for (int i = 0; i < 6; ++i) PIN(res[i]);
}

template <class W> DI void phase_6(const Ctx& x, int P, int t, W wait) {
  const int g = x.g, c = x.c, kw = x.w >> 2, rowblk = 8 * P + 2 * (x.w & 3) + kw;
  int so = O_ST1 + rowblk * 2048 + x.lane * 8, ao0 = O_X1 + (rowblk * 20 + kw) * 1024 + x.lane * 16, ao1 = O_X1 + ((rowblk ^ 1) * 20 + kw) * 1024 + x.lane * 16;
  PIN(so); PIN(ao0); PIN(ao1);
  wait();
  f32x2 p[4];
#pragma unroll
  for (int i = 0; i < 4; ++i) p[i] = __builtin_bit_cast(f32x2, ld8(x.rs, so + i * 512, 0));
  f32x4 acc[7];
  float dummy, var;
  gemm<6, 7>(x, ao0, ao1, acc, dummy, [&](f32x4 (&a0)[7]) {
    const f32x2 ps = (p[0] + p[1]) + (p[2] + p[3]);
    const float m1 = xsum16_32(ps[0]) * (1.f / 640.f), q1 = xsum16_32(ps[1]) * (1.f / 640.f);
    var = fmaxf(q1 - m1 * m1, 0.f) + EPS;
    PIN(var);
    LAS f32x4* cs = (LAS f32x4*)pinp(x.lds + L_MISC + g * 16);
#pragma unroll
    for (int n = 0; n < 7; ++n) a0[n] = cs[4 * n] * -m1;
  });
  const float r1 = __builtin_amdgcn_rsqf(var);
  const f32x3 ge = gelu_k(r1, var * r1);
  const int g0 = (t >> 1) * 7 + (t & 1) * 4, gs = (t >> 1) * 7 + 3, op = O_U + ft_pair(rowblk, 54, g0, g, c);
  st16(x.rs, gelu(ge, acc[0], acc[1]), op, 0);
  st8(x.rs, gelu(ge, acc[6]), O_U + ft_single(rowblk, 54, gs, t & 1, g, c), 0);
  if (t < 15) {
    st16(x.rs, gelu(ge, acc[2], acc[3]), op, 1024);
    st16(x.rs, gelu(ge, acc[4], acc[5]), op, 2048);
  }
}
DI void colsum_6(const Ctx& x, int t) {
  if (x.lane < 56) {
    f32x2 s = {0.f, 0.f};
#pragma unroll
    for (int k = 0; k < 20; ++k) s += __builtin_bit_cast(f32x2, ld8(x.rs, O_CS3 + t * 8960 + x.lane * 8 + k * 448, 0));
    *(LAS f32x2*)(x.lds + L_MISC + x.lane * 8) = s;
  }
}

template <class W> DI void phase_7(const Ctx& x, int P, int hf, int t8, const u32 (&res)[6], u16* out, W wait) {
  const int kw = x.w >> 1, g = x.g, c = x.c;
  const int rowblk = 8 * P + 4 * hf + 2 * (x.w & 1) + (kw & 1);
  const int gp = (t8 >> 1) * 5 + (t8 & 1) * 3 + (kw >> 1), gs = (t8 >> 1) * 5 + 2;
  int ao0 = O_U + (rowblk * 54 + kw) * 1024 + x.lane * 16, ao1 = O_U + ((rowblk ^ 1) * 54 + kw) * 1024 + x.lane * 16;
  PIN(ao0); PIN(ao1);
  wait();
  f32x4 o[5];
  float dummy;
  gemm<7, 5>(x, ao0, ao1, o, dummy, [](f32x4 (&)[5]) {});
  char* orow = (char*)out + (rowblk * 16 + c) * 1280;
  *(v4u*)(orow + gp * 64 + g * 16) = pack8(o[0] + un4(v2u{res[0], res[1]}), o[1] + un4(v2u{res[2], res[3]}));
  if (kw < 2) *(v2u*)(orow + gs * 64 + (t8 & 1) * 32 + g * 8) = pack4(o[4] + un4(v2u{res[4], res[5]}));
}

template <int R0, int R1> DI void stage_load(const Ctx& x, int voff, u64 skip, v4u (&r)[35]) {
#pragma unroll
  for (int k = R0; k < R1; ++k) {
    r[k] = ld16(x.rs, voff, ((skip >> k) & 1) ? 0x7f000000 : k * 4096);
    asm volatile("s_waitcnt vmcnt(" STR(PACE) ")" ::: "memory");
  }
}
template <int R0, int R1> DI void stage_drop(LAS char* p, const v4u (&r)[35]) {
#pragma unroll
  for (int k = R0; k < R1; ++k) {
    if (k % 16 == 0 || k == R0) p = pinp(p + (k == R0 ? (R0 / 16) * 65536 : 65536));
    *(LAS v4u*)(p + (k % 16) * 4096) = r[k];
  }
}
DI void dropped(const Ctx& x) { asm volatile("s_waitcnt lgkmcnt(0)" ::: "memory"); lds_inc(x, C_LAND); }
template <int S> DI void ring_drop(const Ctx& x, LAS char* p, const v4u (&r)[35]) {
  lds_wait_ge(x.lds + L_CTL + C_RING + 4 * S, 8);
  stage_drop<5 * S, S < 6 ? 5 * S + 5 : 33>(p, r);
  asm volatile("s_waitcnt lgkmcnt(0)" ::: "memory");
  lds_inc(x, C_RL + 4 * S);
}
template <class F> DI void weight_stream(const Ctx& x, int t, int t8, F idle) {
  const int lw = x.w & 3, lo = lw * 1024 + x.lane * 16;
  LAS char* p = x.lds + lo;
  v4u r[35];
  stage_load<0, 25>(x, lo + O_B5 + t8 * 102400, 0, r);
  STAMPL(41);
  idle();
  lds_wait_ge(x.lds + L_CTL + C_FREE, 8);
  stage_drop<5, 25>(p, r);
  lds_wait_ge(x.lds + L_CTL + C_EXD + 8, 8);
  stage_drop<0, 5>(p, r);
  dropped(x);
  STAMPL(51);
#pragma unroll 1
  for (int s = 1; s < 4; ++s) {
    const int voff = lo + (s == 1 ? O_B6 + t * 143360 : O_B7 + t8 * 276480 + (s - 2) * 143360);
    if ((HOLD >> s) & 1) lds_wait_ge(x.lds + L_CTL + C_GO, 3 + s);
    const u64 skip = (s == 1 && t == 15) ? 0x810204081ull * (lw == 0 ? 0x5a : lw == 1 ? 0x56 : lw == 2 ? 0x55 : 0x35)
                                         : s == 3 ? (lw < 2 ? 6ull : 7ull) << 32 : 0;
    stage_load<0, 35>(x, voff, skip, r);
    STAMPL(41 + s);
    if (s == 3) break;
    lds_wait_ge(x.lds + L_CTL + C_FREE + 4 * s, 8);
    stage_drop<14, 34>(p, r);
    lds_wait_ge(x.lds + L_CTL + C_EXD + 4 * (s - 1), 8);
    stage_drop<0, 14>(p, r);
    stage_drop<34, 35>(p, r);
    dropped(x);
    STAMPL(51 + s);
  }
  ring_drop<0>(x, p, r); ring_drop<1>(x, p, r); ring_drop<2>(x, p, r); ring_drop<3>(x, p, r);
  ring_drop<4>(x, p, r); ring_drop<5>(x, p, r); ring_drop<6>(x, p, r);
}

DI void wait_b(const Ctx& x, bool poller, int xcd) {
  wait_flags<3, DLYB>(x, poller, {F_B + xcd * 160 + x.lane, F_B + xcd * 160 + 64 + x.lane, F_B + xcd * 160 + 128 + (x.lane & 31)}, 3);
}
#define CODE_KB 48
#define MAP 1
__global__ __launch_bounds__(NTHR, 3) void fused(const u16* __restrict__ xin, const u16* __restrict__ wr, const u16* __restrict__ wi,
                                                 const u16* __restrict__ w1, const u16* __restrict__ w2, const u16* __restrict__ w3,
                                                 const u16* __restrict__ w4, u16* __restrict__ out, char* ws) {
  extern __shared__ __attribute__((aligned(16))) char smem[];
  if (blockIdx.x >= 240 && threadIdx.x >= 512) {
    u64 pc;
    asm volatile("s_getpc_b64 %0" : "=s"(pc));
    const char* code = (const char*)(pc & ~255ull) + (threadIdx.x - 512) * 16;
#pragma unroll 1
    for (int k = 0; k < CODE_KB / 4; ++k) dma16_sc1(code + k * 4096, (u32)(uintptr_t)(LAS char*)smem + L_MISC + 2048);
  }
  Ctx x;
  x.ws = ws; x.rs = __builtin_amdgcn_make_buffer_rsrc(ws, 0, WS_END, 0x00020000); x.lds = (LAS char*)smem;
  x.tid = threadIdx.x; x.lane = x.tid & 63; x.w = __builtin_amdgcn_readfirstlane(x.tid >> 6); x.g = x.lane >> 4; x.c = x.lane & 15;
  GAS u64* ep = (GAS u64*)(ws + O_EPOCH);
  x.epoch = __hip_atomic_load(ep, __ATOMIC_RELAXED, __HIP_MEMORY_SCOPE_AGENT);
  const int b = blockIdx.x, xcd = b & 7, i = b >> 3;
  const int P = 4 * (xcd >> 1) + (i >> 3), t = 8 * (xcd & 1) + (i & 7), hf = (i >> 2) & 1, t8 = 4 * (xcd & 1) + (i & 3);
  STAMP(0);
  if (x.w == 8) lds_st(x.lds + L_CTL + x.lane * 4, 0);
  if (b >= 160) side_jobs(x, xcd, i - 20, wr, wi, w1, w2, w3, w4);
  if (x.w >= 8) {
    auto poll_s = [&]() {
      poll<1, 8>(x, {F_S + xcd * 50 + min(x.lane, 49)});
      lds_st(x.lds + L_CTL + C_LGO, 1);
      STAMPW(9);
    };
    if (b < 160) {
      const int lw = x.w & 3;
      BAR();
      __builtin_amdgcn_s_setprio(3);
      phase_a_stats(x, xin, xcd, i & 3, i >> 2);
      STAMPL(59);
      __builtin_amdgcn_s_setprio(0);
      phase_b_consts(x);
      STAMPL(57);
      publish<8, 160>(x, 0, F_A + b, 3, 12);
      __builtin_amdgcn_s_setprio(3);
      if (lw == 3) {
        lds_wait_ge(x.lds + L_CTL + C_PUB, GWAIT);
        poll<1, 4>(x, {F_W + xcd * 46 + min(x.lane, 45)});
        lds_inc(x, C_GOW);
      }
      if (lw == 1) {
        lds_wait_ge(x.lds + L_CTL + C_PUB, 12);
        wait_flags<1, DLYA>(x, true, {F_A + xcd * 160 + (b & ~31) + (x.lane & 31)}, 2);
        if (b < 96 && x.lane < 8) __hip_atomic_store((GAS u64*)(x.ws + flag_at(F_X + b) + x.lane * 8), x.tag(), __ATOMIC_RELAXED, __HIP_MEMORY_SCOPE_AGENT);
      }
      if (lw == 0) { lds_wait_ge(x.lds + L_CTL + C_GO, 2); poll_s(); }
      __builtin_amdgcn_s_setprio(0);
      lds_wait_ge(x.lds + L_CTL + BCW, 8);
      __builtin_amdgcn_s_sleep(BDLY);
    } else if (x.w == 8) poll_s();
    lds_wait_ge(x.lds + L_CTL + C_LGO, 1);
    dma_lin(x, O_BC + t8 * 102400, 0, 0, 100);
    LANDED(0);
    STAMPL(40);
    if (b < 160) lds_wait_ge(x.lds + L_CTL + C_PUB + 4, 8);
    weight_stream(x, t, t8, [&] {
      if (b < 160) lds_wait_ge(x.lds + L_CTL + C_PUB + 4, 8);
      gen_q_tile(x, t8);
      STAMPL(48);
      if (x.w == 11) {
        poll<2, 6>(x, {F_T + xcd * 96 + x.lane, F_T + xcd * 96 + 64 + (x.lane & 31)});
        lds_st(x.lds + L_CTL + C_LGO, 2);
        colsum_6(x, t);
      } else lds_wait_ge(x.lds + L_CTL + C_LGO, 2);
      STAMPL(45);
    });
    return;
  }
  bool last;
  if (b < 160) {
    phase_a(x, xin, xcd, i & 3, i >> 2);
    STAMP(2);
    publish<8, 160>(x, 0, F_A + b, 3, 12);
    refresh(x);
    phase_b(x, b);
    STAMP(7);
    last = publish<8, 160>(x, 1, F_B + b, 8);
  } else {
    last = x.w == 0;
    if (last) poll<1, 8>(x, {F_B + xcd * 160 + i});
  }
  refresh(x);
  u32 res[6];
  phase_c5(x, 2 * P + hf, t8, res, [&] { wait_b(x, last, xcd); STAMP(11); });
  STAMP(17);
  last = publish(x, 3, F_5 + P * 16 + t, 18);
  refresh(x);
  phase_6(x, P, t, [&] { wait_flags<1, DLY>(x, last, {F_5 + P * 16 + x.c}, 5); STAMP(19); });
  STAMP(21);
  last = publish(x, 4, F_6 + P * 16 + t, 22);
  refresh(x);
  phase_7(x, P, hf, t8, res, out, [&] { wait_flags<1, DLY>(x, last, {F_6 + P * 16 + x.c}, 6); STAMP(23); });
  STAMP(25);
  if (b == 0 && x.w == 0) {
    asm volatile("s_waitcnt lgkmcnt(0)" ::: "memory");
    const u32 tx = __builtin_amdgcn_readfirstlane(*(volatile LAS u32*)(x.lds + L_CTL + 512));
    for (int k = 0; k < 100000; ++k) { if ((u32)__builtin_amdgcn_s_memrealtime() - tx >= 5000) break; __builtin_amdgcn_s_sleep(1); }
  }
  if (b == 0 && x.tid == 0) __hip_atomic_store(ep, x.tag(), __ATOMIC_RELAXED, __HIP_MEMORY_SCOPE_AGENT);
}

#define WSOFF 0
extern "C" void kernel_launch(void* const* d_in, const int* in_sizes, int n_in, void* d_out, int out_size, void* d_ws,
                              size_t ws_size, hipStream_t stream) {
  (void)hipFuncSetAttribute((const void*)fused, hipFuncAttributeMaxDynamicSharedMemorySize, LDS_BYTES);
  fused<<<256, NTHR, LDS_BYTES, stream>>>((const u16*)d_in[0], (const u16*)d_in[1], (const u16*)d_in[2], (const u16*)d_in[4],
                                          (const u16*)d_in[5], (const u16*)d_in[6], (const u16*)d_in[7], (u16*)d_out, (char*)d_ws + WSOFF);
}
```

```cpp
#include <hip/hip_runtime.h>
#include <stdint.h>

#define BPRIO 0
#define INORD 1
typedef unsigned u32;
typedef unsigned long long u64;
typedef unsigned short u16;
typedef u32 v4u __attribute__((ext_vector_type(4)));
typedef u32 v2u __attribute__((ext_vector_type(2)));
typedef float f32x4 __attribute__((ext_vector_type(4)));
typedef float f32x2 __attribute__((ext_vector_type(2)));
typedef __bf16 bf16x8 __attribute__((ext_vector_type(8)));
typedef __bf16 bf16x2 __attribute__((ext_vector_type(2)));
typedef short s16x4 __attribute__((ext_vector_type(4)));
typedef __amdgpu_buffer_rsrc_t rsrc_t;
#define LAS __attribute__((address_space(3)))
#define GAS __attribute__((address_space(1)))
#define DI __device__ __forceinline__

constexpr int ACT = 2048 * 640 * 2;
constexpr int O_EPOCH = 0;
constexpr int O_FLAG = 4096;
enum { F_C = 240, F_5 = 496, F_6 = 752, F_X = 1008, F_S = 1104, F_T = F_S + 8 * 50, F_B = F_T + 8 * 96, F_A = F_B + 8 * 160, F_W = F_A + 8 * 160, F_END = F_W + 8 * 46 };
__host__ __device__ constexpr int flag_at(int i) { return O_FLAG + i * 256; }
constexpr int O_SIG = 2 << 20;
static_assert(flag_at(F_END) <= O_SIG, "flag area");
constexpr int O_MU = O_SIG + 5 * 8192;
constexpr int O_CS3 = O_MU + 8192;
constexpr int O_ST1 = O_CS3 + 20 * 1792 * 4;
constexpr int O_ZT = O_ST1 + 2048 * 128;
constexpr int O_WT = O_ZT + ACT;
constexpr int O_HB = O_WT + ACT;
constexpr int O_BC = O_HB + ACT;
constexpr int O_B5 = O_BC + 8 * 102400 + 8192;
constexpr int O_B6 = O_B5 + 8 * 102400 + 8192;
constexpr int O_B7 = O_B6 + 16 * 143360 + 8192;
constexpr int O_G = O_B7 + 8 * 276480 + 8192;
constexpr int O_X1 = O_G + ACT;
constexpr int O_U = O_X1 + ACT;
constexpr int WS_END = O_U + 2048 * 1728 * 2;

constexpr int LDS_BYTES = 160 * 1024;
constexpr int L_MISC = 156 * 1024;
constexpr int L_CTL = 159 * 1024;
constexpr int L_TJ = 100 * 1024;
constexpr int L_Q = 100 * 1024;
constexpr int L_SQ = 150 * 1024;
constexpr int L_XS = 116 * 1024;
constexpr int L_XB = 132 * 1024;
enum { C_SB = 4, C_GO = 8, C_LAND = 12, C_LGO = 16, C_GOW = 20, C_SB0 = 24, C_SB1 = 28, C_PUB = 32, C_FREE = 64, C_QOK = 76, C_MID = 80, C_EX = 96, C_QF = 128, C_RING = 136, C_RL = 164, C_XL = 192, C_EXD = 208, C_ANY = 224, C_CS = 228 };
constexpr int NTHR = 768;
constexpr float EPS = 1e-5f;
constexpr int SC1 = 16;

DI u32 pk(float a, float b) { f32x2 v = {a, b}; return __builtin_bit_cast(u32, __builtin_convertvector(v, bf16x2)); }
DI float blo(u32 v) { return __builtin_bit_cast(float, v << 16); }
DI float bhi(u32 v) { return __builtin_bit_cast(float, v & 0xffff0000u); }
DI f32x4 mfma(v4u a, v4u b, f32x4 c) {
  return __builtin_amdgcn_mfma_f32_16x16x32_bf16(__builtin_bit_cast(bf16x8, a), __builtin_bit_cast(bf16x8, b), c, 0, 0, 0);
}
DI float dot2(u32 a, u32 b, float c) {
  return __builtin_amdgcn_fdot2_f32_bf16(__builtin_bit_cast(bf16x2, a), __builtin_bit_cast(bf16x2, b), c, false);
}
DI float dot8(v4u a, v4u b, float c) { return dot2(a.w, b.w, dot2(a.z, b.z, dot2(a.y, b.y, dot2(a.x, b.x, c)))); }
DI v2u trread(LAS char* p) { return __builtin_bit_cast(v2u, __builtin_amdgcn_ds_read_tr16_b64_v4i16((LAS s16x4*)p)); }
DI float cosr(float rev) { return __builtin_amdgcn_cosf(rev); }
DI float sinr(float rev) { return __builtin_amdgcn_sinf(rev); }
typedef float f32x3 __attribute__((ext_vector_type(3)));
DI f32x3 gelu_k(float r, float inv_r) { return f32x3{-2.302208198f * r, -0.1029432397f * r * r * r, inv_r}; }
template <int N> DI void gelu(f32x3 k, const f32x2 (&s)[N], u32 (&o)[N]) {
  f32x2 y[N];
#pragma unroll
  for (int i = 0; i < N; ++i) y[i] = s[i] * s[i];
  __builtin_amdgcn_sched_barrier(0);
#pragma unroll
  for (int i = 0; i < N; ++i) y[i] = y[i] * k[1] + k[0];
  __builtin_amdgcn_sched_barrier(0);
#pragma unroll
  for (int i = 0; i < N; ++i) y[i] *= s[i];
  __builtin_amdgcn_sched_barrier(0);
#pragma unroll
  for (int i = 0; i < N; ++i) y[i] = f32x2{__builtin_amdgcn_exp2f(y[i][0]), __builtin_amdgcn_exp2f(y[i][1])};
  __builtin_amdgcn_sched_barrier(0);
#pragma unroll
  for (int i = 0; i < N; ++i) y[i] = y[i] * k[2] + k[2];
  __builtin_amdgcn_sched_barrier(0);
#pragma unroll
  for (int i = 0; i < N; ++i) y[i] = f32x2{__builtin_amdgcn_rcpf(y[i][0]), __builtin_amdgcn_rcpf(y[i][1])};
  __builtin_amdgcn_sched_barrier(0);
#pragma unroll
  for (int i = 0; i < N; ++i) y[i] *= s[i];
  __builtin_amdgcn_sched_barrier(0);
#pragma unroll
  for (int i = 0; i < N; ++i) o[i] = pk(y[i][0], y[i][1]);
}
DI void dma16(const void* gsrc, u32 lds_dst) {
  u32 keep;
  asm volatile("s_mov_b32 %0, m0\n\ts_mov_b32 m0, %2\n\ts_nop 0\n\tglobal_load_lds_dwordx4 %1, off\n\ts_mov_b32 m0, %0"
               : "=&s"(keep) : "v"(gsrc), "s"(__builtin_amdgcn_readfirstlane(lds_dst)) : "memory");
}
DI void dma16_sc1(const void* gsrc, u32 lds_dst) {
  u32 keep;
  asm volatile("s_mov_b32 %0, m0\n\ts_mov_b32 m0, %2\n\ts_nop 0\n\tglobal_load_lds_dwordx4 %1, off sc1\n\ts_mov_b32 m0, %0"
               : "=&s"(keep) : "v"(gsrc), "s"(__builtin_amdgcn_readfirstlane(lds_dst)) : "memory");
}
#define VMWAIT(n) asm volatile("s_waitcnt vmcnt(" #n ")" ::: "memory")
#define BAR() asm volatile("s_waitcnt lgkmcnt(0)\n\ts_barrier" ::: "memory")
#define LDS_ORDER() asm volatile("" ::: "memory")
#define PRIO(n) __builtin_amdgcn_sched_barrier(0)
#define SCHED() __builtin_amdgcn_sched_barrier(0)
DI v4u ld16(rsrc_t r, int voff, int soff) { return __builtin_amdgcn_raw_buffer_load_b128(r, voff, soff, SC1); }
DI v2u ld8(rsrc_t r, int voff, int soff) { return __builtin_amdgcn_raw_buffer_load_b64(r, voff, soff, SC1); }
DI u32 ld4(rsrc_t r, int voff, int soff) { return __builtin_amdgcn_raw_buffer_load_b32(r, voff, soff, SC1); }
DI void st16(rsrc_t r, v4u v, int voff, int soff) { __builtin_amdgcn_raw_buffer_store_b128(v, r, voff + soff, 0, SC1); }
DI void st8(rsrc_t r, v2u v, int voff, int soff) { __builtin_amdgcn_raw_buffer_store_b64(v, r, voff, soff, SC1); }
DI void st4(rsrc_t r, u32 v, int voff, int soff) { __builtin_amdgcn_raw_buffer_store_b32(v, r, voff, soff, SC1); }
DI float xsum16_32(float v) {
  float u = v;
  asm volatile("s_nop 1\n\tv_permlane16_swap_b32 %0, %1" : "+v"(u), "+v"(v));
  v += u; u = v;
  asm volatile("s_nop 1\n\tv_permlane32_swap_b32 %0, %1" : "+v"(u), "+v"(v));
  return v + u;
}
template <int CTRL> DI float dppadd(float v) {
  return v + __builtin_bit_cast(float, __builtin_amdgcn_mov_dpp(__builtin_bit_cast(int, v), CTRL, 0xf, 0xf, true));
}
DI float xsum8(float v) { return dppadd<0x141>(dppadd<0x4E>(dppadd<0xB1>(v))); }
#define PIN(v) asm volatile("" : "+v"(v))

#define STAMP(i)
#define STAMP2(i)
#define STAMPA(i)
#define STAMPW(i)
#define STAMPL(i)
struct Ctx {
  char* ws; rsrc_t rs; LAS char* lds; u64 epoch;
  int tid, lane, w, g, c;
  DI u64 tag() const { u64 e = epoch; asm volatile("" : "+v"(e)); return e + 0x9E3779B97F4A7C15ull; }
};
DI void refresh(Ctx& x) {
  int l = x.lane;
  asm volatile("" : "+v"(l));
  x.lane = l; x.g = l >> 4; x.c = l & 15;
}
DI LAS char* pinp(LAS char* p) { asm volatile("" : "+v"(p)); return p; }
#define LSLP 2
#define GSLP 1
#define PSLP 2
template <int SLP = LSLP> DI void lds_wait_ge(LAS char* p, u32 want) {
  u32 v, n = 1u << 20;
  asm volatile("1:\n\tds_read_b32 %0, %2\n\ts_waitcnt lgkmcnt(0)\n\tv_cmp_gt_u32 vcc, %3, %0\n\ts_sub_u32 %1, %1, 1\n\ts_cbranch_scc1 2f\n\t"
               "s_cbranch_vccz 2f\n\ts_sleep %4\n\ts_branch 1b\n2:"
               : "=&v"(v), "+s"(n) : "v"(p), "s"(want), "n"(SLP) : "vcc", "scc", "memory");
}
DI void lds_wait_ge_pre(LAS char* p, u32 want, u32 v) {
  u32 n = 1u << 20;
  asm volatile("1:\n\tv_cmp_gt_u32 vcc, %3, %0\n\ts_sub_u32 %1, %1, 1\n\ts_cbranch_scc1 2f\n\ts_cbranch_vccz 2f\n\t"
               "ds_read_b32 %0, %2\n\ts_waitcnt lgkmcnt(0)\n\ts_branch 1b\n2:"
               : "+v"(v), "+s"(n) : "v"(p), "n"(want) : "vcc", "scc", "memory");
}
DI void lds_inc(const Ctx& x, int word) {
  u64 keep;
  asm volatile("s_mov_b64 %0, exec\n\ts_mov_b64 exec, 1\n\tds_add_u32 %1, %2\n\ts_mov_b64 exec, %0"
               : "=&s"(keep) : "v"(x.lds + L_CTL + word), "v"(1u) : "memory");
}
template <int REP, int STR> DI void raise(const Ctx& x, int flag) {
  const v2u t = __builtin_bit_cast(v2u, x.tag());
  if (x.lane < 8 * REP) st16(x.rs, v4u{t.x, t.y, t.x, t.y}, flag_at(flag + (x.lane >> 3) * STR) + (x.lane & 7) * 16, 0);
}
DI void clear_flag(const Ctx& x, int flag) { st16(x.rs, v4u{0u, 0u, 0u, 0u}, flag_at(flag) + (x.lane & 7) * 16, 0); }
template <int REP = 1, int STR = 0> DI bool publish(const Ctx& x, int idx, int flag, int stamp, int NW = 8) {
  __builtin_amdgcn_s_setprio(3);
  __builtin_amdgcn_s_waitcnt(0x0070);
  asm volatile("s_waitcnt vmcnt(0) lgkmcnt(0)" ::: "memory");
  u32 old;
  u64 keep;
  asm volatile("s_mov_b64 %1, exec\n\ts_mov_b64 exec, 1\n\tds_add_rtn_u32 %0, %2, %3\n\ts_mov_b64 exec, %1\n\ts_waitcnt lgkmcnt(0)"
               : "=&v"(old), "=&s"(keep) : "v"(x.lds + L_CTL + C_PUB + 4 * idx), "v"(1u) : "memory");
  const bool last = __builtin_amdgcn_readfirstlane(old) == NW - 1;
  if (last) {
    raise<REP, STR>(x, flag);
    STAMPW(stamp);
  }
  asm volatile("" ::: "memory");
  __builtin_amdgcn_s_setprio(0);
  return last;
}
DI void soft_barrier(const Ctx& x, int word, u32 target) {
  asm volatile("s_waitcnt lgkmcnt(0)" ::: "memory");
  lds_inc(x, word);
  lds_wait_ge<0>(x.lds + L_CTL + word, target);
}
template <int N, int SLP = PSLP> DI void poll(const Ctx& x, const int (&off)[N]) {
  const u64 tag = x.tag();
  int o[N];
#pragma unroll
  for (int i = 0; i < N; ++i) o[i] = flag_at(off[i]);
#pragma unroll 1
  for (int spin = 0; spin < (1 << 16); ++spin) {
    v2u v[N];
#pragma unroll
    for (int i = 0; i < N; ++i) v[i] = ld8(x.rs, o[i], 0);
    bool ok = true;
#pragma unroll
    for (int i = 0; i < N; ++i) {
      if (__builtin_bit_cast(u64, v[i]) == tag) o[i] = 0x7f000000;
      ok &= o[i] == 0x7f000000;
    }
    if (__all(ok)) break;
    __builtin_amdgcn_s_sleep(SLP);
    asm volatile("" ::: "memory");
  }
  __builtin_amdgcn_fence(__ATOMIC_ACQUIRE, "wavefront");
  asm volatile("" ::: "memory");
}
DI void lds_st(LAS char* p, u32 v) { __hip_atomic_store((LAS u32*)p, v, __ATOMIC_RELAXED, __HIP_MEMORY_SCOPE_WORKGROUP); }
template <int N, int DELAY, int SLP = PSLP> DI void wait_flags(const Ctx& x, bool poller, const int (&off)[N], u32 gen) {
  if (poller) {
    if (DELAY) __builtin_amdgcn_s_sleep(DELAY);
    poll<N, SLP>(x, off);
    lds_st(x.lds + L_CTL + C_GO, gen);
  } else lds_wait_ge<GSLP>(x.lds + L_CTL + C_GO, gen);
}
#define LANDED(n) do { VMWAIT(n); lds_inc(x, C_LAND); } while (0)
DI void wait_landed(const Ctx& x, u32 n) { lds_wait_ge(x.lds + L_CTL + C_LAND, 4 * n); }

template <int GP> DI void group_dst(int gam, int h, int& tile, int& nb, bool& single) {
  int sup = gam / (2 * GP + 1), e = gam - sup * (2 * GP + 1);
  single = (e == GP);
  tile = 2 * sup + (e > GP ? 1 : (single ? h : 0));
  nb = single ? 2 * GP : 2 * (e > GP ? e - GP - 1 : e) + h;
}

DI v2u pack4(f32x4 a) { return v2u{pk(a[0], a[1]), pk(a[2], a[3])}; }
DI f32x4 unlo(v4u v) { return f32x4{blo(v.x), bhi(v.x), blo(v.y), bhi(v.y)}; }
DI f32x4 unhi(v4u v) { return f32x4{blo(v.z), bhi(v.z), blo(v.w), bhi(v.w)}; }
DI f32x4 un4(v2u v) { return f32x4{blo(v.x), bhi(v.x), blo(v.y), bhi(v.y)}; }
DI v4u pack8(const float (&v)[8]) { return v4u{pk(v[0], v[1]), pk(v[2], v[3]), pk(v[4], v[5]), pk(v[6], v[7])}; }
DI v4u pack8(f32x4 a, f32x4 b) { return v4u{pk(a[0], a[1]), pk(a[2], a[3]), pk(b[0], b[1]), pk(b[2], b[3])}; }
DI int bin_of(int pi) { return pi == 0 ? 160 : (pi & 1) ? 320 - (pi >> 1) : pi >> 1; }
DI void gen_q(int j, int g, int m0, v4u (&qc)[5], v4u (&qs)[5]) {
  const float amp = 0.05590169944f;
  const float kf = (float)j * (1.f / 320.f), a0 = (float)(((m0 + 4 * g) * j) % 320) * (1.f / 320.f);
  const float rc = cosr(32.f * kf), rs = sinr(32.f * kf), c2 = rc + rc;
  f32x2 c0[4], s0[4], c1[4], s1[4];
#pragma unroll
  for (int i = 0; i < 4; ++i) {
    const float u = a0 + (float)(16 * (i >> 1) + 2 * (i & 1)) * kf, v = u + kf;
    c0[i] = j ? f32x2{cosr(u), cosr(v)} * amp : f32x2{amp, -amp};
    s0[i] = j ? f32x2{sinr(u), sinr(v)} * -amp : f32x2{amp, amp};
    c1[i] = c0[i] * rc + s0[i] * rs;
    s1[i] = s0[i] * rc - c0[i] * rs;
  }
  auto pk4 = [](const f32x2 (&v)[4]) { return v4u{pk(v[0][0], v[0][1]), pk(v[1][0], v[1][1]), pk(v[2][0], v[2][1]), pk(v[3][0], v[3][1])}; };
#pragma unroll
  for (int ks = 0; ks < 5; ++ks) {
    qc[ks] = pk4(c0); qs[ks] = pk4(s0);
#pragma unroll
    for (int i = 0; i < 4; ++i) {
      const f32x2 tc = c2 * c1[i] - c0[i], ts = c2 * s1[i] - s0[i];
      c0[i] = c1[i]; c1[i] = tc; s0[i] = s1[i]; s1[i] = ts;
    }
  }
#pragma unroll
  for (int i = 0; i < 5; ++i) { PIN(qc[i]); PIN(qs[i]); }
}

template <int K0, int K1> DI void x_fetch(const Ctx& x, const u16* xin, int mm, int q4, int nt, int rg) {
  const int lam = 8 * rg + (x.lane >> 3);
  const int srow = 256 * mm + 32 * (4 * ((lam >> 4) & 1) + (lam & 3)) + 8 * q4 + 4 * (lam >> 5) + ((lam >> 2) & 3);
  const char* src = (const char*)xin + srow * 1280 + (((x.lane & 7) ^ (lam & 7)) << 4);
#pragma unroll
  for (int kc = K0; kc < K1; ++kc) {
    const int kr = kc + 2 * nt;
    dma16(src + (kr < 10 ? kr : kr - 10) * 128, (u32)(uintptr_t)x.lds + (kc * 8 + rg) * 1024);
  }
}
DI void phase_a(const Ctx& x, const u16* xin, int mm, int q4, int nt) {
  const int w = x.w, g = x.g, c = x.c;
  BAR();
  const int rb = w >> 1, j = 32 * nt + 16 * (w & 1) + c;
  v4u qc[5], qs[5];
  gen_q(j, g, 64 * nt, qc, qs);
  float tc = cosr((float)j * (1.f / 640.f)), ts = sinr((float)j * (1.f / 640.f));
  int off = O_ZT + ((nt * 32 + mm * 4 + q4) * 32 + (j & 31)) * 512 + rb * 128 + g * 16;
  PIN(tc); PIN(ts); PIN(off);
  f32x4 acc[2][2][2];
#pragma unroll
  for (int i = 0; i < 8; ++i) acc[i >> 2][(i >> 1) & 1][i & 1] = f32x4{0.f, 0.f, 0.f, 0.f};
  LAS char* a0 = x.lds + rb * 2048 + (c >> 3) * 1024 + (c & 7) * 128 + ((g ^ (c & 7)) << 4);
  LAS char* a1 = x.lds + rb * 2048 + (c >> 3) * 1024 + (c & 7) * 128 + (((g + 4) ^ (c & 7)) << 4);
  v4u f[2][2], e[2];
  f32x4 r0;
  u32 seen = 0;
  auto split = [&](int d, int odd) {
    const u32 sel = odd ? 0x07060302u : 0x05040100u;
    e[odd] = v4u{__builtin_amdgcn_perm(f[d][0].y, f[d][0].x, sel), __builtin_amdgcn_perm(f[d][0].w, f[d][0].z, sel),
                 __builtin_amdgcn_perm(f[d][1].y, f[d][1].x, sel), __builtin_amdgcn_perm(f[d][1].w, f[d][1].z, sel)};
  };
  SCHED();
  STAMP(36);
  VMWAIT(0); soft_barrier(x, C_XL, 8); STAMP(1);
#pragma unroll
  for (int d = 0; d < 2; ++d) { f[d][0] = *(LAS v4u*)(a0 + d * 8192); f[d][1] = *(LAS v4u*)(a1 + d * 8192); }
  split(0, 0);
#pragma unroll
  for (int kc = 0; kc < 10; ++kc) {
    if (kc == 2 || kc == 5) lds_wait_ge_pre(x.lds + L_CTL + C_XL + (kc == 2 ? 8 : 12), 4, seen);
    if (kc == 9) lds_wait_ge_pre(x.lds + L_CTL + C_XL + 4, 4, seen);
    if (kc == 3) { a0 = pinp(a0 + 40960); a1 = pinp(a1 + 40960); }
    SCHED();
    acc[kc / 5][0][0] = mfma(e[0], qc[kc % 5], acc[kc / 5][0][0]);
    acc[kc / 5][0][1] = mfma(e[0], qs[kc % 5], acc[kc / 5][0][1]);
    SCHED();
    split(kc & 1, 1);
    SCHED();
    if (kc < 8) { f[kc & 1][0] = *(LAS v4u*)(a0 + ((kc + 2) % 5) * 8192); f[kc & 1][1] = *(LAS v4u*)(a1 + ((kc + 2) % 5) * 8192); }
    if (kc == 1 || kc == 4 || kc == 8) seen = *(volatile LAS u32*)(x.lds + L_CTL + C_XL + (kc == 1 ? 8 : kc == 4 ? 12 : 4));
    if (kc == 9) r0 = *(LAS f32x4*)(x.lds + L_MISC + 1024 + (16 * rb + 4 * g) * 4);
    acc[kc / 5][1][0] = mfma(e[1], qc[kc % 5], acc[kc / 5][1][0]);
    acc[kc / 5][1][1] = mfma(e[1], qs[kc % 5], acc[kc / 5][1][1]);
    SCHED();
    if (kc < 9) split((kc + 1) & 1, 0);
  }
  __builtin_amdgcn_s_setprio(2);
  STAMP(37);
  STAMPA(L_CTL + 256 + (x.w == 7 ? 58 * 4 : 512));
  const f32x4 r1 = (c & 1) ? -r0 : r0;
  const f32x4 er = acc[0][0][0] * r0 + acc[1][0][0] * r1, ei = acc[0][0][1] * r0 + acc[1][0][1] * r1;
  const f32x4 oc = acc[0][1][0] * r0 + acc[1][1][0] * r1, os = acc[0][1][1] * r0 + acc[1][1][1] * r1;
  const f32x4 pr = oc * tc + os * ts, pi = os * tc - oc * ts;
  v4u lo = pack8(er + pr, ei + pi), hi = pack8(er - pr, pi - ei);
  if (nt == 0 && (w & 1) == 0) {
    const v4u l0 = pack8(er, -oc);
    const v2u h0 = pack4((ei - os) * 0.70710678f);
    if (c == 0) { lo = l0; hi = v4u{h0.x, h0.y, 0u, 0u}; }
  }
  st16(x.rs, lo, off, 0);
  st16(x.rs, hi, off, 64);
}

DI void phase_a_stats(const Ctx& x, const u16* xin, int mm, int q4, int nt) {
  const int lw = x.w & 3, g = x.g, c = x.c, rt = mm * 4 + q4;
  x_fetch<4, 7>(x, xin, mm, q4, nt, 2 * lw);
  x_fetch<4, 7>(x, xin, mm, q4, nt, 2 * lw + 1);
  x_fetch<7, 10>(x, xin, mm, q4, nt, 2 * lw);
  x_fetch<7, 10>(x, xin, mm, q4, nt, 2 * lw + 1);
  const v4u ones = {0x3f803f80u, 0x3f803f80u, 0x3f803f80u, 0x3f803f80u};
  f32x4 gr = {0.f, 0.f, 0.f, 0.f}, sm = gr;
  LAS char* a0 = x.lds + lw * 2048 + (c >> 3) * 1024 + (c & 7) * 128 + ((g ^ (c & 7)) << 4);
  LAS char* a1 = x.lds + lw * 2048 + (c >> 3) * 1024 + (c & 7) * 128 + (((g + 4) ^ (c & 7)) << 4);
#pragma unroll
  for (int st = 0; st < 3; ++st) {
    constexpr int k0[3] = {4, 0, 7}, k1[3] = {7, 4, 10};
    if (st == 0) { VMWAIT(6); lds_inc(x, C_XL + 8); }
    if (st == 1) { VMWAIT(0); lds_inc(x, C_XL + 12); STAMPL(60); lds_wait_ge<0>(x.lds + L_CTL + C_XL, 8); }
    LAS char* p0 = pinp(a0 + k0[st] * 8192);
    LAS char* p1 = pinp(a1 + k0[st] * 8192);
    v4u f[8];
#pragma unroll
    for (int k = 0; k < k1[st] - k0[st]; ++k) { f[2 * k] = *(LAS v4u*)(p0 + k * 8192); f[2 * k + 1] = *(LAS v4u*)(p1 + k * 8192); }
#pragma unroll
    for (int k = 0; k < 2 * (k1[st] - k0[st]); ++k) { gr = mfma(f[k], f[k], gr); sm = mfma(ones, f[k], sm); }
    SCHED();
  }
  const int r = c & 3;
  const float mu = sm[0] * (1.f / 640.f);
  const float var = fmaxf((r == 0 ? gr[0] : r == 1 ? gr[1] : r == 2 ? gr[2] : gr[3]) * (1.f / 640.f) - mu * mu, 0.f) + EPS;
  const float rstd = __builtin_amdgcn_rsqf(var);
  if ((c >> 2) == g) {
    *(LAS float*)(x.lds + L_MISC + 1024 + (lw * 16 + c) * 4) = rstd;
    st4(x.rs, __builtin_bit_cast(u32, var * rstd), O_SIG + nt * 8192 + (rt * 64 + lw * 16 + c) * 4, 0);
    if (nt == 0) st4(x.rs, __builtin_bit_cast(u32, mu), O_MU + (rt * 64 + lw * 16 + c) * 4, 0);
  }
  asm volatile("s_waitcnt lgkmcnt(0)" ::: "memory");
  lds_inc(x, C_XL + 4);
}
DI int stat_at(int n1, int n2) {
  return ((n1 >> 3) * 4 + (n2 >> 3)) * 64 + (2 * ((n2 >> 2) & 1) + ((n1 >> 2) & 1)) * 16 + 4 * (n2 & 3) + (n1 & 3);
}

DI void wt_dma(const Ctx& x, const u16* wr, const u16* wi, int v, int lo) {
  const int fq = v >> 4, fh = v & 15, lw = x.w & 3;
#pragma unroll
  for (int i = 0; i < 2; ++i) {
    const char* src = (const char*)(i ? wi : wr) + (4 * fq + lw) * 41088 + ((fh * 2568) & ~15);
#pragma unroll
    for (int pc = 0; pc < 3; ++pc) dma16(src + min(pc * 64 + x.lane, 160) * 16, (u32)(uintptr_t)x.lds + lo + (lw * 2 + i) * 3072 + pc * 1024);
  }
}
DI void wt_out(const Ctx& x, int v, int lo) {
  const int k0 = 2 * ((x.lane >> 3) * 20 + (x.w & 3) * 5) + ((x.lane >> 2) & 1);
  LAS const u16* sp = (LAS const u16*)(x.lds + lo) + (v & 1) * 4 + (x.lane & 3) * 321;
#pragma unroll
  for (int it = 0; it < 5; ++it) {
    LAS const u16* q = sp + bin_of(k0 + 2 * it);
    v4u o;
#pragma unroll
    for (int r = 0; r < 4; ++r) o[r] = (u32)q[r * 3072] | ((u32)q[r * 3072 + 1536] << 16);
    st16(x.rs, o, O_WT + v * 20480 + ((x.w & 3) * 5 + it) * 1024 + x.lane * 16, 0);
  }
}
template <int GP> DI int blk_addr(const Ctx& x, int gam0, int rb, int& tile, int& nb) {
  const int p = x.c & 3, r0 = 8 * x.g + (x.c >> 2);
  bool single;
  group_dst<GP>(gam0 + (rb >> 1), rb & 1, tile, nb, single);
  const int ch = (rb >> 1) * 4 + (single ? 2 * (rb & 1) + (p >> 1) : p), hb = single ? (p & 1) : (rb & 1);
  return r0 * 128 + ((ch ^ (((r0 >> 1) & 1) << 2)) << 4) + hb * 8;
}
DI void tile_pos(int j, int& ks, int& ct) {
  const bool is3 = j < 540;
  const int jj = j - (is3 ? 0 : j < 1080 ? 540 : 1080);
  ks = (jj * (is3 ? 2428 : 6554)) >> 16;
  ct = jj - ks * (is3 ? 27 : 10);
}
DI void tile_dma(const Ctx& x, int j, int lds_off, const u16* w2, const u16* w3, const u16* w4) {
  const int row = x.lane >> 3, ch = ((x.lane & 7) ^ (((row >> 1) & 1) << 2)) << 4, rs = j < 540 ? 3456 : 1280;
  int ks, ct;
  tile_pos(j, ks, ct);
  const char* src = (const char*)(j < 540 ? w3 : j < 1080 ? w4 : w2) + (ks * 32 * rs + ct * 128) + (row * rs + ch);
#pragma unroll
  for (int i = 0; i < 4; ++i) dma16(src + i * 8 * rs, (u32)(uintptr_t)x.lds + lds_off + i * 1024);
}
struct Tile { v4u f[4], cs[4]; int dst[4], cso[4]; };
DI void tile_read(const Ctx& x, int j, bool live, int lds_off, Tile& t) {
  const bool is3 = j < 540;
  const int GP = is3 ? 3 : 2, tb = is3 ? 143360 : j < 1080 ? 276480 : 102400, base = is3 ? O_B6 : j < 1080 ? O_B7 : O_B5;
  int ks, ct;
  tile_pos(j, ks, ct);
  const int p = x.c & 3, r0 = 8 * x.g + (x.c >> 2), sw = ((r0 >> 1) & 1) << 6;
  LAS char* lp = x.lds + lds_off + r0 * 128;
  const v4u ones = {0x3f803f80u, 0x3f803f80u, 0x3f803f80u, 0x3f803f80u};
#pragma unroll
  for (int rb = 0; rb < 4; ++rb) {
    const int gam = 2 * ct + (rb >> 1), h = rb & 1, sup = (gam * (is3 ? 37 : 52)) >> 8, e = gam - sup * (2 * GP + 1);
    const bool single = e == GP, hi = e > GP;
    const int d = (2 * sup + (int)(hi || (single && h))) * tb + (ks * (2 * GP + 1) + (single ? 2 * GP : 2 * (e - (hi ? GP + 1 : 0)) + h)) * 1024;
    LAS char* ad = lp + (((rb >> 1) * 64 + (single ? h * 32 + p * 8 : p * 16 + h * 8)) ^ sw);
    const v2u lo = trread(ad), hi2 = trread(ad + 512);
    t.f[rb] = v4u{lo.x, lo.y, hi2.x, hi2.y};
    t.cs[rb] = __builtin_bit_cast(v4u, mfma(t.f[rb], ones, f32x4{0.f, 0.f, 0.f, 0.f}));
    t.dst[rb] = live ? base + d + x.lane * 16 : 0x7f000000;
    t.cso[rb] = (live && is3 && x.c == 0) ? O_CS3 + (d >> 4) + x.g * 16 : 0x7f000000;
    PIN(t.dst[rb]); PIN(t.cso[rb]);
  }
}
DI void tile_store(const Ctx& x, const Tile& t) {
#pragma unroll
  for (int rb = 0; rb < 4; ++rb) st16(x.rs, t.f[rb], t.dst[rb], 0);
#pragma unroll
  for (int rb = 0; rb < 4; ++rb) st16(x.rs, t.cs[rb], t.cso[rb], 0);
}
template <int K0, int K1> DI void w1_fetch(const Ctx& x, const u16* w1, int ct, int nt, int rg) {
  const int row = 8 * rg + (x.lane >> 3);
  const char* src = (const char*)w1 + (2 * (row & 31) + (row >> 5)) * 1280 + ct * 128 + (((x.lane & 7) ^ (((row >> 1) & 1) << 2)) << 4);
#pragma unroll
  for (int i = K0; i < K1; ++i) {
    const int kr = i + 2 * nt;
    dma16(src + (kr < 10 ? kr : kr - 10) * 64 * 1280, (u32)(uintptr_t)x.lds + (i * 8 + rg) * 1024);
  }
}
DI void side_w1p(const Ctx& x, const u16* w1, int ct, int nt) {
  w1_fetch<0, 4>(x, w1, ct, nt, x.w);
  BAR();
  const int j0 = 32 * nt + 16 * (x.w & 1);
  v4u qc[5], qs[5];
  gen_q(j0 + x.c, x.g, 64 * nt, qc, qs);
  f32x4 tc, ts;
#pragma unroll
  for (int r = 0; r < 4; ++r) {
    const float a = (float)(j0 + 4 * x.g + r) * (1.f / 640.f);
    tc[r] = 1.41421356f * cosr(a); ts[r] = 1.41421356f * sinr(a);
  }
  PIN(tc); PIN(ts);
  f32x4 acc[2][2][2];
#pragma unroll
  for (int i = 0; i < 8; ++i) acc[i >> 2][(i >> 1) & 1][i & 1] = f32x4{0.f, 0.f, 0.f, 0.f};
  int tile, nb;
  int addr = blk_addr<2>(x, 2 * ct, x.w >> 1, tile, nb) - x.g * 512;
  SCHED();
  VMWAIT(0);
  soft_barrier(x, C_SB, 8);
  STAMP(1);
  v2u lo[4], hi[4];
#pragma unroll
  for (int i = 0; i < 4; ++i) { lo[i] = trread(x.lds + addr + i * 4096); hi[i] = trread(x.lds + addr + i * 4096 + 2048); }
#pragma unroll
  for (int kc = 0; kc < 10; ++kc) {
    if (kc == 2) lds_wait_ge<0>(x.lds + L_CTL + C_XL, 4);
    if (kc == 3) { addr += 40960; PIN(addr); }
#pragma unroll
    for (int odd = 0; odd < 2; ++odd) {
      const int i = 2 * (kc & 1) + odd;
      const v4u f = {lo[i].x, lo[i].y, hi[i].x, hi[i].y};
      acc[kc / 5][odd][0] = mfma(qc[kc % 5], f, acc[kc / 5][odd][0]);
      acc[kc / 5][odd][1] = mfma(qs[kc % 5], f, acc[kc / 5][odd][1]);
      SCHED();
      if (kc < 8) {
        LAS char* ad = x.lds + addr + ((kc + 2) % 5) * 8192 + odd * 4096;
        lo[i] = trread(ad); hi[i] = trread(ad + 2048);
      }
      SCHED();
    }
  }
  const f32x4 sg = {1.f, -1.f, 1.f, -1.f};
  f32x4 er = acc[0][0][0] + sg * acc[1][0][0], ei = acc[0][0][1] + sg * acc[1][0][1];
  const f32x4 oc = acc[0][1][0] + sg * acc[1][1][0], os = acc[0][1][1] + sg * acc[1][1][1];
  f32x4 pr = oc * tc + os * ts, pi = os * tc - oc * ts;
  const bool p0 = j0 == 0 && x.g == 0;
  const f32x4 es = {p0 ? 1.f : 1.41421356f, 1.41421356f, 1.41421356f, 1.41421356f};
  er *= es; ei *= es;
  if (p0) { pr[0] = -oc[0]; pi[0] = -os[0]; }
  const int o = O_BC + tile * 102400 + (((j0 >> 3) + (x.g >> 1)) * 5 + nb) * 1024 + (2 * (x.g & 1) * 16 + x.c) * 16;
#pragma unroll
  for (int h = 0; h < 2; ++h)
    st16(x.rs, v4u{pk(er[2 * h], ei[2 * h]), pk(pr[2 * h], pi[2 * h]), pk(er[2 * h + 1], ei[2 * h + 1]), pk(pr[2 * h + 1], pi[2 * h + 1])}, o, h * 256);
}
#define SDLY 0
#define YDLY 0
#define XMID 1
DI void side_jobs(const Ctx& x, int xcd, int sl, const u16* wr, const u16* wi, const u16* w1, const u16* w2, const u16* w3, const u16* w4) {
  const int y = sl < 2 ? sl * 8 + xcd : (sl < 7 && xcd >= 2) ? 16 + (sl - 2) * 6 + (xcd - 2) : -1;
  const int ct = sl >= 7 ? xcd : 8 + xcd, nt = sl >= 7 ? sl - 7 : sl - 2, xr = nt * 10 + ct;
  const int v = sl * 8 + xcd, j0 = y < 0 ? xr * 11 : 550 + y * 12, nj = y < 0 ? 11 : min(12, 1080 - j0);
  if (x.w == 9) {
    clear_flag(x, F_T + v + (x.lane >> 3) * 96);
    clear_flag(x, y >= 0 ? F_W + y + (x.lane >> 3) * 46 : F_S + xr + (x.lane >> 3) * 50);
  }
  if (SDLY) __builtin_amdgcn_s_sleep(SDLY);
  if (y >= 0) {
    const int job = y * 3 + (x.w >> 2), lo = (x.w >> 2) * 24576;
    if (job < 128) wt_dma(x, wr, wi, job, lo);
    BAR();
    if (YDLY) __builtin_amdgcn_s_sleep(YDLY);
    tile_dma(x, j0 + x.w, L_TJ + x.w * 4096, w2, w3, w4);
    VMWAIT(4);
    BAR();
    STAMP(1);
    if (job < 128) wt_out(x, job, lo);
    STAMP(5);
    publish<8, 46>(x, 0, F_W + y, 2, 12);
  } else {
    if (x.w < 8) side_w1p(x, w1, ct, nt);
    else {
      BAR();
      __builtin_amdgcn_s_setprio(3);
      w1_fetch<4, 10>(x, w1, ct, nt, 2 * (x.w & 3));
      w1_fetch<4, 10>(x, w1, ct, nt, 2 * (x.w & 3) + 1);
      tile_dma(x, 1080 + xr * 4 + x.w - 8, L_TJ + x.w * 4096, w2, w3, w4);
      VMWAIT(4);
      lds_inc(x, C_XL);
      VMWAIT(0);
      Tile t;
      tile_read(x, 1080 + xr * 4 + x.w - 8, true, L_TJ + x.w * 4096, t);
      tile_store(x, t);
    }
    STAMP(5);
    publish<8, 50>(x, 1, F_S + xr, 3, 12);
    if (x.w >= 8) {
#pragma unroll 1
      for (int k = x.w & 3; k < 11; k += 4) tile_dma(x, j0 + k, L_TJ + k * 4096, w2, w3, w4);
      VMWAIT(0);
      lds_inc(x, C_XL + 4);
    } else lds_wait_ge(x.lds + L_CTL + C_XL + 4, 4);
  }
  lds_wait_ge(x.lds + L_CTL + C_PUB + (y < 0 ? 4 : 0), 12);
  if (x.w >= 8) return;
  if (!XMID && y < 0) {
    tile_dma(x, j0 + x.w, L_TJ + x.w * 4096, w2, w3, w4);
    if (x.w < 3) tile_dma(x, j0 + 8 + x.w, L_TJ + (8 + x.w) * 4096, w2, w3, w4);
    VMWAIT(0);
  }
  Tile t0, t1;
  tile_read(x, j0 + x.w, x.w < nj, L_TJ + x.w * 4096, t0);
  tile_read(x, j0 + 8 + (x.w & 3), x.w + 8 < nj, L_TJ + (8 + (x.w & 3)) * 4096, t1);
  STAMP(59);
  wait_flags<1, 0, 4>(x, x.w == 7, {F_X + v}, 1);
  STAMP(61);
  tile_store(x, t0);
  tile_store(x, t1);
  STAMP(62);
  publish<8, 96>(x, 6, F_T + v, 47);
  lds_wait_ge(x.lds + L_CTL + C_PUB + 24, 8);
}

#define BSLP 1
DI v4u negv(v4u a) { return a ^ 0x80008000u; }
constexpr int L_BK = 80 * 1024;
DI void phase_b_consts(const Ctx& x) {
  const int g = x.g, c = x.c, wq = x.w & 3;
  LAS char* dst = pinp(x.lds + L_BK + wq * 9216 + x.lane * 16);
  const float f1 = (float)(16 * wq + c);
  float cv[8], sv[8];
#pragma unroll
  for (int ks = 0; ks < 2; ++ks) {
#pragma unroll
    for (int j = 0; j < 8; ++j) { const float a = f1 * (float)(32 * ks + 8 * g + j) * (1.f / 64.f); cv[j] = cosr(a); sv[j] = sinr(a); }
    *(LAS v4u*)(dst + ks * 1024) = pack8(cv);
    *(LAS v4u*)(dst + 2048 + ks * 1024) = pack8(sv);
    f32x4 tc, ts;
#pragma unroll
    for (int r = 0; r < 4; ++r) {
      const float a = f1 * (float)(16 * ks + 4 * g + r) * (1.f / 2048.f);
      tc[r] = 0.01858136f * cosr(a); ts[r] = 0.01858136f * sinr(a);
    }
    *(LAS f32x4*)(dst + 4096 + ks * 1024) = tc;
    *(LAS f32x4*)(dst + 6144 + ks * 1024) = ts;
  }
#pragma unroll
  for (int j = 0; j < 8; ++j)
    cv[j] = cosr((float)(16 * (wq & 1) + c) * (float)(16 * (j >> 2) + 4 * g + (j & 3)) * (1.f / 32.f) - (wq >> 1) * 0.25f);
  *(LAS v4u*)(dst + 8192) = pack8(cv);
  asm volatile("s_waitcnt lgkmcnt(0)" ::: "memory");
  lds_inc(x, C_SB1);
}
DI void phase_b(const Ctx& x, int p) {
  const int w = x.w, lane = x.lane, g = x.g, c = x.c, wq = w & 3, col = w >> 2;
  v4u C64[2], S64[2], N64[2], C32[2], S32[2], N32[2];
  f32x4 tc[2], ts[2];
  lds_wait_ge(x.lds + L_CTL + C_SB1, 4);
  {
    LAS char* k0 = pinp(x.lds + L_BK + lane * 16);
    LAS char* kw = k0 + wq * 9216;
#pragma unroll
    for (int i = 0; i < 2; ++i) {
      C64[i] = *(LAS v4u*)(kw + i * 1024); S64[i] = *(LAS v4u*)(kw + 2048 + i * 1024); N64[i] = negv(S64[i]);
      tc[i] = *(LAS f32x4*)(kw + 4096 + i * 1024); ts[i] = *(LAS f32x4*)(kw + 6144 + i * 1024);
      C32[i] = *(LAS v4u*)(k0 + i * 9216 + 8192); S32[i] = *(LAS v4u*)(k0 + (2 + i) * 9216 + 8192); N32[i] = negv(S32[i]);
    }
  }
  const int nt = p >> 5;
#pragma unroll
  for (int i = 0; i < 2; ++i) { PIN(C64[i]); PIN(S64[i]); PIN(N64[i]); PIN(C32[i]); PIN(S32[i]); PIN(N32[i]); PIN(tc[i]); PIN(ts[i]); }
  STAMP(4);
  int wo = O_WT + (g * 16 + 4 * wq + (c >> 2)) * 20480 + (p % 20) * 1024 + (p / 20) * 128 + col * 64 + (c & 3) * 16;
  int zo = O_ZT + ((nt * 32 + 4 * w + (lane >> 5)) * 32 + (p & 31)) * 512 + (lane & 31) * 16;
  LAS char* xd = pinp(x.lds + L_XS + ((lane >> 2) & 1) * 8192 + (w >> 2) * 1024 + ((w & 3) * 16 + 4 * (lane >> 4) + (lane & 3)) * 16 + (lane & 8));
  LAS char* xs = pinp(x.lds + L_XS + col * 8192 + lane * 16);
  int so = O_SIG + nt * 8192 + stat_at(16 * wq + 4 * g, 16 * col + c) * 4;
  LAS char* zp = pinp(x.lds + L_XS + (col * 2 + (wq >> 1)) * 1024 + ((2 * (wq & 1) + (g >> 1)) * 16 + c) * 16 + (g & 1) * 8);
  LAS char* img = pinp(x.lds + L_XB + col * 9216 + (16 * wq + c) * 72 + g * 8);
  LAS char* rd = pinp(x.lds + L_XB + (8 * g + (c >> 2)) * 72 + col * 32 + (c & 3) * 8);
  int ob = O_HB + p * 16384 + ((16 * wq + 4 * g) * 32 + 16 * col + 2 * (c & 7) + (c >> 3)) * 8;
  PIN(ob); PIN(zo); PIN(wo); PIN(so);
  v4u wv[2];
  auto ldw = [&] {
#pragma unroll
    for (int t = 0; t < 2; ++t) wv[t] = ld16(x.rs, wo, t * 64 * 20480);
  };
  lds_wait_ge<1>(x.lds + L_CTL + C_ANY, 1);
  const bool early = __builtin_amdgcn_readfirstlane(*(volatile LAS u32*)(x.lds + L_CTL + C_GOW)) != 0;
  asm volatile("" ::: "memory");
  if (early) ldw();
  SCHED();
  lds_wait_ge<1>(x.lds + L_CTL + C_GO, 2);
  STAMP(6);
  const v4u l0 = ld16(x.rs, zo, 0), l1 = ld16(x.rs, zo, 32768);
  *(LAS v2u*)xd = l0.xy; *(LAS v2u*)(xd + 4096) = l0.zw;
  *(LAS v2u*)(xd + 2048) = l1.xy; *(LAS v2u*)(xd + 6144) = l1.zw;
  LDS_ORDER();
  lds_inc(x, C_SB0);
  if (!early) { lds_wait_ge<0>(x.lds + L_CTL + C_GOW, 1); ldw(); }
  STAMP(5);
  const f32x4 sg = __builtin_bit_cast(f32x4, ld16(x.rs, so, 0));
  SCHED();
  lds_wait_ge<0>(x.lds + L_CTL + C_SB0, 8);
  STAMP(50);
  v4u xr[2][2], xi[2][2];
#pragma unroll
  for (int i = 0; i < 4; ++i) {
    xr[i >> 1][i & 1] = *(LAS v4u*)(xs + i * 1024);
    xi[i >> 1][i & 1] = *(LAS v4u*)(xs + 4096 + i * 1024);
  }
  v2u z[4];
#pragma unroll
  for (int i = 0; i < 4; ++i) z[i] = *(LAS v2u*)(zp + i * 4096);
  SCHED();
  const f32x4 zero = {0.f, 0.f, 0.f, 0.f};
  f32x4 ar[2], ai[2], br[2], bi[2];
  PRIO(1);
#pragma unroll
  for (int b = 0; b < 2; ++b) {
    ar[b] = mfma(xr[b][0], C64[0], zero); ai[b] = mfma(xi[b][0], C64[0], zero);
    ar[b] = mfma(xi[b][0], S64[0], ar[b]); ai[b] = mfma(xr[b][0], N64[0], ai[b]);
    ar[b] = mfma(xr[b][1], C64[1], ar[b]); ai[b] = mfma(xi[b][1], C64[1], ai[b]);
    ar[b] = mfma(xi[b][1], S64[1], ar[b]); ai[b] = mfma(xr[b][1], N64[1], ai[b]);
  }
  PRIO(0);
#pragma unroll
  for (int b = 0; b < 2; ++b) { br[b] = ar[b] * tc[b] + ai[b] * ts[b]; bi[b] = ai[b] * tc[b] - ar[b] * ts[b]; }
  v4u pr = pack8(br[0], br[1]), pi = pack8(bi[0], bi[1]);
  PRIO(1);
#pragma unroll
  for (int t = 0; t < 2; ++t) {
    ar[t] = mfma(C32[t], pr, zero); ai[t] = mfma(C32[t], pi, zero);
    ar[t] = mfma(S32[t], pi, ar[t]); ai[t] = mfma(N32[t], pr, ai[t]);
  }
  PRIO(0);
  f32x4 wre[2], wim[2];
#pragma unroll
  for (int t = 0; t < 2; ++t)
#pragma unroll
    for (int r = 0; r < 4; ++r) { wre[t][r] = blo(wv[t][r]); wim[t][r] = bhi(wv[t][r]); }
  SCHED();
#pragma unroll
  for (int t = 0; t < 2; ++t) { br[t] = ar[t] * wre[t] - ai[t] * wim[t]; bi[t] = ar[t] * wim[t] + ai[t] * wre[t]; }
  pr = pack8(br[0], br[1]); pi = pack8(bi[0], bi[1]);
  PRIO(1);
#pragma unroll
  for (int t = 0; t < 2; ++t) {
    ar[t] = mfma(C32[t], pr, zero); ai[t] = mfma(C32[t], pi, zero);
    ar[t] = mfma(N32[t], pi, ar[t]); ai[t] = mfma(S32[t], pr, ai[t]);
  }
  PRIO(0);
#pragma unroll
  for (int t = 0; t < 2; ++t) {
    const f32x4 ur = ar[t] * tc[t] - ai[t] * ts[t], ui = ai[t] * tc[t] + ar[t] * ts[t];
    *(LAS v2u*)(img + t * 32) = pack4(ur);
    *(LAS v2u*)(img + 4608 + t * 32) = pack4(ui);
  }
  STAMP(38);
  LDS_ORDER();
  lds_inc(x, C_SB);
  const f32x4 sh = sg * 0.70710678f;
#pragma unroll
  for (int cc = 0; cc < 2; ++cc) { ar[cc] = sh * un4(z[2 * cc]); ai[cc] = sh * un4(z[2 * cc + 1]); PIN(ar[cc]); PIN(ai[cc]); }
  lds_wait_ge<BSLP>(x.lds + L_CTL + C_SB, 8);
  STAMP(39);
  v4u ur[2][2], ui[2][2];
#pragma unroll
  for (int i = 0; i < 4; ++i) {
    LAS char* q = rd + (i >> 1) * 9216 + (i & 1) * 2304;
    const v2u r0 = trread(q), r1 = trread(q + 288), i0 = trread(q + 4608), i1 = trread(q + 4608 + 288);
    ur[i >> 1][i & 1] = v4u{r0.x, r0.y, r1.x, r1.y};
    ui[i >> 1][i & 1] = v4u{i0.x, i0.y, i1.x, i1.y};
  }
  SCHED();
#pragma unroll
  for (int cc = 0; cc < 2; ++cc)
#pragma unroll
    for (int ks = 0; ks < 2; ++ks) {
      ar[cc] = mfma(C64[ks], ur[cc][ks], ar[cc]); ai[cc] = mfma(C64[ks], ui[cc][ks], ai[cc]);
      ar[cc] = mfma(N64[ks], ui[cc][ks], ar[cc]); ai[cc] = mfma(S64[ks], ur[cc][ks], ai[cc]);
    }
  f32x4 er = ar[0] + ar[1], ei = ai[0] - ai[1], dr = ar[0] - ar[1], di = ai[0] + ai[1];
  if (p == 0) {
    er = ar[0] * 1.41421356f; dr = ai[0] * 1.41421356f; ei = ar[1]; di = ar[1];
  }
#pragma unroll
  for (int r = 0; r < 4; ++r) st8(x.rs, v2u{pk(er[r], ei[r]), pk(dr[r], di[r])}, ob + r * 256, 0);
}

#define DLY 12
#define B5W 1
#define BCW C_SB0
#define BDLY 1
#define GWAIT 6
#define DLYA 5
#define DLYB 2
#define ABL 0
#define LATE7 1
#define HALFW 0
#define HSLP 4
#define HOLD6 0
#define RW C_LGO
#define RGO 6
#define RIT 12
#define LA3 4
#define DLYC 1
#define LA5 5
#define LA6 4
#define LA7 4
#define PACE 6
#define STR2(x) #x
#define STR(x) STR2(x)
DI void dma_lin(const Ctx& x, int ws_off, int lds_off, int p0, int p1) {
  const char* src = x.ws + ws_off + x.lane * 16;
#pragma unroll 1
  for (int pc = p0 + (x.w & 3); pc < p1; pc += 4) {
    dma16_sc1(src + pc * 1024, (u32)(uintptr_t)x.lds + lds_off + pc * 1024);
    asm volatile("s_waitcnt vmcnt(" STR(PACE) ")" ::: "memory");
  }
}
DI int ft_pair(int rowblk, int KS, int gam, int g, int c) { return (rowblk * KS + gam) * 1024 + (g * 16 + c) * 16; }
DI int ft_single(int rowblk, int KS, int gam, int h, int g, int c) {
  return (rowblk * KS + gam) * 1024 + ((2 * h + (g >> 1)) * 16 + c) * 16 + (g & 1) * 8;
}

template <int PH, class L> DI void gemm16(const Ctx& x, v4u (&a)[10], L lda, f32x4 (&acc)[5], float& ssq) {
  constexpr int LA = PH == 4 ? 10 : PH == 3 ? LA3 : LA5, EXO = PH == 3 ? 0 : PH == 4 ? L_Q : 136 * 1024;
  constexpr int MID = PH == 5 ? C_QF : C_MID + (PH - 1) * 4, NIT = PH == 4 ? 5 : 10;
  const int kw = x.w >> 2;
  if (PH == 3) {
#pragma unroll
    for (int it = 0; it < LA; ++it) lda(it);
  }
  SCHED();
  if (PH == 4) lds_wait_ge(x.lds + L_CTL + C_QOK, 8);
  else wait_landed(x, PH == 3 ? 1 : 2);
  STAMP(PH == 3 ? 26 : PH == 4 ? 30 : 27);
  LAS char* bs = x.lds + (PH == 4 ? L_Q + kw * 25600 : kw * 51200) + x.lane * 16;
  LAS char* bq[3] = {pinp(bs + kw * 2048), pinp(bs + (kw ^ 1) * 2048), pinp(bs + 4096)};
  v4u f[2][5];
#pragma unroll
  for (int d = 0; d < 2; ++d)
#pragma unroll
    for (int n = 0; n < 5; ++n) f[d][n] = *(LAS v4u*)(bq[n >> 1] + (n & 1) * 1024 + d * 5120);
  if (PH == 4) {
#pragma unroll
    for (int i = 0; i < 10; i += 2) {
      const v4u u = a[i], v = a[i + 1];
      a[i] = v4u{u.x, v.x, u.z, v.z};
      a[i + 1] = v4u{u.y, v.y, u.w, v.w};
    }
  }
  f32x4 gr = {0.f, 0.f, 0.f, 0.f};
  u32 seen = 0;
  auto fix = [&](int it) {
    const v4u v = a[it];
    a[it] = v4u{(u32)__builtin_amdgcn_update_dpp(v.x, v.z, 0x128, 0xf, 0xc, false), (u32)__builtin_amdgcn_update_dpp(v.y, v.w, 0x128, 0xf, 0xc, false),
                (u32)__builtin_amdgcn_update_dpp(v.z, v.x, 0x128, 0xf, 0x3, false), (u32)__builtin_amdgcn_update_dpp(v.w, v.y, 0x128, 0xf, 0x3, false)};
  };
  if (PH == 3) fix(0);
#pragma unroll
  for (int it = 0; it < NIT; ++it) {
    if (it == NIT - 1) lds_wait_ge_pre(x.lds + L_CTL + MID, 8, seen);
#pragma unroll
    for (int m = 0; m < 5; ++m) {
      const int n = it == NIT - 1 ? 4 - m : m;
      SCHED();
      if (PH == 3 && m == 0) gr = mfma(a[it], a[it], gr);
      if (PH == 4 && n == 4) gr = mfma(f[it & 1][n], a[2 * it + 1], gr);
      acc[n] = mfma(f[it & 1][n], a[PH == 4 ? 2 * it + (n & 1) : it], acc[n]);
      SCHED();
      if (it + 2 < NIT) f[it & 1][n] = *(LAS v4u*)(bq[n >> 1] + (n & 1) * 1024 + (it + 2) * 5120);
      if (m == 0 && it + LA < 10) lda(it + LA);
      if (PH == 3 && m == 3 && it < 9) fix(it + 1);
      if (it == NIT - 2 && m == 0) seen = *(volatile LAS u32*)(x.lds + L_CTL + MID);
    }
    if (PH != 5 && it == 1) { LDS_ORDER(); lds_inc(x, MID); }
  }
  STAMP(PH == 3 ? 12 : PH == 4 ? 31 : 16);
  if (PH == 4) { acc[4][1] = gr[1]; acc[4][3] = gr[3]; }
  LAS char* ex = pinp(x.lds + EXO + (x.w & 3) * 5120 + x.lane * 16);
  LAS char* mine = ex + kw * 2048;
  float s0 = 0.f;
  if (PH == 3) {
    const int r = x.c & 3;
    s0 = (x.c >> 2) == x.g ? (r == 0 ? gr[0] : r == 1 ? gr[1] : r == 2 ? gr[2] : gr[3]) : 0.f;
    *(LAS float*)(x.lds + L_SQ + x.w * 256 + x.lane * 4) = s0;
  }
  if (kw) *(LAS f32x4*)(ex + 4096) = acc[4];
  *(LAS f32x4*)(mine + 1024) = acc[3];
  *(LAS f32x4*)mine = acc[2];
  LDS_ORDER();
  lds_st(x.lds + L_CTL + C_EX + x.w * 4, PH);
  lds_inc(x, PH == 3 ? C_FREE : PH == 4 ? C_QF : C_FREE + 4);
  if (PH == 4) lda(1);
  lds_wait_ge<0>(x.lds + L_CTL + C_EX + (x.w ^ 4) * 4, PH);
  STAMP(PH == 3 ? 32 : PH == 4 ? 10 : 33);
  LAS char* other = ex + (kw ^ 1) * 2048;
  if (PH == 3) ssq = xsum16_32(s0 + *(LAS float*)(x.lds + L_SQ + (x.w ^ 4) * 256 + x.lane * 4));
  acc[0] += *(LAS f32x4*)other;
  acc[1] += *(LAS f32x4*)(other + 1024);
  acc[4] += *(LAS f32x4*)(ex + 4096);
  if (PH != 4) {
    LDS_ORDER();
    lds_inc(x, PH == 3 ? C_EXD + 8 : C_EXD);
  }
}
DI void gen_q_tile(const Ctx& x, int t8) {
#pragma unroll 1
  for (int u = x.w; u < 10; u += 8) {
    const int nb = u >> 1, k0 = 80 * (u & 1) + 2 * x.g;
    const int n = (t8 >> 1) * 160 + (nb == 4 ? 64 + (t8 & 1) * 16 + x.c : (t8 & 1) * 96 + (nb >> 1) * 32 + 2 * x.c + (nb & 1));
    const float da = (float)n * (1.f / 640.f), a0 = __builtin_amdgcn_fractf((float)(n * k0) * (1.f / 640.f));
    const float rc = cosr(16.f * da), rs = sinr(16.f * da), c2 = rc + rc;
    LAS char* dst = pinp(x.lds + L_Q + ((u & 1) * 25 + nb) * 1024 + x.lane * 16);
    f32x2 v0[4], v1[4];
#pragma unroll
    for (int i = 0; i < 4; ++i) {
      const float a = a0 + (float)(8 * (i & 1) + (i >> 1)) * da;
      v0[i] = f32x2{cosr(a), sinr(a)} * f32x2{0.07905694150f, -0.07905694150f};
      v1[i] = v0[i] * rc + v0[i].yx * f32x2{rs, -rs};
    }
#pragma unroll
    for (int ks = 0; ks < 5; ++ks) {
      v4u f = {pk(v0[0][0], v0[0][1]), pk(v0[1][0], v0[1][1]), pk(v0[2][0], v0[2][1]), pk(v0[3][0], v0[3][1])};
      if (ks == 0) f.x = k0 ? f.x : 0x3d653d65u ^ ((n & 1) ? 0x80008000u : 0u) ^ ((n & 2) ? 0x8000u : 0u);
      *(LAS v4u*)(dst + ks * 5120) = f;
#pragma unroll
      for (int i = 0; i < 4; ++i) { const f32x2 t = c2 * v1[i] - v0[i]; v0[i] = v1[i]; v1[i] = t; }
    }
  }
  LDS_ORDER();
  lds_inc(x, C_QOK);
}

template <class W> DI void phase_c5(Ctx& x, int grp, int t8, u32 (&res)[6], W wait) {
  const int kw = x.w >> 2;
  const int rowblk = 4 * grp + 2 * (x.w & 1) + ((x.w >> 1) & 1);
  const int gp = (t8 >> 1) * 5 + (t8 & 1) * 3 + kw, gs = (t8 >> 1) * 5 + 2;
  int op = ft_pair(rowblk, 20, gp, x.g, x.c), os = ft_single(rowblk, 20, gs, t8 & 1, x.g, x.c);
  int mo = O_MU + stat_at(rowblk >> 1, 16 * (rowblk & 1) + x.c) * 4;
  int ho = O_HB + (80 * kw + 2 * x.g + (x.c >> 3)) * 16384 + rowblk * 128 + (x.c & 7) * 16;
  PIN(op); PIN(os); PIN(mo); PIN(ho);
  wait();
  const f32x4 zero = {0.f, 0.f, 0.f, 0.f};
  v4u a[10];
  f32x4 o[5] = {zero, zero, zero, zero, zero};
  float ssq, mu = 0.f;
  gemm16<3>(x, a, [&](int it) {
    a[it] = ld16(x.rs, ho, it * 131072);
    if (it == 9) mu = __builtin_bit_cast(float, ld4(x.rs, mo, 0));
  }, o, ssq);
  const float vh = ssq * (1.f / 640.f);
  float sc = __builtin_amdgcn_rsqf(vh + EPS);
  {
    const float v2 = vh * sc * sc + EPS, s2 = __builtin_amdgcn_rsqf(v2);
    const f32x3 ge = gelu_k(sc * s2, (vh + EPS) * sc * v2 * s2);
    if (kw == 0) {
      u32 q[6];
      gelu<6>(ge, {o[0].xy, o[0].zw, o[1].xy, o[1].zw, o[4].xy, o[4].zw}, q);
      st16(x.rs, v4u{q[0], q[1], q[2], q[3]}, O_G + op, 0);
      st8(x.rs, v2u{q[4], q[5]}, O_G + os, 0);
    } else {
      u32 q[4];
      gelu<4>(ge, {o[0].xy, o[0].zw, o[1].xy, o[1].zw}, q);
      st16(x.rs, v4u{q[0], q[1], q[2], q[3]}, O_G + op, 0);
    }
  }
  PIN(sc); PIN(mu);
  STAMP(13);
  publish(x, 2, F_C + grp * 8 + t8, 14);
  int ao[2];
#pragma unroll
  for (int i = 0; i < 2; ++i) {
    const int r = rowblk * 16 + x.c - (i ? -1 - kw : 1 + kw);
    ao[i] = (u32)r < 2048u ? O_G + ft_pair(r >> 4, 20, 10 * kw, x.g, r & 15) : 0x7f000000;
  }
  const int fl = F_C + min(max(grp - 1 + min(x.lane >> 3, 2), 0), 31) * 8 + (x.lane & 7);
  PIN(ao[0]); PIN(ao[1]);
  auto lda5 = [&](int it) { a[it] = ld16(x.rs, it >= 5 ? ao[1] : ao[0], it * 1024); };
#pragma unroll
  for (int n = 0; n < 5; ++n) o[n] = zero;
  gemm16<4>(x, a, [&](int) {
    if (x.w == 0) lds_wait_ge<1>(x.lds + L_CTL + C_PUB + 8, 8);
    wait_flags<1, DLYC>(x, x.w == 0, {fl}, 4);
    STAMP(15);
#pragma unroll
    for (int it = 0; it < LA5; ++it) lda5(it);
    SCHED();
  }, o, ssq);
  {
    const f32x4 ev = o[0], od = o[1];
    o[0] = f32x4{ev[0], od[0], ev[1], od[1]};
    o[1] = f32x4{ev[2], od[2], ev[3], od[3]};
    const v4u hp = pack8(o[0] + mu, o[1] + mu);
    const v2u hq = pack4(o[4] + mu);
    res[0] = hp.x; res[1] = hp.y; res[2] = hp.z; res[3] = hp.w; res[4] = hq.x; res[5] = hq.y;
  }
  o[0] *= sc; o[1] *= sc; o[2] = zero; o[3] = zero; o[4] *= kw ? 0.f : sc;
#pragma unroll
  for (int i = 0; i < 6; ++i) PIN(res[i]);
  gemm16<5>(x, a, lda5, o, ssq);
  const v4u r = pack8(o[0], o[1]);
  st16(x.rs, r, O_X1 + op, 0);
  const v4u ones = {0x3f803f80u, 0x3f803f80u, 0x3f803f80u, 0x3f803f80u};
  float s1 = dot8(r, ones, 0.f), s2 = dot8(r, r, 0.f);
  if (kw == 0) {
    const v2u q = pack4(o[4]);
    st8(x.rs, q, O_X1 + os, 0);
    s1 = dot2(q.y, ones.x, dot2(q.x, ones.x, s1));
    s2 = dot2(q.y, q.y, dot2(q.x, q.x, s2));
  }
  s1 = xsum16_32(s1); s2 = xsum16_32(s2);
  if (x.g == 0) st8(x.rs, v2u{__builtin_bit_cast(u32, s1), __builtin_bit_cast(u32, s2)}, O_ST1 + (rowblk * 16 + t8 * 2 + kw) * 128 + x.c * 8, 0);
  const v4u rp = pack8(o[0] + un4(v2u{res[0], res[1]}), o[1] + un4(v2u{res[2], res[3]}));
  const v2u rq = pack4(o[4] + un4(v2u{res[4], res[5]}));
  res[0] = rp.x; res[1] = rp.y; res[2] = rp.z; res[3] = rp.w; res[4] = rq.x; res[5] = rq.y;
#pragma unroll
  for (int i = 0; i < 6; ++i) PIN(res[i]);
}

template <class W> DI void phase_6(const Ctx& x, int P, int t, W wait) {
  constexpr int LA = LA6;
  const int g = x.g, c = x.c, rowblk = 8 * P + 2 * (x.w & 3);
  int so = O_ST1 + rowblk * 2048 + x.lane * 8, ao = O_X1 + rowblk * 20480 + x.lane * 16;
  int op = O_U + ft_pair(rowblk, 54, (t >> 1) * 7 + (t & 1) * 4, g, c), oq = t < 15 ? op : 0x7f000000;
  int os = O_U + ft_single(rowblk, 54, (t >> 1) * 7 + 3, t & 1, g, c);
  PIN(so); PIN(ao); PIN(op); PIN(oq); PIN(os);
  wait();
  v4u a[20][2];
  auto lda = [&](int it, int i) { a[it][i] = ld16(x.rs, ao, it * 1024 + i * 20480); };
#pragma unroll
  for (int it = 0; it < LA; ++it) { lda(it, 0); lda(it, 1); }
  SCHED();
  wait_landed(x, 3);
  STAMP(28);
  f32x4 acc[2][7];
  f32x2 p[4];
  f32x3 ge[2];
  float nm[2];
  u32 seen = 0;
  v4u f[7];
  LAS char* bs = x.lds + x.lane * 16;
  LAS char* cs = pinp(x.lds + L_MISC + g * 16);
#pragma unroll
  for (int n = 0; n < 7; ++n) { acc[0][n] = f32x4{0.f, 0.f, 0.f, 0.f}; acc[1][n] = acc[0][n]; f[n] = *(LAS v4u*)(bs + n * 1024); }
#pragma unroll
  for (int it = 0; it < 20; ++it) {
    if ((it + 1) % 8 == 0) bs = pinp(bs + 8 * 7168);
    if (it == 19) lds_wait_ge_pre(x.lds + L_CTL + C_CS, 1, seen);
#pragma unroll
    for (int n = 0; n < 7; ++n) {
      SCHED();
      acc[0][n] = mfma(f[n], a[it][0], acc[0][n]);
      acc[1][n] = mfma(f[n], a[it][1], acc[1][n]);
      SCHED();
      f[n] = *(LAS v4u*)(it < 19 ? bs + ((it + 1) % 8) * 7168 + n * 1024 : cs + n * 64);
      if ((n == 1 || n == 4) && it + LA < 20) lda(it + LA, n == 4);
      if (it == RIT && n == 6) lds_inc(x, C_MID);
      if (it == 18 && n == 0) seen = *(volatile LAS u32*)(x.lds + L_CTL + C_CS);
      const int i = it >= 9, q = it - 1 - 8 * i;
      if (q == 0 && (n & 1) == 0) p[n >> 1] = __builtin_bit_cast(f32x2, ld8(x.rs, so + (n >> 1) * 512, i * 2048));
      if (q == 6 && n == 0) { p[0] = (p[0] + p[1]) + (p[2] + p[3]); PIN(p[0]); }
      if (q == 6 && n == 2) { nm[i] = xsum16_32(p[0][0]) * (-1.f / 640.f); PIN(nm[i]); }
      if (q == 6 && n == 5) { p[0][1] = xsum16_32(p[0][1]); PIN(p[0]); }
      if (q == 7 && n == 0) { p[0][1] = fmaxf(p[0][1] * (1.f / 640.f) - nm[i] * nm[i], 0.f) + EPS; p[0][0] = __builtin_amdgcn_rsqf(p[0][1]); PIN(p[0]); }
      if (q == 7 && n == 2) { ge[i] = gelu_k(p[0][0], p[0][1] * p[0][0]); PIN(ge[i]); }
    }
  }
  lds_inc(x, C_FREE + 8);
  STAMP(20);
#pragma unroll
  for (int i = 0; i < 2; ++i) {
#pragma unroll
    for (int n = 0; n < 7; ++n) acc[i][n] += __builtin_bit_cast(f32x4, f[n]) * nm[i];
    SCHED();
    u32 q[8], r[6];
    gelu<8>(ge[i], {acc[i][0].xy, acc[i][0].zw, acc[i][1].xy, acc[i][1].zw, acc[i][2].xy, acc[i][2].zw, acc[i][3].xy, acc[i][3].zw}, q);
    st16(x.rs, v4u{q[0], q[1], q[2], q[3]}, op + i * 55296, 0);
    st16(x.rs, v4u{q[4], q[5], q[6], q[7]}, oq + i * 55296, 1024);
    gelu<6>(ge[i], {acc[i][4].xy, acc[i][4].zw, acc[i][5].xy, acc[i][5].zw, acc[i][6].xy, acc[i][6].zw}, r);
    st16(x.rs, v4u{r[0], r[1], r[2], r[3]}, oq + i * 55296, 2048);
    st8(x.rs, v2u{r[4], r[5]}, os + i * 55296, 0);
  }
}
DI void colsum_6(const Ctx& x, int t) {
  f32x2 v[20];
#pragma unroll
  for (int k = 0; k < 20; ++k) v[k] = __builtin_bit_cast(f32x2, ld8(x.rs, O_CS3 + t * 8960 + min(x.lane, 55) * 8, k * 448));
  SCHED();
#pragma unroll
  for (int k = 1; k < 20; ++k) v[0] += v[k];
  *(LAS f32x2*)(x.lds + L_MISC + x.lane * 8) = v[0];
  LDS_ORDER();
  lds_inc(x, C_CS);
}

template <class W> DI void phase_7(const Ctx& x, int grp, int t8, const u32 (&res)[6], u16* out, W wait) {
  constexpr int LA = LA7;
  const int kw = (x.w >> 1) & 1, hi = x.w >> 2, g = x.g, c = x.c;
  const int rowblk = 4 * grp + 2 * (x.w & 1) + kw;
  int ao0 = O_U + (rowblk * 54 + kw) * 1024 + x.lane * 16, ao1 = O_U + ((rowblk ^ 1) * 54 + kw) * 1024 + x.lane * 16;
  PIN(ao0); PIN(ao1);
  f32x4 o[3] = {un4(v2u{res[0], res[1]}), un4(v2u{res[2], res[3]}), un4(v2u{res[4], res[5]})};
  LAS char* ctl = x.lds + L_CTL;
  if (!hi) {
    wait();
    v4u a[27][2];
    auto lda = [&](int it, int i) { a[it][i] = ld16(x.rs, i ? ao1 : ao0, it * 2048); };
#pragma unroll
    for (int it = 0; it < LA; ++it) { lda(it, 0); lda(it, 1); }
    SCHED();
    wait_landed(x, 4);
    STAMP(29);
    const f32x4 zero = {0.f, 0.f, 0.f, 0.f};
    f32x4 acc[2][5] = {{o[0], o[1], zero, zero, o[2]}, {zero, zero, zero, zero, zero}};
    LAS char* bs = x.lds + kw * 5120 + x.lane * 16;
    v4u f[2][5];
#pragma unroll
    for (int d = 0; d < 2; ++d)
#pragma unroll
      for (int n = 0; n < 5; ++n) f[d][n] = *(LAS v4u*)(bs + d * 10240 + n * 1024);
    u32 seen = 0;
#pragma unroll
    for (int it = 0; it < 26; ++it) {
      const int st = (it + 2) % 14;
      if (!(ABL & 4) && it >= 12 && it < 25 && it % 2 == 0) lds_wait_ge_pre(ctl + C_RL + 4 * ((it - 12) / 2), 4, seen);
      if (st % 5 == 0) bs = pinp(bs + (st ? 51200 : -102400));
#pragma unroll
      for (int n = 0; n < 5; ++n) {
        SCHED();
        acc[0][n] = mfma(f[it & 1][n], a[it][0], acc[0][n]);
        acc[1][n] = mfma(f[it & 1][n], a[it][1], acc[1][n]);
        SCHED();
        if (!(ABL & 1) && it + 2 < 27) f[it & 1][n] = *(LAS v4u*)(bs + (st % 5) * 10240 + n * 1024);
        if ((n == 0 || n == 2) && it + LA < 27) { if (ABL & 2) a[it + LA][n >> 1] = a[it][n >> 1]; else lda(it + LA, n >> 1); }
        if (n == 1 && it >= 11 && it < 24 && it % 2 == 1) seen = *(volatile LAS u32*)(ctl + C_RL + 4 * ((it - 11) / 2));
        if (n == 1 && it == 25) seen = *(volatile LAS u32*)(ctl + C_MID + 4);
      }
      if (it < 14 && it % 2 == 1) lds_inc(x, C_RING + 4 * (it / 2));
      if (it == 17) lds_inc(x, C_MID + 4);
    }
    lds_wait_ge_pre(ctl + C_MID + 4, 4, seen);
    LAS char* mine = pinp(x.lds + x.w * 7168 + x.lane * 16);
#pragma unroll
    for (int n = 0; n < 5; ++n) acc[1][n] = mfma(f[0][n], a[26][1], acc[1][n]);
    acc[0][2] = mfma(f[0][2], a[26][0], acc[0][2]);
    acc[0][3] = mfma(f[0][3], a[26][0], acc[0][3]);
    o[0] = mfma(f[0][0], a[26][0], acc[0][0]);
    o[1] = mfma(f[0][1], a[26][0], acc[0][1]);
    o[2] = mfma(f[0][4], a[26][0], acc[0][4]);
    STAMP(24);
#pragma unroll
    for (int n = 0; n < 5; ++n) *(LAS f32x4*)(mine + n * 1024) = acc[1][n];
    *(LAS f32x4*)(mine + 5120) = acc[0][2];
    *(LAS f32x4*)(mine + 6144) = acc[0][3];
    LDS_ORDER();
    lds_st(ctl + C_EX + x.w * 4, 8);
  }
  const int gp = (t8 >> 1) * 5 + (t8 & 1) * 3 + hi, gs = (t8 >> 1) * 5 + 2;
  char* orow = (char*)out + (rowblk * 16 + c) * 1280;
  LAS char* other = pinp(x.lds + ((x.w & 3) ^ 2) * 7168 + hi * 2048 + x.lane * 16);
  LAS char* own = pinp(x.lds + (x.w & 3) * 7168 + 5120 + x.lane * 16);
  if (hi) lds_wait_ge<4>(ctl + C_MID + 4, 4);
  lds_wait_ge<0>(ctl + C_EX + ((x.w & 1) + (x.lane & 2)) * 4, 8);
  STAMP(35);
  o[0] += *(LAS f32x4*)other;
  o[1] += *(LAS f32x4*)(other + 1024);
  if (hi) { o[0] += *(LAS f32x4*)own; o[1] += *(LAS f32x4*)(own + 1024); }
  else o[2] += *(LAS f32x4*)(other + 4096);
  __builtin_nontemporal_store(pack8(o[0], o[1]), (v4u*)(orow + gp * 64 + g * 16));
  if (!hi) *(v2u*)(orow + gs * 64 + (t8 & 1) * 32 + g * 8) = pack4(o[2]);
}

template <int R0, int R1> DI void stage_load(const Ctx& x, int voff, u64 skip, v4u (&r)[35]) {
#pragma unroll
  for (int k = R0; k < R1; ++k) {
    r[k] = ld16(x.rs, voff, ((skip >> k) & 1) ? 0x7f000000 : k * 4096);
    asm volatile("s_waitcnt vmcnt(" STR(PACE) ")" ::: "memory");
  }
}
template <int R0, int R1, bool HALF = false> DI void stage_drop(LAS char* p, const v4u (&r)[35]) {
#pragma unroll
  for (int k = R0; k < R1; ++k) {
    if (k % 16 == 0 || k == R0) p = pinp(p + (k == R0 ? (R0 / 16) * 65536 : 65536));
    if (HALF) {
      *(volatile LAS v2u*)(p + (k % 16) * 4096) = r[k].xy;
      *(volatile LAS v2u*)(p + (k % 16) * 4096 + 8) = r[k].zw;
    } else *(LAS v4u*)(p + (k % 16) * 4096) = r[k];
  }
}
DI void dropped(const Ctx& x) { LDS_ORDER(); lds_inc(x, C_LAND); }
template <int S> DI void ring_drop(const Ctx& x, LAS char* p, const v4u (&r)[35]) {
  lds_wait_ge(x.lds + L_CTL + C_RING + 4 * S, 4);
  if (!(ABL & 8)) stage_drop<5 * S, (S < 6 ? 5 * S + 5 : 33), HALFW>(p, r);
  LDS_ORDER();
  lds_inc(x, C_RL + 4 * S);
}
template <class F> DI void weight_stream(const Ctx& x, int t, int t8, F idle) {
  const int lw = x.w & 3, lo = lw * 1024 + x.lane * 16;
  LAS char* p = x.lds + lo;
  v4u r[35];
  stage_load<0, 25>(x, lo + O_B5 + t8 * 102400, 0, r);
  STAMPL(41);
  idle();
  lds_wait_ge(x.lds + L_CTL + C_FREE, 8);
  stage_drop<5, 25>(p, r);
  lds_wait_ge(x.lds + L_CTL + C_EXD + 8, 8);
  stage_drop<0, 5>(p, r);
  dropped(x);
  STAMPL(51);
#pragma unroll 1
  for (int s = 1; s < 3; ++s) {
    const u64 skip = (s == 1 && t == 15) ? 0x810204081ull * (lw == 0 ? 0x5a : lw == 1 ? 0x56 : lw == 2 ? 0x55 : 0x35) : 0;
    if (s == 2) lds_wait_ge<4>(x.lds + L_CTL + C_MID, 4);
    stage_load<0, 35>(x, lo + (s == 1 ? O_B6 + t * 143360 : O_B7 + t8 * 276480), skip, r);
    STAMPL(41 + s);
    if (s == 2) lds_inc(x, C_LGO);
    lds_wait_ge(x.lds + L_CTL + C_FREE + 4 * s, s == 1 ? 8 : 4);
    stage_drop<0, 34>(p, r);
    lds_wait_ge(x.lds + L_CTL + C_EXD, 8);
    stage_drop<34, 35>(p, r);
    dropped(x);
    STAMPL(51 + s);
  }
}
DI void ring_stream(const Ctx& x, int t8) {
  const int lw = x.w & 3, lo = lw * 1024 + x.lane * 16;
  LAS char* p = x.lds + lo;
  v4u r[35];
  lds_wait_ge<4>(x.lds + L_CTL + RW, RGO);
  stage_load<0, 33>(x, lo + O_B7 + t8 * 276480 + 143360, lw < 2 ? 0 : 1ull << 32, r);
  STAMPA(L_CTL + 256 + (x.w == 4 ? 44 * 4 : 512));
  ring_drop<0>(x, p, r); ring_drop<1>(x, p, r); ring_drop<2>(x, p, r); ring_drop<3>(x, p, r);
  ring_drop<4>(x, p, r); ring_drop<5>(x, p, r); ring_drop<6>(x, p, r);
}

DI void wait_b(const Ctx& x, bool poller, int xcd) {
  wait_flags<3, DLYB>(x, poller, {F_B + xcd * 160 + x.lane, F_B + xcd * 160 + 64 + x.lane, F_B + xcd * 160 + 128 + (x.lane & 31)}, 3);
}
#define CODE_KB 48
#define MAP 1
__global__ __launch_bounds__(NTHR, 3) void fused(const u16* __restrict__ xin, const u16* __restrict__ wr, const u16* __restrict__ wi,
                                                 const u16* __restrict__ w1, const u16* __restrict__ w2, const u16* __restrict__ w3,
                                                 const u16* __restrict__ w4, u16* __restrict__ out, char* ws) {
  extern __shared__ __attribute__((aligned(16))) char smem[];
  u64 pc;
  asm volatile("s_getpc_b64 %0" : "=s"(pc));
  Ctx x;
  x.ws = ws; x.rs = __builtin_amdgcn_make_buffer_rsrc(ws, 0, WS_END, 0x00020000); x.lds = (LAS char*)smem;
  x.tid = threadIdx.x; x.lane = x.tid & 63; x.w = __builtin_amdgcn_readfirstlane(x.tid >> 6); x.g = x.lane >> 4; x.c = x.lane & 15;
  if (blockIdx.x < 160 && x.w < 8) x_fetch<0, 4>(x, xin, blockIdx.x & 7, (blockIdx.x >> 3) & 3, blockIdx.x >> 5, x.w);
  if (blockIdx.x >= 240 && threadIdx.x >= 512) {
    const char* code = (const char*)(pc & ~255ull) + (threadIdx.x - 512) * 16;
#pragma unroll 1
    for (int k = 0; k < CODE_KB / 4; ++k) dma16_sc1(code + k * 4096, (u32)(uintptr_t)(LAS char*)smem + L_MISC + 2048);
  }
  GAS u64* ep = (GAS u64*)(ws + O_EPOCH);
  x.epoch = __hip_atomic_load(ep, __ATOMIC_RELAXED, __HIP_MEMORY_SCOPE_AGENT);
  const int b = blockIdx.x, xcd = b & 7, i = b >> 3;
  const int P = 4 * (xcd >> 1) + (i >> 3), t = 8 * (xcd & 1) + (i & 7);
  const int grp = 2 * P + ((i >> 2) & 1), t8 = 4 * (xcd & 1) + (i & 3);
  STAMP(0);
  if (x.w == 8) lds_st(x.lds + L_CTL + x.lane * 4, 0);
  if (x.w == 9) {
    const int q = x.lane >> 3;
    if (b < 160) { clear_flag(x, F_A + b + q * 160); clear_flag(x, F_B + b + q * 160); }
    if (q < (b < 96 ? 4 : 3)) clear_flag(x, q == 0 ? F_C + grp * 8 + t8 : q == 1 ? F_5 + grp * 8 + t8 : q == 2 ? F_6 + P * 16 + t : F_X + b);
  }
  if (b >= 160) side_jobs(x, xcd, i - 20, wr, wi, w1, w2, w3, w4);
  if (x.w >= 8) {
    auto poll_s = [&]() {
      poll<1, 8>(x, {F_S + xcd * 50 + min(x.lane, 49)});
      lds_st(x.lds + L_CTL + C_LGO, 1);
      STAMPW(9);
    };
    if (b < 160) {
      const int lw = x.w & 3;
      BAR();
      __builtin_amdgcn_s_setprio(3);
      phase_a_stats(x, xin, xcd, i & 3, i >> 2);
      STAMPL(59);
      publish<8, 160>(x, 0, F_A + b, 3, 12);
      phase_b_consts(x);
      STAMPL(57);
      __builtin_amdgcn_s_setprio(3);
      if (lw == 3) {
        lds_wait_ge(x.lds + L_CTL + C_PUB, GWAIT);
        poll<1, 4>(x, {F_W + xcd * 46 + min(x.lane, 45)});
        lds_inc(x, C_GOW);
        lds_inc(x, C_ANY);
      }
      if (lw == 1) {
        lds_wait_ge(x.lds + L_CTL + C_PUB, 12);
        wait_flags<1, DLYA>(x, true, {F_A + xcd * 160 + (b & ~31) + (x.lane & 31)}, 2);
        lds_inc(x, C_ANY);
        if (b < 96) raise<1, 0>(x, F_X + b);
      }
      if (lw == 0) { lds_wait_ge(x.lds + L_CTL + C_GO, 2); poll_s(); }
      __builtin_amdgcn_s_setprio(0);
      lds_wait_ge(x.lds + L_CTL + BCW, 8);
      __builtin_amdgcn_s_sleep(BDLY);
    } else if (x.w == 8) poll_s();
    lds_wait_ge(x.lds + L_CTL + C_LGO, 1);
    dma_lin(x, O_BC + t8 * 102400, 0, 0, 100);
    LANDED(0);
    STAMPL(40);
    if (b < 160) lds_wait_ge(x.lds + L_CTL + C_PUB + 4, 8);
    weight_stream(x, t, t8, [&] {
      if (x.w == 11) {
        poll<2, 6>(x, {F_T + xcd * 96 + x.lane, F_T + xcd * 96 + 64 + (x.lane & 31)});
        lds_st(x.lds + L_CTL + C_LGO, 2);
      } else lds_wait_ge(x.lds + L_CTL + C_LGO, 2);
      STAMPL(45);
    });
    return;
  }
  bool last;
  if (b < 160) {
    phase_a(x, xin, xcd, i & 3, i >> 2);
    STAMP(2);
    publish<8, 160>(x, 0, F_A + b, 3, 12);
    refresh(x);
    phase_b(x, b);
    STAMP(7);
    publish<8, 160>(x, 1, F_B + b, 8);
    lds_wait_ge<1>(x.lds + L_CTL + C_PUB + 4, 8);
  }
  gen_q_tile(x, t8);
  STAMP(48);
  last = x.w == 2;
  if (last && b >= 160) poll<1, HSLP>(x, {F_B + xcd * 160 + i});
  refresh(x);
  u32 res[6];
  phase_c5(x, grp, t8, res, [&] { wait_b(x, last, xcd); STAMP(11); });
  STAMP(17);
  last = publish(x, 3, F_5 + grp * 8 + t8, 18);
  refresh(x);
  if (x.w < 4) {
    phase_6(x, P, t, [&] { wait_flags<1, DLY>(x, last, {F_5 + P * 16 + x.c}, 5); STAMP(19); });
    STAMP(21);
    last = publish(x, 4, F_6 + P * 16 + t, 22, 4);
  } else {
    if (last) wait_flags<1, DLY>(x, true, {F_5 + P * 16 + x.c}, 5);
    if (x.w == 4) colsum_6(x, t);
    ring_stream(x, t8);
  }
  refresh(x);
  phase_7(x, grp, t8, res, out, [&] { wait_flags<1, DLY>(x, last, {F_6 + (grp >> 1) * 16 + x.c}, 6); STAMP(23); });
  STAMP(25);
  if (b == 0 && x.tid == 0) __hip_atomic_store(ep, x.tag(), __ATOMIC_RELAXED, __HIP_MEMORY_SCOPE_AGENT);
}

#define WSOFF 0
extern "C" void kernel_launch(void* const* d_in, const int* in_sizes, int n_in, void* d_out, int out_size, void* d_ws,
                              size_t ws_size, hipStream_t stream) {
  (void)hipFuncSetAttribute((const void*)fused, hipFuncAttributeMaxDynamicSharedMemorySize, LDS_BYTES);
  fused<<<256, NTHR, LDS_BYTES, stream>>>((const u16*)d_in[0], (const u16*)d_in[1], (const u16*)d_in[2], (const u16*)d_in[4],
                                          (const u16*)d_in[5], (const u16*)d_in[6], (const u16*)d_in[7], (u16*)d_out, (char*)d_ws + WSOFF);
}
```
